```python
import torch

def _softmax(x, axis=-1):
    return torch.softmax(x, dim=axis)
def _gelu(x):
    return torch.nn.functional.gelu(x, approximate="tanh")
def _silu(x):
    return torch.nn.functional.silu(x)
def _sigmoid(x):
    return torch.sigmoid(x)
def _layernorm(x, axis=-1, eps=1e-5):
    m = x.mean(dim=axis, keepdim=True)
    v = x.var(dim=axis, keepdim=True, unbiased=False)
    return (x - m) / torch.sqrt(v + eps)
def _scatter_add(tgt, idx, src, axis=0):
    return tgt.clone().index_add_(axis, idx, src)
def _segment_sum(src, seg, n):
    out = torch.zeros((n,) + src.shape[1:], dtype=src.dtype)
    return out.index_add_(0, seg, src)
def _pad(x, pw):
    flat = tuple(v for pair in reversed(pw) for v in pair)
    return torch.nn.functional.pad(x, flat)

kernel_name = 'standalone__edge_featured_mpnn_gatedgcn__graph'

def setup_inputs():

    _w = (lambda a: (lambda t: t.to(torch.bfloat16) if t.is_floating_point() else t)(torch.as_tensor(a)))
    N, deg, D, De = 50000, 16, 64, max(16, 1)
    E = N * deg
    return {
        "x": _w((lambda *s: torch.randn(*s, dtype=torch.bfloat16) * 0.4)(N, D)),
        "e_attr": _w((lambda *s: torch.randn(*s, dtype=torch.bfloat16) * 0.8)(E, De)),
        "src": _w((lambda lo, hi, *s: torch.randint(lo, hi, s))(0, N, E).to(torch.int64)),
        "dst": _w((lambda lo, hi, *s: torch.randint(lo, hi, s))(0, N, E).to(torch.int64)),
        "w_a": _w((lambda *s: torch.randn(*s, dtype=torch.bfloat16) * 0.8)(D, D)),
        "w_b": _w((lambda *s: torch.randn(*s, dtype=torch.bfloat16) * 0.4)(D, D)),
        "w_c": _w((lambda *s: torch.randn(*s, dtype=torch.bfloat16) * 0.8)(De, D)),
        "w_u": _w((lambda *s: torch.randn(*s, dtype=torch.bfloat16) * 0.05)(D, D)),
        "w_v": _w((lambda *s: torch.randn(*s, dtype=torch.bfloat16) * 0.05)(D, D)),
    }

def reference(x, e_attr, src, dst, w_a, w_b, w_c, w_u, w_v):
    N, D = x.shape
    hv = x @ w_v
    g = x[dst] @ w_a + x[src] @ w_b + e_attr @ w_c
    eta = _sigmoid(g)
    denom = _segment_sum(eta, dst, N) + 1e-6
    eta_n = eta / denom[dst]
    agg = _segment_sum(eta_n * hv[src], dst, N)
    z = x @ w_u + agg
    relu_z = torch.where(z > 0, z, 0 * z)
    return x + relu_z
```

```cpp
#include <hip/hip_runtime.h>
#include <hip/hip_bf16.h>
#include <stdint.h>

using bf16x8 = __attribute__((ext_vector_type(8))) short;
using f32x16 = __attribute__((ext_vector_type(16))) float;
using u32x4 = __attribute__((ext_vector_type(4))) unsigned;
using u32x2 = __attribute__((ext_vector_type(2))) unsigned;
using i32x4 = __attribute__((ext_vector_type(4))) int;
using f32x4 = __attribute__((ext_vector_type(4))) float;
using u16x4 = __attribute__((ext_vector_type(4))) unsigned short;

#define MFMA32(a, b, c) __builtin_amdgcn_mfma_f32_32x32x16_bf16((a), (b), (c), 0, 0, 0)
#define LAS __attribute__((address_space(3)))
#if defined(__HIP_DEVICE_COMPILE__)
#define NOPK __attribute__((target("no-packed-fp32-ops")))
#else
#define NOPK
#endif
#define SRD(p, bytes) __builtin_amdgcn_make_buffer_rsrc((void*)(p), (short)0, (int)(bytes), 0x00020000)

constexpr int NPG = 25;
constexpr int GPB = 8;
constexpr int NPB = NPG * GPB;
constexpr int MAXB = 256;
constexpr int NCH = 256;
constexpr int CHMAX = 3136;
constexpr int NFRAG = 34;

constexpr int P_EST = CHMAX * 4;
constexpr int P_TUP = CHMAX * 4;
constexpr int P_INV = CHMAX * 2;
constexpr int PART_LDS = P_EST + P_TUP + P_INV + MAXB * 4 + 16;

__global__ __launch_bounds__(1024) void k_part(const int* __restrict__ src, const int* __restrict__ dst,
                                               const unsigned short* __restrict__ eattr,
                                               int E, int chunk, int nb, int OS,
                                               unsigned short* __restrict__ O, unsigned* __restrict__ TC,
                                               u32x4* __restrict__ EC,
                                               const unsigned short* __restrict__ wa,
                                               const unsigned short* __restrict__ wb,
                                               const unsigned short* __restrict__ wc,
                                               const unsigned short* __restrict__ wu,
                                               const unsigned short* __restrict__ wv,
                                               unsigned short* __restrict__ wpack) {
  __shared__ __attribute__((aligned(16))) char smem[PART_LDS];
  const int tid = threadIdx.x, lane = tid & 63;
  const int wave = __builtin_amdgcn_readfirstlane(tid >> 6);
  const int c = blockIdx.x;
  const int base = c * chunk;
  const int n = max(min(chunk, E - base), 0);
  LAS unsigned* tup = (LAS unsigned*)(smem + P_EST);
  LAS unsigned short* inv = (LAS unsigned short*)(smem + P_EST + P_TUP);
  LAS int* cnt = (LAS int*)(smem + P_EST + P_TUP + P_INV);

  unsigned wval = 0;
  const bool dow = (c < NFRAG) && (wave < 8);
  if (dow) {
    const int f = c, i = tid;
    const int ln = i >> 3, j = i & 7;
    const int nn = ln & 31, h = ln >> 5;
    const unsigned short* p;
    if (f < 24) {
      const int m = f >> 3, t = (f >> 1) & 3, ct = f & 1;
      const unsigned short* W = (m == 0) ? wb : (m == 1) ? wv : wa;
      p = W + (32 * h + 8 * t + j) * 64 + 32 * ct + nn;
    } else if (f < 26) {
      p = wc + (8 * h + j) * 64 + 32 * (f & 1) + nn;
    } else {
      const int g = f - 26, t = (g >> 1) & 3, ct = g & 1;
      p = wu + (32 * h + 8 * t + j) * 64 + 32 * ct + nn;
    }
    asm volatile("global_load_ushort %0, %1, off" : "=v"(wval) : "v"(p) : "memory");
  }
  i32x4 dd, ss;
  {
    const auto rd = SRD(dst, E * 4), rs = SRD(src, E * 4);
    const unsigned vo = (unsigned)(base + tid * 4) * 4u;
    asm volatile("buffer_load_dwordx4 %0, %2, %3, 0 offen\n\tbuffer_load_dwordx4 %1, %2, %4, 0 offen"
                 : "=&v"(dd), "=&v"(ss) : "v"(vo), "s"(rd), "s"(rs) : "memory");
  }
  if (tid < MAXB) cnt[tid] = 0;
  asm volatile("s_waitcnt lgkmcnt(0)\n\ts_barrier" ::: "memory");
  asm volatile("s_waitcnt vmcnt(0)" : "+v"(dd), "+v"(ss), "+v"(wval) :: "memory");
  if (dow) {
    if (c < 8 || c == 24 || c == 25) {
      unsigned u = __float_as_uint(__uint_as_float(wval << 16) * -1.44269504088896340736f);
      wval = (u + 0x7FFFu + ((u >> 16) & 1u)) >> 16;
    }
    wpack[c * 512 + tid] = (unsigned short)wval;
  }
  int bk[4], rk[4];
#pragma unroll
  for (int k = 0; k < 4; ++k) {
    const bool v = tid * 4 + k < n;
    bk[k] = v ? (int)((unsigned)dd[k] / (unsigned)NPB) : -1;
    rk[k] = 0;
    if (v) rk[k] = __hip_atomic_fetch_add(&cnt[bk[k]], 1, __ATOMIC_RELAXED, __HIP_MEMORY_SCOPE_WORKGROUP);
  }
  asm volatile("s_waitcnt lgkmcnt(0)\n\ts_barrier" ::: "memory");
  if (wave == 0) {
    const i32x4 t = *(LAS i32x4*)(cnt + lane * 4);
    const int s = t[0] + t[1] + t[2] + t[3];
    int inc = s;
#pragma unroll
    for (int o = 1; o < 64; o <<= 1) {
      const int y = __shfl_up(inc, o);
      if (lane >= o) inc += y;
    }
    i32x4 r;
    r[0] = inc - s; r[1] = r[0] + t[0]; r[2] = r[1] + t[1]; r[3] = r[2] + t[2];
    *(LAS i32x4*)(cnt + lane * 4) = r;
    if (lane * 4 <= nb) {
      const u16x4 ov = {(unsigned short)r[0], (unsigned short)r[1], (unsigned short)r[2], (unsigned short)r[3]};
      *reinterpret_cast<u16x4*>(O + (size_t)c * OS + lane * 4) = ov;
    }
    if (lane == 63 && nb == MAXB) O[(size_t)c * OS + MAXB] = (unsigned short)inc;
  }
  asm volatile("s_waitcnt lgkmcnt(0)\n\ts_barrier" ::: "memory");
#pragma unroll
  for (int k = 0; k < 4; ++k) {
    if (bk[k] >= 0) {
      const int pos = cnt[bk[k]] + rk[k];
      tup[pos] = ((unsigned)ss[k] << 8) | (unsigned)(dd[k] - bk[k] * NPB);
      ((LAS unsigned*)smem)[pos] = base + tid * 4 + k;
    }
  }
  asm volatile("s_waitcnt vmcnt(0) lgkmcnt(0)\n\ts_barrier" ::: "memory");
  if (tid * 4 < n) *reinterpret_cast<u32x4*>(TC + base + tid * 4) = *(LAS u32x4*)(tup + tid * 4);
  if (tid * 4 < n) *reinterpret_cast<u32x4*>((unsigned*)EC + base + tid * 4) = *(LAS u32x4*)((LAS unsigned*)smem + tid * 4);
}

__device__ __forceinline__ NOPK unsigned pack_bf16(float lo, float hi) {
  typedef __attribute__((ext_vector_type(2))) float f2;
  typedef __attribute__((ext_vector_type(2))) __bf16 b2;
  f2 v = {lo, hi};
  b2 r = __builtin_convertvector(v, b2);
  return __builtin_bit_cast(unsigned, r);
}
#define WGSYNC() do { __builtin_amdgcn_fence(__ATOMIC_RELEASE, "workgroup"); __builtin_amdgcn_s_barrier(); __builtin_amdgcn_fence(__ATOMIC_ACQUIRE, "workgroup"); } while (0)
#define U2F(u) __builtin_bit_cast(float, (unsigned)(u))
constexpr int RING_SLOT = 5 * 1024;
constexpr int LCAP = 544;
constexpr int LPAD = LCAP + 96;
constexpr int SSCR = 32 * 80;
constexpr int WAVE_LDS = 2 * RING_SLOT + SSCR;
constexpr int M_WL = 0;
constexpr int M_OH = 8 * 1024;
constexpr int M_LX = M_OH + 2048;
constexpr int M_LE = M_LX + GPB * LPAD * 4;
constexpr int M_CNT = M_LE + GPB * LPAD * 4;
constexpr int M_WV = M_CNT + 64;
constexpr int MAIN_LDS = M_WV + GPB * WAVE_LDS;
static_assert(MAIN_LDS <= 160 * 1024, "LDS");

__global__ __launch_bounds__(512, 2) NOPK void k_main(const unsigned short* __restrict__ x,
                                                 const u32x4* __restrict__ EC,
                                                 const unsigned* __restrict__ TC,
                                                 const unsigned short* __restrict__ O,
                                                 int OS, int chunk, int nb,
                                                 const unsigned short* __restrict__ eattr_, int E_,
                                                 const bf16x8* __restrict__ wpack,
                                                 unsigned short* __restrict__ out, int N) {
  const unsigned short* eattr = eattr_; const int E = E_;
  __shared__ __attribute__((aligned(16))) char smem[MAIN_LDS];
  const int tid = threadIdx.x, lane = tid & 63;
  const int wave = __builtin_amdgcn_readfirstlane(tid >> 6);
  const int r = lane & 31, h = lane >> 5;
  int b;
  {
    const int q = nb >> 3, rm = nb & 7, xc = blockIdx.x & 7, ix = blockIdx.x >> 3;
    b = (xc < rm ? xc * (q + 1) : rm * (q + 1) + (xc - rm) * q) + ix;
  }
  const int g = b * GPB + wave;
  const int n0 = min(g * NPG, N - 1);
  const bool active = g * NPG < N;
  const int nodes = active ? min(NPG, N - n0) : 1;

  const int ck = tid >> 1, hf = tid & 1;
  int lo, rn;
  {
    const unsigned short* op = O + (size_t)ck * OS + b;
    lo = op[0];
    rn = (int)op[1] - lo;
  }
  bf16x8 xg[4];
  {
    const int node = n0 + min(r, nodes - 1);
    const bf16x8* xs = reinterpret_cast<const bf16x8*>(x + (size_t)node * 64 + 32 * h);
#pragma unroll
    for (int k = 0; k < 4; ++k) xg[k] = xs[k];
  }
  bf16x8 wa[8];
#pragma unroll
  for (int k = 0; k < 8; ++k) wa[k] = wpack[(16 + k) * 64 + lane];

  bf16x8 wB[4][2], wC[2];
#pragma unroll
  for (int k = 0; k < 8; ++k) wB[k >> 1][k & 1] = wpack[k * 64 + lane];
  wC[0] = wpack[24 * 64 + lane];
  wC[1] = wpack[25 * 64 + lane];
  LAS int* cnt = (LAS int*)(smem + M_CNT);
  if (tid < 16) cnt[tid] = 0;
  {
    bf16x8* wls = reinterpret_cast<bf16x8*>(smem + M_WL);
    wls[tid] = wpack[8 * 64 + tid];
    unsigned* oh = reinterpret_cast<unsigned*>(smem + M_OH);
    const int i = tid;
    const int sl = i >> 4, hh = (i >> 3) & 1, ks = (i >> 2) & 1, jp = i & 3;
    const int j0 = 2 * jp, j1 = 2 * jp + 1;
    const int nd0 = 16 * ks + 8 * (j0 >> 2) + 4 * hh + (j0 & 3);
    const int nd1 = 16 * ks + 8 * (j1 >> 2) + 4 * hh + (j1 & 3);
    oh[i] = (sl == nd0 ? 0x3F80u : 0u) | (sl == nd1 ? 0x3F800000u : 0u);
  }
  const unsigned rec0 = (unsigned)(ck * chunk + lo);
  u32x4 tv0, tv1;
  {
    const unsigned* tp = TC + rec0 + hf * 8;
    tv0 = u32x4{tp[0], tp[1], tp[2], tp[3]};
    tv1 = u32x4{tp[4], tp[5], tp[6], tp[7]};
  }
  u32x4 te0, te1;
  const unsigned* TE = (const unsigned*)EC;
  {
    const unsigned* tp = TE + rec0 + hf * 8;
    te0 = u32x4{tp[0], tp[1], tp[2], tp[3]};
    te1 = u32x4{tp[4], tp[5], tp[6], tp[7]};
  }

  const LAS bf16x8* wl = (const LAS bf16x8*)(smem + M_WL);
  const LAS bf16x8* ohl = (const LAS bf16x8*)(smem + M_OH);
  LAS unsigned* lxa = (LAS unsigned*)(smem + M_LX);
  LAS unsigned* lea = (LAS unsigned*)(smem + M_LE);
  char* wbase = smem + M_WV + wave * WAVE_LDS;
  const unsigned ring_a = (unsigned)(size_t)(LAS char*)wbase;

  auto put = [&](unsigned t, unsigned rec) NOPK {
    const unsigned s = t & 255u;
    const unsigned gg = (s * 41u) >> 10;
    const unsigned sl = s - gg * NPG;
    const int rk = __hip_atomic_fetch_add(&cnt[gg], 1, __ATOMIC_RELAXED, __HIP_MEMORY_SCOPE_WORKGROUP);
    if (rk < LCAP) {
      lxa[gg * LPAD + rk] = (t >> 8) << 7;
      lea[gg * LPAD + rk] = (rec << 5) | sl;
    }
  };

  bf16x8 XAf[2][2];
#pragma unroll
  for (int ct = 0; ct < 2; ++ct) {
    f32x16 A;
#pragma unroll
    for (int i = 0; i < 16; ++i) A[i] = 0.f;
#pragma unroll
    for (int k = 0; k < 4; ++k) A = MFMA32(xg[k], wa[k * 2 + ct], A);
#pragma unroll
    for (int s = 0; s < 2; ++s) {
      u32x4 p;
#pragma unroll
      for (int j = 0; j < 4; ++j) p[j] = pack_bf16(A[8 * s + 2 * j] * -1.44269504088896340736f, A[8 * s + 2 * j + 1] * -1.44269504088896340736f);
      XAf[s][ct] = __builtin_bit_cast(bf16x8, p);
    }
  }
  WGSYNC();
#pragma unroll
  for (int t = 0; t < 8; ++t) {
    const int i = hf * 8 + t;
    if (i < rn) put(t < 4 ? tv0[t & 3] : tv1[t & 3], t < 4 ? te0[t & 3] : te1[t & 3]);
  }
  for (int i = 16 + hf; i < rn; i += 2) put(TC[rec0 + i], TE[rec0 + i]);
  WGSYNC();
  bool slow = false;
  {
    const int cm = cnt[lane & 7];
    slow = __builtin_amdgcn_ballot_w64(cm > LCAP) != 0;
  }
  int pos0 = 0, total = 1;
  if (slow) {
    LAS int* ws = (LAS int*)(smem + M_WV);
    const int v = hf ? 0 : rn;
    int inc = v;
#pragma unroll 1
    for (int o = 1; o < 64; o <<= 1) {
      const int y = __builtin_amdgcn_ds_bpermute((lane - o) * 4, inc);
      if (lane >= o) inc += y;
    }
    if (lane == 63) ws[wave] = inc;
    WGSYNC();
    pos0 = inc - v - (hf ? rn : 0);
    total = 0;
#pragma unroll 1
    for (int i = 0; i < 8; ++i) {
      const int w = ws[i];
      if (i < wave) pos0 += w;
      total += w;
    }
    WGSYNC();
  }

  f32x16 Y[4];
#pragma unroll
  for (int q = 0; q < 4; ++q)
#pragma unroll
    for (int i = 0; i < 16; ++i) Y[q][i] = 0.f;

  const auto rx = SRD(x, N * 128);
  const auto re = SRD(eattr, E * 32);
  const unsigned xc0 = (unsigned)(((lane & 7) ^ ((lane >> 2) & 4)) * 16);
  const unsigned ec0 = (unsigned)(((lane & 1) ^ ((lane >> 4) & 1)) * 16);
  const LAS unsigned* lx = lxa + wave * LPAD;
  const LAS unsigned* le = lea + wave * LPAD;
  const int ohoff = ((((r >> 2) & 1) * 16) + ((r >> 4) * 8) + (((r >> 3) & 1) * 4) + (r & 3)) * 2;
  const int xa0 = (r & 3) * 1024 + (r >> 2) * 128 + (((r & 3) | ((h ^ ((r >> 3) & 1)) << 2)) * 16);
  const int ea0 = 4096 + r * 32 + ((h ^ ((r >> 3) & 1)) * 16);
#define SB() __builtin_amdgcn_sched_barrier(0)
#define GLDS(vo, rs, mv) asm volatile("s_mov_b32 m0, %2\n\ts_nop 0\n\tbuffer_load_dwordx4 %0, %1, 0 offen lds" \
                                      :: "v"(vo), "s"(rs), "s"(mv) : "memory")
  asm volatile("" : "+v"(wB[0][0]), "+v"(wB[0][1]), "+v"(wB[1][0]), "+v"(wB[1][1]), "+v"(wB[2][0]), "+v"(wB[2][1]),
               "+v"(wB[3][0]), "+v"(wB[3][1]), "+v"(wC[0]), "+v"(wC[1]));
  const LAS bf16x8* wvl = wl + lane;
  f32x16 G0, G1, V;
  u32x4 pd[2], pn[2];
  bf16x8 S[2], St[2], as[4], ae, bv[4];
  const f32x16 zero16 = {0.f, 0.f, 0.f, 0.f, 0.f, 0.f, 0.f, 0.f, 0.f, 0.f, 0.f, 0.f, 0.f, 0.f, 0.f, 0.f};
  LAS char* sm = (LAS char*)wbase + 2 * RING_SLOT;
  LAS u32x4* mine = (LAS u32x4*)(sm + r * 80 + h * 32);
  {
    const u32x4 z4 = {0u, 0u, 0u, 0u};
    mine[0] = z4;
    mine[1] = z4;
  }
  const LAS char* ring = (const LAS char*)wbase;
  unsigned ga[5];
  unsigned slr;
  int sl;
  int rs = 0;

#define E1(i) Gc[i] = __builtin_amdgcn_exp2f(Gc[i])
#define E2(i) Gc[i] = Gc[i] + 1.0f
#define E3(i) Gc[i] = __builtin_amdgcn_rcpf(Gc[i])
#define MU(i) V[i] = V[i] * Gc[i]
#define CD(j) pd[(j) >> 2][(j) & 3] = pack_bf16(Gc[2 * (j)], Gc[2 * (j) + 1])
#define CN(j) pn[(j) >> 2][(j) & 3] = pack_bf16(V[2 * (j)], V[2 * (j) + 1])
#define BC(v) __builtin_bit_cast(bf16x8, v)

  for (int wb0 = 0; wb0 < total; wb0 += LCAP) {
    if (slow) {
      asm volatile("s_waitcnt vmcnt(0)" ::: "memory");
      WGSYNC();
      if (tid < 16) cnt[tid] = 0;
      WGSYNC();
      const int i0 = max(wb0 - pos0, 0), i1 = min(wb0 + LCAP - pos0, rn);
#pragma unroll 1
      for (int i = i0 + hf; i < i1; i += 2) put(TC[rec0 + i], TE[rec0 + i]);
      WGSYNC();
    }
    const int cn = __builtin_amdgcn_readfirstlane(cnt[wave]);
    const int ntiles = (cn + 31) >> 5;
    {
      LAS unsigned* wx = lxa + wave * LPAD;
      LAS unsigned* we = lea + wave * LPAD;
      const int e1 = ntiles * 32 + 96;
      for (int p = cn + lane; p < e1; p += 64) { wx[p] = 0xFFFFFF80u; we[p] = 0xFFFFFFFFu; }
    }
    if (ntiles == 0) continue;
    const LAS unsigned* lxp = lx + (lane >> 3) * 4;
    const LAS unsigned* lep = le + (lane >> 1);
    const LAS unsigned* lsp = le + r;
#pragma unroll
    for (int t = 0; t < 3; ++t) {
      const u32x4 xo = *(const LAS u32x4*)(lxp + t * 32);
      const unsigned eo = lep[t * 32];
      ga[0] = xo[0] ^ xc0; ga[1] = xo[1] ^ xc0 ^ 16u; ga[2] = xo[2] ^ xc0 ^ 32u; ga[3] = xo[3] ^ xc0 ^ 48u;
      ga[4] = (eo & ~31u) | ec0;
      if (t < 2) {
        const unsigned d = ring_a + (rs ^ (t * RING_SLOT));
        GLDS(ga[0], rx, d); GLDS(ga[1], rx, d + 1024); GLDS(ga[2], rx, d + 2048); GLDS(ga[3], rx, d + 3072);
        GLDS(ga[4], re, d + 4096);
      }
    }
    sl = lsp[0] & 31u;
    St[0] = ohl[sl * 4 + h * 2 + 0];
    St[1] = ohl[sl * 4 + h * 2 + 1];
    bv[0] = wvl[0 * 64];
    bv[1] = wvl[2 * 64];
    asm volatile("s_waitcnt vmcnt(5)" ::: "memory");
#pragma unroll
    for (int k = 0; k < 4; ++k) as[k] = *(const LAS bf16x8*)(ring + rs + (xa0 ^ (16 * k)));
    ae = *(const LAS bf16x8*)(ring + rs + ea0);
    G0 = MFMA32(St[0], XAf[0][0], zero16);
    G0 = MFMA32(St[1], XAf[1][0], G0);
    G0 = MFMA32(ae, wC[0], G0);
#pragma unroll
    for (int k = 0; k < 4; ++k) G0 = MFMA32(as[k], wB[k][0], G0);
    pd[1] = u32x4{0u, 0u, 0u, 0u};
    pn[1] = u32x4{0u, 0u, 0u, 0u};
    S[1] = BC(pd[1]);
    SB();
    for (int i = 0; i < ntiles; ++i) {
      {
        f32x16& Gc = G0;
        const unsigned d = ring_a + rs;
        LAS unsigned short* ohw = (LAS unsigned short*)(sm + sl * 80 + ohoff);
        Y[1] = MFMA32(S[1], BC(pd[1]), Y[1]);
        bv[2] = wvl[4 * 64];
        E1(0); E1(1); E1(2); E1(3);
        SB();
        Y[3] = MFMA32(S[1], BC(pn[1]), Y[3]);
        bv[3] = wvl[6 * 64];
        E2(0); E2(1); E1(4); E1(5); E2(2); E2(3);
        SB();
        V = MFMA32(as[0], bv[0], zero16);
        E3(0); E3(1); E1(6); E1(7); E2(4); E2(5);
        SB();
        V = MFMA32(as[1], bv[1], V);
        E3(2); E3(3); E1(8); E1(9); E2(6); E2(7);
        SB();
        V = MFMA32(as[2], bv[2], V);
        E3(4); E3(5); E1(10); E1(11); E2(8); E2(9); CD(0);
        SB();
        V = MFMA32(as[3], bv[3], V);
        E3(6); E3(7); E1(12); E1(13); E2(10); E2(11); CD(1);
        SB();
        G1 = MFMA32(St[0], XAf[0][1], zero16);
        E3(8); E3(9); E1(14); E1(15); E2(12); E2(13); CD(2);
        SB();
        G1 = MFMA32(St[1], XAf[1][1], G1);
        *ohw = (unsigned short)0x3F80;
        asm volatile("" ::: "memory");
        S[0] = BC(mine[0]);
        S[1] = BC(mine[1]);
        asm volatile("" ::: "memory");
        *ohw = (unsigned short)0;
        E3(10); E3(11); E2(14); E2(15); CD(3);
        SB();
        G1 = MFMA32(ae, wC[1], G1);
        GLDS(ga[0], rx, d);
        E3(12); E3(13); MU(0); MU(1); MU(2); MU(3);
        SB();
        G1 = MFMA32(as[0], wB[0][1], G1);
        GLDS(ga[1], rx, d + 1024);
        E3(14); E3(15); MU(4); MU(5); CN(0); CN(1);
        SB();
        G1 = MFMA32(as[1], wB[1][1], G1);
        GLDS(ga[2], rx, d + 2048);
        MU(6); MU(7); CD(4); CD(5); CN(2);
        SB();
        G1 = MFMA32(as[2], wB[2][1], G1);
        GLDS(ga[3], rx, d + 3072);
        MU(8); MU(9); MU(10); MU(11); CN(3); CD(6);
        SB();
        G1 = MFMA32(as[3], wB[3][1], G1);
        GLDS(ga[4], re, d + 4096);
        slr = lsp[(i + 1) * 32];
        MU(12); MU(13); CN(4); CN(5); CD(7);
        SB();
        Y[0] = MFMA32(S[0], BC(pd[0]), Y[0]);
        bv[0] = wvl[1 * 64];
        MU(14); MU(15); CN(6);
        SB();
        Y[2] = MFMA32(S[0], BC(pn[0]), Y[2]);
        bv[1] = wvl[3 * 64];
        CN(7);
        SB();
      }
      rs ^= RING_SLOT;
      {
        f32x16& Gc = G1;
        const LAS char* rp = ring + rs;
        sl = slr & 31u;
        Y[0] = MFMA32(S[1], BC(pd[1]), Y[0]);
        bv[2] = wvl[5 * 64];
        St[0] = ohl[sl * 4 + h * 2 + 0];
        E1(0); E1(1); E1(2); E1(3);
        SB();
        Y[2] = MFMA32(S[1], BC(pn[1]), Y[2]);
        bv[3] = wvl[7 * 64];
        St[1] = ohl[sl * 4 + h * 2 + 1];
        E2(0); E2(1); E1(4); E1(5); E2(2); E2(3);
        SB();
        V = MFMA32(as[0], bv[0], zero16);
        E3(0); E3(1); E1(6); E1(7); E2(4); E2(5);
        SB();
        V = MFMA32(as[1], bv[1], V);
        E3(2); E3(3); E1(8); E1(9); E2(6); E2(7);
        SB();
        V = MFMA32(as[2], bv[2], V);
        asm volatile("s_waitcnt vmcnt(5)" ::: "memory");
        ae = *(const LAS bf16x8*)(rp + ea0);
        E3(4); E3(5); E1(10); E1(11); E2(8); E2(9); CD(0);
        SB();
        V = MFMA32(as[3], bv[3], V);
        as[0] = *(const LAS bf16x8*)(rp + xa0);
        E3(6); E3(7); E1(12); E1(13); E2(10); E2(11); CD(1);
        SB();
        G0 = MFMA32(St[0], XAf[0][0], zero16);
        as[1] = *(const LAS bf16x8*)(rp + (xa0 ^ 16));
        E3(8); E3(9); E1(14); E1(15); E2(12); E2(13); CD(2);
        SB();
        G0 = MFMA32(St[1], XAf[1][0], G0);
        as[2] = *(const LAS bf16x8*)(rp + (xa0 ^ 32));
        E3(10); E3(11); E2(14); E2(15); CD(3);
        SB();
        G0 = MFMA32(ae, wC[0], G0);
        as[3] = *(const LAS bf16x8*)(rp + (xa0 ^ 48));
        E3(12); E3(13); MU(0); MU(1); MU(2); MU(3);
        SB();
        G0 = MFMA32(as[0], wB[0][0], G0);
        const u32x4 xo = *(const LAS u32x4*)(lxp + (i + 3) * 32);
        const unsigned eo = lep[(i + 3) * 32];
        E3(14); E3(15); MU(4); MU(5); CN(0); CN(1);
        SB();
        G0 = MFMA32(as[1], wB[1][0], G0);
        MU(6); MU(7); CD(4); CD(5); CN(2);
        SB();
        G0 = MFMA32(as[2], wB[2][0], G0);
        MU(8); MU(9); MU(10); MU(11); CN(3); CD(6);
        SB();
        G0 = MFMA32(as[3], wB[3][0], G0);
        ga[0] = xo[0] ^ xc0; ga[1] = xo[1] ^ xc0 ^ 16u;
        MU(12); MU(13); CN(4); CN(5); CD(7);
        SB();
        Y[1] = MFMA32(S[0], BC(pd[0]), Y[1]);
        bv[0] = wvl[0 * 64];
        ga[2] = xo[2] ^ xc0 ^ 32u; ga[3] = xo[3] ^ xc0 ^ 48u;
        MU(14); MU(15); CN(6);
        SB();
        Y[3] = MFMA32(S[0], BC(pn[0]), Y[3]);
        bv[1] = wvl[2 * 64];
        ga[4] = (eo & ~31u) | ec0;
        CN(7);
        SB();
      }
    }
    Y[1] = MFMA32(S[1], BC(pd[1]), Y[1]);
    Y[3] = MFMA32(S[1], BC(pn[1]), Y[3]);
  }
  u32x4 xr[4];
#pragma unroll
  for (int q = 0; q < 4; ++q) {
    const int row = min(q * 8 + (lane >> 3), nodes - 1);
    xr[q] = *reinterpret_cast<const u32x4*>(x + (size_t)(n0 + row) * 64 + (lane & 7) * 8);
  }
  bf16x8 xg2[4];
  {
    const int node = n0 + min(r, nodes - 1);
    const bf16x8* xs = reinterpret_cast<const bf16x8*>(x + (size_t)node * 64 + 32 * h);
#pragma unroll
    for (int k = 0; k < 4; ++k) xg2[k] = __builtin_nontemporal_load(xs + k);
  }
  bf16x8 wu[8];
#pragma unroll
  for (int k = 0; k < 8; ++k) wu[k] = wpack[(26 + k) * 64 + lane];
  asm volatile("s_waitcnt vmcnt(0)" ::: "memory");
  if (!active) return;

  LAS float* zl = (LAS float*)wbase;
#pragma unroll
  for (int ct = 0; ct < 2; ++ct) {
    f32x16 U;
#pragma unroll
    for (int i = 0; i < 16; ++i) U[i] = 0.f;
#pragma unroll
    for (int k = 0; k < 4; ++k) U = MFMA32(xg2[k], wu[k * 2 + ct], U);
#pragma unroll
    for (int i = 0; i < 16; ++i) {
      const int row = (i & 3) + 8 * (i >> 2) + 4 * h;
      const float z = U[i] + Y[2 + ct][i] / (Y[ct][i] + 1e-6f);
      zl[row * 68 + ct * 32 + r] = z > 0.f ? z : 0.f;
    }
  }
#pragma unroll
  for (int q = 0; q < 4; ++q) {
    const int row = q * 8 + (lane >> 3);
    const f32x4 z0 = *(const LAS f32x4*)(&zl[row * 68 + (lane & 7) * 8]);
    const f32x4 z1 = *(const LAS f32x4*)(&zl[row * 68 + (lane & 7) * 8 + 4]);
    u32x4 o;
    o[0] = pack_bf16(U2F(xr[q][0] << 16) + z0[0], U2F(xr[q][0] & 0xffff0000u) + z0[1]);
    o[1] = pack_bf16(U2F(xr[q][1] << 16) + z0[2], U2F(xr[q][1] & 0xffff0000u) + z0[3]);
    o[2] = pack_bf16(U2F(xr[q][2] << 16) + z1[0], U2F(xr[q][2] & 0xffff0000u) + z1[1]);
    o[3] = pack_bf16(U2F(xr[q][3] << 16) + z1[2], U2F(xr[q][3] & 0xffff0000u) + z1[3]);
    if (row < nodes) *reinterpret_cast<u32x4*>(out + (size_t)(n0 + row) * 64 + (lane & 7) * 8) = o;
  }
}

extern "C" void kernel_launch(void* const* d_in, const int* in_sizes, int n_in,
                              void* d_out, int out_size, void* d_ws, size_t ws_size,
                              hipStream_t stream) {
  const unsigned short* x = (const unsigned short*)d_in[0];
  const unsigned short* eattr = (const unsigned short*)d_in[1];
  const int* src = (const int*)d_in[2];
  const int* dst = (const int*)d_in[3];
  const unsigned short* wa = (const unsigned short*)d_in[4];
  const unsigned short* wb = (const unsigned short*)d_in[5];
  const unsigned short* wc = (const unsigned short*)d_in[6];
  const unsigned short* wu = (const unsigned short*)d_in[7];
  const unsigned short* wv = (const unsigned short*)d_in[8];
  unsigned short* out = (unsigned short*)d_out;
  const int N = in_sizes[0] / 64;
  const int E = in_sizes[2];
  const int nb = (N + NPB - 1) / NPB;
  const int chunk = (((E + NCH - 1) / NCH) + 3) & ~3;
  const int OS = (nb + 4) & ~3;

  char* ws = (char*)d_ws;
  size_t off = 0;
  unsigned short* O = (unsigned short*)(ws + off);
  off += ((size_t)NCH * OS * 2 + 255) & ~(size_t)255;
  unsigned short* wpack = (unsigned short*)(ws + off);
  off += NFRAG * 1024;
  unsigned* TC = (unsigned*)(ws + off);
  off += ((size_t)NCH * chunk * 4 + 256 + 255) & ~(size_t)255;
  u32x4* EC = (u32x4*)(ws + off);

  k_part<<<NCH, 1024, 0, stream>>>(src, dst, eattr, E, chunk, nb, OS, O, TC, EC, wa, wb, wc, wu, wv, wpack);
  k_main<<<nb, 512, 0, stream>>>(x, EC, TC, O, OS, chunk, nb, eattr, E, (const bf16x8*)wpack, out, N);
}
```

```cpp
#include <hip/hip_runtime.h>
#include <hip/hip_bf16.h>
#include <stdint.h>

using bf16x8 = __attribute__((ext_vector_type(8))) short;
using f32x16 = __attribute__((ext_vector_type(16))) float;
using u32x4 = __attribute__((ext_vector_type(4))) unsigned;
using u32x2 = __attribute__((ext_vector_type(2))) unsigned;
using i32x4 = __attribute__((ext_vector_type(4))) int;
using f32x4 = __attribute__((ext_vector_type(4))) float;

#define MFMA32(a, b, c) __builtin_amdgcn_mfma_f32_32x32x16_bf16((a), (b), (c), 0, 0, 0)
#define LAS __attribute__((address_space(3)))
#if defined(__HIP_DEVICE_COMPILE__)
#define NOPK __attribute__((target("no-packed-fp32-ops")))
#else
#define NOPK
#endif
#define SRD(p, bytes) __builtin_amdgcn_make_buffer_rsrc((void*)(p), (short)0, (int)(bytes), 0x00020000)
#define LDS_ADD(p, v) __hip_atomic_fetch_add((p), (v), __ATOMIC_RELAXED, __HIP_MEMORY_SCOPE_WORKGROUP)

#define CST(p, v) __builtin_amdgcn_raw_buffer_store_b128((v), rc, (p) * 4, 0, 17)
#define QORD 0
constexpr int GPB = 8;
constexpr int NPB = 196;
constexpr int MAXB = 256;
constexpr int NCH = 256;
constexpr int CELL = 32;
constexpr int NFRAG = 34;
constexpr float NLOG2E = -1.44269504088896340736f;

__global__ __launch_bounds__(1024) void k_part(const int* __restrict__ src, const int* __restrict__ dst,
                                               int E, int chunk, int nb, unsigned qmul,
                                               unsigned* __restrict__ TC,
                                               unsigned* __restrict__ OVN, u32x4* __restrict__ OV,
                                               const unsigned short* __restrict__ wa,
                                               const unsigned short* __restrict__ wb,
                                               const unsigned short* __restrict__ wc,
                                               const unsigned short* __restrict__ wu,
                                               const unsigned short* __restrict__ wv,
                                               unsigned short* __restrict__ wpack) {
  __shared__ __attribute__((aligned(16))) unsigned cT[MAXB * CELL * 2];
  __shared__ int cnt[MAXB + 4];
  const int tid = threadIdx.x;
  const int c = blockIdx.x;
  const int base = c * chunk;
  const int n = max(min(chunk, E - base), 0);
  const auto rd = SRD(dst, E * 4), rs = SRD(src, E * 4);

  i32x4 dd = __builtin_bit_cast(i32x4, __builtin_amdgcn_raw_buffer_load_b128(rd, (base + tid * 4) * 4, 0, 0));
  i32x4 ss = __builtin_bit_cast(i32x4, __builtin_amdgcn_raw_buffer_load_b128(rs, (base + tid * 4) * 4, 0, 0));
  unsigned wval = 0;
  const bool dow = (c < NFRAG) && (tid < 512);
  if (dow) {
    const int f = c, i = tid;
    const int ln = i >> 3, j = i & 7;
    const int nn = ln & 31, h = ln >> 5;
    const unsigned short* p;
    if (f < 24) {
      const int m = f >> 3, t = (f >> 1) & 3, ct = f & 1;
      const unsigned short* W = (m == 0) ? wb : (m == 1) ? wv : wa;
      p = W + (32 * h + 8 * t + j) * 64 + 32 * ct + nn;
    } else if (f < 26) {
      p = wc + (8 * h + j) * 64 + 32 * (f & 1) + nn;
    } else {
      const int g = f - 26, t = (g >> 1) & 3, ct = g & 1;
      p = wu + (32 * h + 8 * t + j) * 64 + 32 * ct + nn;
    }
    wval = *p;
  }
  if (tid < MAXB + 4) cnt[tid] = 0;
  __syncthreads();
  for (int i0 = 0; i0 < n; i0 += 4096) {
    if (i0 > 0) {
      dd = __builtin_bit_cast(i32x4, __builtin_amdgcn_raw_buffer_load_b128(rd, (base + i0 + tid * 4) * 4, 0, 0));
      ss = __builtin_bit_cast(i32x4, __builtin_amdgcn_raw_buffer_load_b128(rs, (base + i0 + tid * 4) * 4, 0, 0));
    }
    int bk[4], rk[4];
#pragma unroll
    for (int k = 0; k < 4; ++k) {
      const bool v = i0 + tid * 4 + k < n;
      bk[k] = v ? (int)((unsigned)dd[k] / (unsigned)NPB) : MAXB + 1;
      rk[k] = LDS_ADD(&cnt[bk[k]], 1);
    }
#pragma unroll
    for (int k = 0; k < 4; ++k) {
      if (bk[k] < MAXB) {
        const unsigned s = (unsigned)(dd[k] - bk[k] * NPB);
        const unsigned g = s & 7u, sl = s >> 3;
        const unsigned rt = ((unsigned)ss[k] << 7) | ((QORD ? __umulhi((unsigned)ss[k], qmul) : 3u) << 5) | (g << 2);
        const unsigned re = ((unsigned)(base + i0 + tid * 4 + k) << 5) | sl;
        if (rk[k] < CELL - 1) {
          const int i = rk[k] + 1;
          cT[bk[k] * (2 * CELL) + i + (i & 16)] = rt;
          cT[bk[k] * (2 * CELL) + i + (i & 16) + 16] = re;
        } else {
          const int o = LDS_ADD(&cnt[MAXB], 1);
          OV[(size_t)base + o] = u32x4{rt, re, (unsigned)bk[k], 0u};
        }
      }
    }
  }
  if (dow) {
    if (c < 8 || c == 24 || c == 25) {
      const unsigned u = __float_as_uint(__uint_as_float(wval << 16) * NLOG2E);
      wval = (u + 0x7FFFu + ((u >> 16) & 1u)) >> 16;
    }
    wpack[c * 512 + tid] = (unsigned short)wval;
  }
  __syncthreads();
  {
    const int b = tid >> 2, q = tid & 3;
    const int m = min(cnt[b], CELL - 1);
    if (b < nb) {
      const auto rc = SRD(TC, MAXB * NCH * CELL * 8);
      const int cell = (b * NCH + c) * (2 * CELL) + 4 * q;
      const unsigned* lc = cT + b * (2 * CELL) + 4 * q;
      if (4 * q <= m) {
        u32x4 v = *reinterpret_cast<const u32x4*>(lc);
        if (q == 0) v[0] = (unsigned)m;
        CST(cell, v);
        CST(cell + 16, *reinterpret_cast<const u32x4*>(lc + 16));
      }
      if (4 * q + 16 <= m) {
        CST(cell + 32, *reinterpret_cast<const u32x4*>(lc + 32));
        CST(cell + 48, *reinterpret_cast<const u32x4*>(lc + 48));
      }
    }
    if (tid == 0) OVN[c] = (unsigned)cnt[MAXB];
  }
}

__device__ __forceinline__ NOPK unsigned pack_bf16(float lo, float hi) {
  typedef __attribute__((ext_vector_type(2))) float f2;
  typedef __attribute__((ext_vector_type(2))) __bf16 b2;
  f2 v = {lo, hi};
  b2 r = __builtin_convertvector(v, b2);
  return __builtin_bit_cast(unsigned, r);
}
#define WGSYNC() do { __builtin_amdgcn_fence(__ATOMIC_RELEASE, "workgroup"); __builtin_amdgcn_s_barrier(); \
                      __builtin_amdgcn_fence(__ATOMIC_ACQUIRE, "workgroup"); } while (0)
#define U2F(u) __builtin_bit_cast(float, (unsigned)(u))
constexpr int RING_SLOT = 5 * 1024;
constexpr int LCAP = 512;
constexpr int LPAD = LCAP + 96;
constexpr int SSCR = 32 * 80;
constexpr int WAVE_LDS = 2 * RING_SLOT;
constexpr int M_WL = 0;
constexpr int M_OH = 16 * 1024;
constexpr int M_CNT = M_OH + 2048;
constexpr int M_ST = M_CNT + 640;
constexpr int M_SC = M_ST + 2048;
constexpr int M_LX = M_SC + GPB * SSCR;
constexpr int M_LE = M_LX + GPB * LPAD * 4;
constexpr int M_WV = M_LE + GPB * LPAD * 4;
constexpr int MAIN_LDS = M_WV + GPB * WAVE_LDS;
static_assert((M_LE - M_LX) % 256 == 0 && M_LE + GPB * LPAD * 4 + NCH * CELL * 4 <= MAIN_LDS, "lists");
static_assert(MAIN_LDS <= 160 * 1024, "LDS");

__global__ __launch_bounds__(512, 2) NOPK void k_main(const unsigned short* __restrict__ x,
                                                      const unsigned short* __restrict__ eattr,
                                                      const unsigned* __restrict__ TC,
                                                      const unsigned* __restrict__ OVN,
                                                      const u32x4* __restrict__ OV,
                                                      int chunk, int E,
                                                      const bf16x8* __restrict__ wpack,
                                                      unsigned short* __restrict__ out, int N) {
  __shared__ __attribute__((aligned(16))) char smem[MAIN_LDS];
  const int tid = threadIdx.x, lane = tid & 63;
  const int wave = __builtin_amdgcn_readfirstlane(tid >> 6);
  const int r = lane & 31, h = lane >> 5;
  const int b = blockIdx.x;
  const int gn0 = b * NPB + wave;
  const int nodes = max((min(NPB - wave, N - gn0) + 7) >> 3, 0);
  const int n0 = min(gn0, N - 1);

  const int pc = lane & 7;
  u32x4 tv[4], te[4];
  {
    const unsigned* cp = TC + ((size_t)b * NCH + wave * 32 + (lane >> 3) * 4) * (2 * CELL) + pc * 4 + (pc & 4) * 4;
#pragma unroll
    for (int k = 0; k < 4; ++k) tv[k] = *reinterpret_cast<const u32x4*>(cp + k * (2 * CELL));
#pragma unroll
    for (int k = 0; k < 4; ++k) te[k] = *reinterpret_cast<const u32x4*>(cp + k * (2 * CELL) + 16);
  }
  const u32x4 ovn4 = *reinterpret_cast<const u32x4*>(OVN + lane * 4);
  LAS int* cnt = (LAS int*)(smem + M_CNT);
  if (tid < 144) cnt[tid] = (tid >= 48 && tid < 112) ? (M_WV - M_LX) + (tid - 48) * 516 : (tid >= 24 && tid < 32) ? (tid - 24) * (LPAD * 4) : 0;
  asm volatile("s_waitcnt lgkmcnt(0)\n\ts_barrier" ::: "memory");
#define GLDS(vo, rs, mv) asm volatile("s_mov_b32 m0, %2\n\ts_nop 0\n\tbuffer_load_dwordx4 %0, %1, 0 offen lds" \
                                      :: "v"(vo), "s"(rs), "s"(mv) : "memory")
  {
    const auto rw = SRD(wpack, NFRAG * 1024);
    const unsigned lb = (unsigned)(size_t)(LAS char*)smem;
    const unsigned vo = (unsigned)(wave * 1024 + lane * 16);
    GLDS(vo, rw, lb + M_SC + wave * 1024);
    GLDS(vo + 8192u, rw, lb + M_WL + wave * 1024);
    GLDS(vo + 16384u, rw, lb + M_SC + 8192 + wave * 1024);
    GLDS(vo + 26624u, rw, lb + M_WL + 8192 + wave * 1024);
    if (wave < 2) GLDS(vo + 24576u, rw, lb + M_SC + 16384 + wave * 1024);
  }
  bf16x8 xg[4];
  {
    const int node = n0 + 8 * max(min(r, nodes - 1), 0);
    const bf16x8* xs = reinterpret_cast<const bf16x8*>(x + (size_t)node * 64 + 32 * h);
#pragma unroll
    for (int k = 0; k < 4; ++k) xg[k] = xs[k];
  }
  {
    unsigned* oh = reinterpret_cast<unsigned*>(smem + M_OH);
    const int i = tid;
    const int sl = i >> 4, hh = (i >> 3) & 1, ks = (i >> 2) & 1, jp = i & 3;
    const int j0 = 2 * jp, j1 = 2 * jp + 1;
    const int nd0 = 16 * ks + 8 * (j0 >> 2) + 4 * hh + (j0 & 3);
    const int nd1 = 16 * ks + 8 * (j1 >> 2) + 4 * hh + (j1 & 3);
    oh[i] = (sl == nd0 ? 0x3F80u : 0u) | (sl == nd1 ? 0x3F800000u : 0u);
  }
  const LAS bf16x8* wl = (const LAS bf16x8*)(smem + M_WL);
  const LAS bf16x8* ohl = (const LAS bf16x8*)(smem + M_OH);
  LAS unsigned* lxa = (LAS unsigned*)(smem + M_LX);
  LAS unsigned* lea = (LAS unsigned*)(smem + M_LE);
  char* wbase = smem + M_WV + wave * WAVE_LDS;
  const unsigned ring_a = (unsigned)(size_t)(LAS char*)wbase;
  {
    int ad[16];
    unsigned ci[16];
    const unsigned dum = (unsigned)(192 + lane * 4);
#pragma unroll
    for (int k = 0; k < 4; ++k) {
      const unsigned m = (unsigned)__builtin_amdgcn_ds_swizzle((int)tv[k][0], 0x0018);
#pragma unroll
      for (int j = 0; j < 4; ++j) {
        const bool v = (unsigned)(pc * 4 + j - 1) < m;
        ci[k * 4 + j] = v ? tv[k][j] & 0x7Cu : dum;
      }
    }
#pragma unroll
    for (int q = 0; q < 16; ++q) ad[q] = LDS_ADD((LAS int*)((LAS char*)cnt + ci[q]), 4);
#pragma unroll
    for (int k = 0; k < 4; ++k) {
#pragma unroll
      for (int j = 0; j < 4; ++j) {
        LAS unsigned* p = (LAS unsigned*)((LAS char*)lxa + ad[k * 4 + j]);
        p[0] = tv[k][j];
        p[GPB * LPAD] = te[k][j];
      }
    }
  }
  if (__builtin_amdgcn_ballot_w64((ovn4[0] | ovn4[1] | ovn4[2] | ovn4[3]) != 0u) != 0) cnt[32] = 1;
  asm volatile("s_waitcnt vmcnt(0) lgkmcnt(0)\n\ts_barrier" : "+v"(xg[0]), "+v"(xg[1]), "+v"(xg[2]), "+v"(xg[3]) :: "memory");
  bool ready = __builtin_amdgcn_ballot_w64(cnt[24 + pc] - pc * (LPAD * 4) > LCAP * 4) == 0;
  ((LAS unsigned*)(smem + M_ST))[tid] = (ready ? 0u : 0xFFFFu) | ((unsigned)(tid & 1) << 16);

  f32x16 Y[4];
#pragma unroll
  for (int q = 0; q < 4; ++q)
#pragma unroll
    for (int i = 0; i < 16; ++i) Y[q][i] = 0.f;

  const auto rx = SRD(x, N * 128);
  const auto re = SRD(eattr, E * 32);
  const unsigned xc0 = (unsigned)(((lane & 7) ^ ((lane >> 2) & 4)) * 16);
  const unsigned ec0 = (unsigned)(((lane & 1) ^ ((lane >> 4) & 1)) * 16);
  const LAS unsigned* lx = lxa + wave * LPAD;
  const LAS unsigned* le = lea + wave * LPAD;
  const int ohoff = ((((r >> 2) & 1) * 16) + ((r >> 4) * 8) + (((r >> 3) & 1) * 4) + (r & 3)) * 2;
  const int xa0 = (r & 3) * 1024 + (r >> 2) * 128 + (((r & 3) | ((h ^ ((r >> 3) & 1)) << 2)) * 16);
  const int ea0 = 4096 + r * 32 + ((h ^ ((r >> 3) & 1)) * 16);
#define SB() __builtin_amdgcn_sched_barrier(0)
  bf16x8 wB[4][2], wC[2], XAf[2][2];
  const LAS bf16x8* wvl = wl + lane;
  f32x16 G0, G1, V;
  u32x4 pd[2], pn[2];
  bf16x8 S[2], St[2], as[4], ae, bv[4];
  const f32x16 zero16 = {0.f, 0.f, 0.f, 0.f, 0.f, 0.f, 0.f, 0.f, 0.f, 0.f, 0.f, 0.f, 0.f, 0.f, 0.f, 0.f};
  LAS char* sm = (LAS char*)(smem + M_SC + wave * SSCR);
  LAS u32x4* mine = (LAS u32x4*)(sm + r * 80 + h * 32);
  const LAS char* ring = (const LAS char*)wbase;
  unsigned ga[5];
  unsigned slr;
  int sl;
  int rs = 0;

#define E1(i) Gc[i] = __builtin_amdgcn_exp2f(Gc[i])
#define E2(i) Gc[i] = Gc[i] + 1.0f
#define E3(i) Gc[i] = __builtin_amdgcn_rcpf(Gc[i])
#define MU(i) V[i] = V[i] * Gc[i]
#define CD(j) pd[(j) >> 2][(j) & 3] = pack_bf16(Gc[2 * (j)], Gc[2 * (j) + 1])
#define CN(j) pn[(j) >> 2][(j) & 3] = pack_bf16(V[2 * (j)], V[2 * (j) + 1])
#define XG(k) ((xo[k] & ~127u) | (xc0 ^ (16u * (k))))
#define BC(v) __builtin_bit_cast(bf16x8, v)

  int more, ntiles;
  const LAS unsigned* lxp = lx + (lane >> 3) * 4;
  const LAS unsigned* lep = le + (lane >> 1);
  const LAS unsigned* lsp = le + r;
  auto round_begin = [&]() NOPK __attribute__((always_inline)) {
    const int cn = min((__builtin_amdgcn_readfirstlane(cnt[24 + wave]) - wave * (LPAD * 4)) >> 2, LCAP);
    more = __builtin_amdgcn_readfirstlane(cnt[32]);
    ntiles = (cn + 31) >> 5;
    {
      LAS unsigned* wx = lxa + wave * LPAD;
      LAS unsigned* we = lea + wave * LPAD;
      const int e0 = ntiles * 32;
      if (cn + r < e0) { wx[cn + r] = 0xFFFFFF80u; we[cn + r] = 0xFFFFFFFFu; }
      wx[e0 + lane] = lane < nodes ? (unsigned)(n0 + 8 * lane) << 7 : 0xFFFFFF80u;
      we[e0 + lane] = 0xFFFFFFFFu;
      wx[e0 + 64 + r] = 0xFFFFFF80u;
      we[e0 + 64 + r] = 0xFFFFFFFFu;
    }
  };
  auto gather = [&](int t, bool issue) NOPK __attribute__((always_inline)) {
    const u32x4 xo = *(const LAS u32x4*)(lxp + t * 32);
    const unsigned eo = lep[t * 32];
    ga[0] = XG(0); ga[1] = XG(1); ga[2] = XG(2); ga[3] = XG(3);
    ga[4] = (eo & ~31u) | ec0;
    if (issue) {
      const unsigned d = ring_a + (rs ^ (t * RING_SLOT));
      GLDS(ga[0], rx, d); GLDS(ga[1], rx, d + 1024); GLDS(ga[2], rx, d + 2048); GLDS(ga[3], rx, d + 3072);
      GLDS(ga[4], re, d + 4096);
    }
  };
  if (ready) {
    round_begin();
    if (wave < 4) gather(0, true);
  }
  {
      const LAS bf16x8* ws = (const LAS bf16x8*)(smem + M_SC) + lane;
#pragma unroll
      for (int ct = 0; ct < 2; ++ct) {
        f32x16 A = zero16;
#pragma unroll
        for (int k = 0; k < 4; ++k) A = MFMA32(xg[k], ws[(8 + k * 2 + ct) * 64], A);
#pragma unroll
        for (int s = 0; s < 2; ++s) {
          u32x4 p;
#pragma unroll
          for (int j = 0; j < 4; ++j) p[j] = pack_bf16(A[8 * s + 2 * j] * NLOG2E, A[8 * s + 2 * j + 1] * NLOG2E);
          XAf[s][ct] = BC(p);
        }
      }
#pragma unroll
      for (int k = 0; k < 8; ++k) wB[k >> 1][k & 1] = ws[k * 64];
      wC[0] = ws[16 * 64];
      wC[1] = ws[17 * 64];
      asm volatile("s_waitcnt lgkmcnt(0)\n\ts_barrier"
                   : "+v"(wB[0][0]), "+v"(wB[0][1]), "+v"(wB[1][0]), "+v"(wB[1][1]), "+v"(wB[2][0]), "+v"(wB[2][1]),
                     "+v"(wB[3][0]), "+v"(wB[3][1]), "+v"(wC[0]), "+v"(wC[1]) :: "memory");
      const u32x4 z4 = {0u, 0u, 0u, 0u};
      mine[0] = z4;
      mine[1] = z4;
    }
  for (;;) {
   if (!ready) {
    asm volatile("s_waitcnt vmcnt(0)" ::: "memory");
    WGSYNC();
    {
      int t2 = (int)__builtin_amdgcn_mbcnt_hi(~0u, __builtin_amdgcn_mbcnt_lo(~0u, 0u));
      asm volatile("" : "+v"(t2));
      const int l2 = t2;
      t2 += wave * 64;
      if (t2 < 16) cnt[24 + t2] = t2 < 8 ? t2 * (LPAD * 4) : 0;
      WGSYNC();
      auto put = [&](unsigned t, unsigned e) NOPK -> bool {
        const unsigned gg = (t >> 2) & 7u;
        const int ad = LDS_ADD(&cnt[24 + gg], 4) >> 2;
        if (ad - (int)gg * LPAD >= LCAP) return false;
        lxa[ad] = t;
        lea[ad] = e;
        return true;
      };
      LAS unsigned* stp = (LAS unsigned*)(smem + M_ST) + t2;
      unsigned pe = *stp & 0xFFFFu, ocur = *stp >> 16;
#pragma unroll 1
      for (int j = 0; j < 16; ++j) {
        if ((pe >> j) & 1u) {
          const unsigned* cp = TC + ((size_t)b * NCH + wave * 32 + (l2 >> 3) * 4 + (j >> 2)) * (2 * CELL);
          const int i = (l2 & 7) * 4 + (j & 3);
          if ((unsigned)(i - 1) >= cp[0] || put(cp[i + (i & 16)], cp[i + (i & 16) + 16])) pe &= ~(1u << j);
        }
      }
      const unsigned on = OVN[t2 >> 1];
      const size_t ob = (size_t)(t2 >> 1) * chunk;
#pragma unroll 1
      for (; ocur < on; ocur += 2) {
        const u32x4 v = OV[ob + ocur];
        if ((int)v[2] == b && !put(v[0], v[1])) break;
      }
      if (pe != 0u || ocur < on) cnt[32] = 1;
      *stp = pe | (ocur << 16);
    }
    WGSYNC();
    round_begin();
   }
    if (!ready || wave >= 4) gather(0, true);
    gather(1, true);
    gather(2, false);
    ready = false;
    sl = lsp[0] & 31u;
    St[0] = ohl[sl * 4 + h * 2 + 0];
    St[1] = ohl[sl * 4 + h * 2 + 1];
    bv[0] = wvl[0 * 64];
    bv[1] = wvl[2 * 64];
    asm volatile("s_waitcnt vmcnt(5)" ::: "memory");
#pragma unroll
    for (int k = 0; k < 4; ++k) as[k] = *(const LAS bf16x8*)(ring + rs + (xa0 ^ (16 * k)));
    ae = *(const LAS bf16x8*)(ring + rs + ea0);
    G0 = MFMA32(St[0], XAf[0][0], zero16);
    G0 = MFMA32(St[1], XAf[1][0], G0);
    G0 = MFMA32(ae, wC[0], G0);
#pragma unroll
    for (int k = 0; k < 4; ++k) G0 = MFMA32(as[k], wB[k][0], G0);
    pd[1] = u32x4{0u, 0u, 0u, 0u};
    pn[1] = u32x4{0u, 0u, 0u, 0u};
    S[1] = BC(pd[1]);
    SB();
    for (int i = 0; i < ntiles; ++i) {
      {
        f32x16& Gc = G0;
        const unsigned d = ring_a + rs;
        LAS unsigned short* ohw = (LAS unsigned short*)(sm + sl * 80 + ohoff);
        Y[1] = MFMA32(S[1], BC(pd[1]), Y[1]);
        bv[2] = wvl[4 * 64];
        E1(0); E1(1); E1(2); E1(3);
        SB();
        Y[3] = MFMA32(S[1], BC(pn[1]), Y[3]);
        bv[3] = wvl[6 * 64];
        E2(0); E2(1); E1(4); E1(5); E2(2); E2(3);
        SB();
        V = MFMA32(as[0], bv[0], zero16);
        E3(0); E3(1); E1(6); E1(7); E2(4); E2(5);
        SB();
        V = MFMA32(as[1], bv[1], V);
        E3(2); E3(3); E1(8); E1(9); E2(6); E2(7);
        SB();
        V = MFMA32(as[2], bv[2], V);
        E3(4); E3(5); E1(10); E1(11); E2(8); E2(9); CD(0);
        SB();
        V = MFMA32(as[3], bv[3], V);
        E3(6); E3(7); E1(12); E1(13); E2(10); E2(11); CD(1);
        SB();
        G1 = MFMA32(St[0], XAf[0][1], zero16);
        E3(8); E3(9); E1(14); E1(15); E2(12); E2(13); CD(2);
        SB();
        G1 = MFMA32(St[1], XAf[1][1], G1);
        *ohw = (unsigned short)0x3F80;
        asm volatile("" ::: "memory");
        S[0] = BC(mine[0]);
        S[1] = BC(mine[1]);
        asm volatile("" ::: "memory");
        *ohw = (unsigned short)0;
        E3(10); E3(11); E2(14); E2(15); CD(3);
        SB();
        G1 = MFMA32(ae, wC[1], G1);
        GLDS(ga[0], rx, d);
        E3(12); E3(13); MU(0); MU(1); MU(2); MU(3);
        SB();
        G1 = MFMA32(as[0], wB[0][1], G1);
        GLDS(ga[1], rx, d + 1024);
        E3(14); E3(15); MU(4); MU(5); CN(0); CN(1);
        SB();
        G1 = MFMA32(as[1], wB[1][1], G1);
        GLDS(ga[2], rx, d + 2048);
        MU(6); MU(7); CD(4); CD(5); CN(2);
        SB();
        G1 = MFMA32(as[2], wB[2][1], G1);
        GLDS(ga[3], rx, d + 3072);
        MU(8); MU(9); MU(10); MU(11); CN(3); CD(6);
        SB();
        G1 = MFMA32(as[3], wB[3][1], G1);
        GLDS(ga[4], re, d + 4096);
        slr = lsp[(i + 1) * 32];
        MU(12); MU(13); CN(4); CN(5); CD(7);
        SB();
        Y[0] = MFMA32(S[0], BC(pd[0]), Y[0]);
        bv[0] = wvl[1 * 64];
        MU(14); MU(15); CN(6);
        SB();
        Y[2] = MFMA32(S[0], BC(pn[0]), Y[2]);
        bv[1] = wvl[3 * 64];
        CN(7);
        SB();
      }
      rs ^= RING_SLOT;
      {
        f32x16& Gc = G1;
        const LAS char* rp = ring + rs;
        sl = slr & 31u;
        Y[0] = MFMA32(S[1], BC(pd[1]), Y[0]);
        bv[2] = wvl[5 * 64];
        St[0] = ohl[sl * 4 + h * 2 + 0];
        E1(0); E1(1); E1(2); E1(3);
        SB();
        Y[2] = MFMA32(S[1], BC(pn[1]), Y[2]);
        bv[3] = wvl[7 * 64];
        St[1] = ohl[sl * 4 + h * 2 + 1];
        E2(0); E2(1); E1(4); E1(5); E2(2); E2(3);
        SB();
        V = MFMA32(as[0], bv[0], zero16);
        E3(0); E3(1); E1(6); E1(7); E2(4); E2(5);
        SB();
        V = MFMA32(as[1], bv[1], V);
        E3(2); E3(3); E1(8); E1(9); E2(6); E2(7);
        SB();
        V = MFMA32(as[2], bv[2], V);
        asm volatile("s_waitcnt vmcnt(5)" ::: "memory");
        ae = *(const LAS bf16x8*)(rp + ea0);
        E3(4); E3(5); E1(10); E1(11); E2(8); E2(9); CD(0);
        SB();
        V = MFMA32(as[3], bv[3], V);
        as[0] = *(const LAS bf16x8*)(rp + xa0);
        E3(6); E3(7); E1(12); E1(13); E2(10); E2(11); CD(1);
        SB();
        G0 = MFMA32(St[0], XAf[0][0], zero16);
        as[1] = *(const LAS bf16x8*)(rp + (xa0 ^ 16));
        E3(8); E3(9); E1(14); E1(15); E2(12); E2(13); CD(2);
        SB();
        G0 = MFMA32(St[1], XAf[1][0], G0);
        as[2] = *(const LAS bf16x8*)(rp + (xa0 ^ 32));
        E3(10); E3(11); E2(14); E2(15); CD(3);
        SB();
        G0 = MFMA32(ae, wC[0], G0);
        as[3] = *(const LAS bf16x8*)(rp + (xa0 ^ 48));
        E3(12); E3(13); MU(0); MU(1); MU(2); MU(3);
        SB();
        G0 = MFMA32(as[0], wB[0][0], G0);
        const u32x4 xo = *(const LAS u32x4*)(lxp + (i + 3) * 32);
        const unsigned eo = lep[(i + 3) * 32];
        E3(14); E3(15); MU(4); MU(5); CN(0); CN(1);
        SB();
        G0 = MFMA32(as[1], wB[1][0], G0);
        MU(6); MU(7); CD(4); CD(5); CN(2);
        SB();
        G0 = MFMA32(as[2], wB[2][0], G0);
        MU(8); MU(9); MU(10); MU(11); CN(3); CD(6);
        SB();
        G0 = MFMA32(as[3], wB[3][0], G0);
        ga[0] = XG(0); ga[1] = XG(1);
        MU(12); MU(13); CN(4); CN(5); CD(7);
        SB();
        Y[1] = MFMA32(S[0], BC(pd[0]), Y[1]);
        bv[0] = wvl[0 * 64];
        ga[2] = XG(2); ga[3] = XG(3);
        MU(14); MU(15); CN(6);
        SB();
        Y[3] = MFMA32(S[0], BC(pn[0]), Y[3]);
        bv[1] = wvl[2 * 64];
        ga[4] = (eo & ~31u) | ec0;
        CN(7);
        SB();
      }
    }
    Y[1] = MFMA32(S[1], BC(pd[1]), Y[1]);
    Y[3] = MFMA32(S[1], BC(pn[1]), Y[3]);
    if (!more) break;
  }
  u32x4 xr[4];
#pragma unroll
  for (int q = 0; q < 4; ++q) {
    const int row = q * 8 + (lane >> 3);
    xr[q] = *(const LAS u32x4*)(ring + rs + (row & 3) * 1024 + (row >> 2) * 128 +
                                (((lane & 7) ^ (((row >> 3) & 1) << 2) ^ (row & 3)) * 16));
  }
  f32x16 U0 = zero16, U1 = zero16;
#pragma unroll
  for (int k = 0; k < 4; ++k) {
    U0 = MFMA32(as[k], wvl[(8 + k * 2) * 64], U0);
    U1 = MFMA32(as[k], wvl[(9 + k * 2) * 64], U1);
  }
  asm volatile("s_waitcnt vmcnt(0) lgkmcnt(0)" : "+v"(xr[0]), "+v"(xr[1]), "+v"(xr[2]), "+v"(xr[3]) :: "memory");
  LAS float* zl = (LAS float*)wbase;
#pragma unroll
  for (int i = 0; i < 16; ++i) {
    const int row = (i & 3) + 8 * (i >> 2) + 4 * h;
    const float z0 = U0[i] + Y[2][i] * __builtin_amdgcn_rcpf(Y[0][i] + 1e-6f);
    const float z1 = U1[i] + Y[3][i] * __builtin_amdgcn_rcpf(Y[1][i] + 1e-6f);
    zl[row * 68 + r] = z0 > 0.f ? z0 : 0.f;
    zl[row * 68 + 32 + r] = z1 > 0.f ? z1 : 0.f;
  }
#pragma unroll
  for (int q = 0; q < 4; ++q) {
    const int row = q * 8 + (lane >> 3);
    const f32x4 z0 = *(const LAS f32x4*)(&zl[row * 68 + (lane & 7) * 8]);
    const f32x4 z1 = *(const LAS f32x4*)(&zl[row * 68 + (lane & 7) * 8 + 4]);
    u32x4 o;
    o[0] = pack_bf16(U2F(xr[q][0] << 16) + z0[0], U2F(xr[q][0] & 0xffff0000u) + z0[1]);
    o[1] = pack_bf16(U2F(xr[q][1] << 16) + z0[2], U2F(xr[q][1] & 0xffff0000u) + z0[3]);
    o[2] = pack_bf16(U2F(xr[q][2] << 16) + z1[0], U2F(xr[q][2] & 0xffff0000u) + z1[1]);
    o[3] = pack_bf16(U2F(xr[q][3] << 16) + z1[2], U2F(xr[q][3] & 0xffff0000u) + z1[3]);
    if (row < nodes) *reinterpret_cast<u32x4*>(out + (size_t)(n0 + 8 * row) * 64 + (lane & 7) * 8) = o;
  }
}

extern "C" void kernel_launch(void* const* d_in, const int* in_sizes, int n_in,
                              void* d_out, int out_size, void* d_ws, size_t ws_size,
                              hipStream_t stream) {
  const unsigned short* x = (const unsigned short*)d_in[0];
  const unsigned short* eattr = (const unsigned short*)d_in[1];
  const int* src = (const int*)d_in[2];
  const int* dst = (const int*)d_in[3];
  const unsigned short* wa = (const unsigned short*)d_in[4];
  const unsigned short* wb = (const unsigned short*)d_in[5];
  const unsigned short* wc = (const unsigned short*)d_in[6];
  const unsigned short* wu = (const unsigned short*)d_in[7];
  const unsigned short* wv = (const unsigned short*)d_in[8];
  unsigned short* out = (unsigned short*)d_out;
  const int N = in_sizes[0] / 64;
  const int E = in_sizes[2];
  const int nb = (N + NPB - 1) / NPB;
  const int chunk = (((E + NCH - 1) / NCH) + 3) & ~3;

  const unsigned qmul = (unsigned)((4ull << 32) / (unsigned long long)N) - 1u;
  char* ws = (char*)d_ws;
  size_t off = 0;
  unsigned* TC = (unsigned*)(ws + off);
  off += (size_t)MAXB * NCH * CELL * 8;
  unsigned* OVN = (unsigned*)(ws + off);
  off += NCH * 4;
  unsigned short* wpack = (unsigned short*)(ws + off);
  off += NFRAG * 1024;
  u32x4* OV = (u32x4*)(ws + off);

  k_part<<<NCH, 1024, 0, stream>>>(src, dst, E, chunk, nb, qmul, TC, OVN, OV, wa, wb, wc, wu, wv, wpack);
  k_main<<<nb, 512, 0, stream>>>(x, eattr, TC, OVN, OV, chunk, E, (const bf16x8*)wpack, out, N);
}
```

```cpp
#include <hip/hip_runtime.h>
#include <hip/hip_bf16.h>
#include <stdint.h>

using bf16x8 = __attribute__((ext_vector_type(8))) short;
using f32x16 = __attribute__((ext_vector_type(16))) float;
using u32x4 = __attribute__((ext_vector_type(4))) unsigned;
using u32x2 = __attribute__((ext_vector_type(2))) unsigned;
using i32x4 = __attribute__((ext_vector_type(4))) int;
using f32x4 = __attribute__((ext_vector_type(4))) float;

#define MFMA32(a, b, c) __builtin_amdgcn_mfma_f32_32x32x16_bf16((a), (b), (c), 0, 0, 0)
#define LAS __attribute__((address_space(3)))
#if defined(__HIP_DEVICE_COMPILE__)
#define NOPK __attribute__((target("no-packed-fp32-ops")))
#else
#define NOPK
#endif
#define SRD(p, bytes) __builtin_amdgcn_make_buffer_rsrc((void*)(p), (short)0, (int)(bytes), 0x00020000)
#define LDS_ADD(p, v) __hip_atomic_fetch_add((p), (v), __ATOMIC_RELAXED, __HIP_MEMORY_SCOPE_WORKGROUP)

#define CST(p, v) __builtin_amdgcn_raw_buffer_store_b128((v), rc, (p) * 4, 0, 17)
#define WALL 1
constexpr int GPB = 8;
constexpr int NPB = 196;
constexpr int MAXB = 256;
constexpr int NCH = 256;
constexpr int CELL = 32;
constexpr int CAP = 28;
constexpr int CSTR = 36;
constexpr int OVCAP = 4096;
constexpr int NFRAG = 34;
constexpr float NLOG2E = -1.44269504088896340736f;

__global__ __launch_bounds__(1024) void k_part(const int* __restrict__ src, const int* __restrict__ dst,
                                               int E, int nb,
                                               unsigned* __restrict__ TC,
                                               u32x4* __restrict__ OVH, u32x4* __restrict__ OV,
                                               const unsigned short* __restrict__ wa,
                                               const unsigned short* __restrict__ wb,
                                               const unsigned short* __restrict__ wc,
                                               const unsigned short* __restrict__ wu,
                                               const unsigned short* __restrict__ wv,
                                               unsigned short* __restrict__ wpack) {
  __shared__ __attribute__((aligned(16))) unsigned cT[MAXB * CSTR];
  __shared__ __attribute__((aligned(16))) unsigned ovl[8];
  __shared__ int cnt[MAXB + 4];
  const int tid = threadIdx.x, lane = tid & 63;
  const int rnd = __builtin_amdgcn_readfirstlane(tid >> 6);
  const int c = blockIdx.x;
  const int e0 = (rnd * NCH + c) * 256 + lane * 4;
  const auto rd = SRD(dst, E * 4), rs = SRD(src, E * 4);

  const i32x4 dd = __builtin_bit_cast(i32x4, __builtin_amdgcn_raw_buffer_load_b128(rd, e0 * 4, 0, 0));
  const i32x4 ss = __builtin_bit_cast(i32x4, __builtin_amdgcn_raw_buffer_load_b128(rs, e0 * 4, 0, 0));
  unsigned wval = 0;
  const bool dow = (c < NFRAG) && (tid < 512);
  if (dow) {
    const int f = c, i = tid;
    const int ln = i >> 3, j = i & 7;
    const int nn = ln & 31, h = ln >> 5;
    const unsigned short* p;
    if (f < 24) {
      const int m = f >> 3, t = (f >> 1) & 3, ct = f & 1;
      const unsigned short* W = (m == 0) ? wb : (m == 1) ? wv : wa;
      p = W + (32 * h + 8 * t + j) * 64 + 32 * ct + nn;
    } else if (f < 26) {
      p = wc + (8 * h + j) * 64 + 32 * (f & 1) + nn;
    } else {
      const int g = f - 26, t = (g >> 1) & 3, ct = g & 1;
      p = wu + (32 * h + 8 * t + j) * 64 + 32 * ct + nn;
    }
    wval = *p;
  }
  if (tid < 2 * MAXB) *reinterpret_cast<u32x2*>(cT + (tid >> 1) * CSTR + (tid & 1) * 16) = u32x2{0u, 0u};
  else if (tid < 3 * MAXB + 4) cnt[tid - 2 * MAXB] = 0;
  else if (tid < 3 * MAXB + 12) ovl[tid - (3 * MAXB + 4)] = 0x100u;
  __syncthreads();
  if ((rnd * NCH + c) * 256 < E) {
    int bk[4], rk[4];
#pragma unroll
    for (int k = 0; k < 4; ++k) {
      const bool v = e0 + k < E;
      bk[k] = v ? (int)((unsigned)dd[k] / (unsigned)NPB) : MAXB + 1;
      rk[k] = LDS_ADD(&cnt[bk[k]], 1);
    }
#pragma unroll
    for (int k = 0; k < 4; ++k) {
      if (bk[k] < MAXB) {
        const unsigned s = (unsigned)(dd[k] - bk[k] * NPB);
        const unsigned w = ((unsigned)ss[k] << 16) | ((s & 7u) << 13) | ((unsigned)(lane * 4 + k) << 5) | (s >> 3);
        if (rk[k] < CAP) {
          const int d = rk[k] + (rk[k] < CAP / 2 ? 2 : 4);
          cT[bk[k] * CSTR + d] = w;
          __hip_atomic_fetch_or(&cT[bk[k] * CSTR + (d & 16) + ((d >> 3) & 1)], (unsigned)rnd << (4 * (d & 7)),
                                __ATOMIC_RELAXED, __HIP_MEMORY_SCOPE_WORKGROUP);
        } else {
          const int o = LDS_ADD(&cnt[MAXB], 1);
          OV[c * OVCAP + o] = u32x4{w >> 9, ((unsigned)(e0 + k) << 5) | (s >> 3), (unsigned)bk[k], 0u};
          if (o < 4) { ovl[2 * o] = w; ovl[2 * o + 1] = ((unsigned)rnd << 12) | (unsigned)bk[k]; }
        }
      }
    }
  }
  if (dow) {
    if (c < 8 || c == 24 || c == 25) {
      const unsigned u = __float_as_uint(__uint_as_float(wval << 16) * NLOG2E);
      wval = (u + 0x7FFFu + ((u >> 16) & 1u)) >> 16;
    }
    wpack[c * 512 + tid] = (unsigned short)wval;
  }
  __syncthreads();
  {
    const auto rc = SRD(TC, MAXB * NCH * CELL * 4);
    const int p = tid & 7;
    const int first = (CAP / 2) * (p >> 2) + 4 * (p & 3) - 2;
#pragma unroll
    for (int q = 0; q < 2; ++q) {
      const int b = (tid >> 3) + q * 128;
      const int m = cnt[b];
      u32x4 v = *reinterpret_cast<const u32x4*>(cT + b * CSTR + 4 * p);
      if ((p & 3) == 0) v[0] |= (unsigned)min(max(m - first - 2, 0), CAP / 2);
      if (b < nb && (WALL || (p & 3) == 0 || first < m)) CST((b * NCH + c) * CELL + 4 * p, v);
    }
    if (tid < 2) {
      u32x4 v = *reinterpret_cast<const u32x4*>(ovl + 4 * tid);
      v[1] |= (unsigned)cnt[MAXB] << 16;
      v[3] |= (unsigned)cnt[MAXB] << 16;
      OVH[c * 2 + tid] = v;
    }
  }
}

__device__ __forceinline__ NOPK unsigned pack_bf16(float lo, float hi) {
  typedef __attribute__((ext_vector_type(2))) float f2;
  typedef __attribute__((ext_vector_type(2))) __bf16 b2;
  f2 v = {lo, hi};
  b2 r = __builtin_convertvector(v, b2);
  return __builtin_bit_cast(unsigned, r);
}
#define WGSYNC() do { __builtin_amdgcn_fence(__ATOMIC_RELEASE, "workgroup"); __builtin_amdgcn_s_barrier(); \
                      __builtin_amdgcn_fence(__ATOMIC_ACQUIRE, "workgroup"); } while (0)
#define U2F(u) __builtin_bit_cast(float, (unsigned)(u))
constexpr int RING_SLOT = 5 * 1024;
constexpr int LCAP = 512;
constexpr int LPAD = LCAP + 96;
constexpr int SSCR = 32 * 80;
constexpr int WAVE_LDS = 2 * RING_SLOT;
constexpr int M_WL = 0;
constexpr int M_OH = 16 * 1024;
constexpr int M_CNT = M_OH + 2048;
constexpr int M_ST = M_CNT + 640;
constexpr int M_SC = M_ST + 2048;
constexpr int M_LX = M_SC + GPB * SSCR;
constexpr int M_LE = M_LX + GPB * LPAD * 4;
constexpr int M_WV = M_LE + GPB * LPAD * 4;
constexpr int MAIN_LDS = M_WV + GPB * WAVE_LDS;
static_assert((M_LE - M_LX) % 256 == 0 && M_LE + GPB * LPAD * 4 + NCH * CELL * 4 <= MAIN_LDS, "lists");
static_assert(MAIN_LDS <= 160 * 1024, "LDS");

__global__ __launch_bounds__(512, 2) NOPK void k_main(const unsigned short* __restrict__ x,
                                                      const unsigned short* __restrict__ eattr,
                                                      const unsigned* __restrict__ TC,
                                                      const u32x4* __restrict__ OVH,
                                                      const u32x4* __restrict__ OV,
                                                      int E,
                                                      const bf16x8* __restrict__ wpack,
                                                      unsigned short* __restrict__ out, int N) {
  __shared__ __attribute__((aligned(16))) char smem[MAIN_LDS];
  const int tid = threadIdx.x, lane = tid & 63;
  const int wave = __builtin_amdgcn_readfirstlane(tid >> 6);
  const int r = lane & 31, h = lane >> 5;
  const int b = blockIdx.x;
  const int gn0 = b * NPB + wave;
  const int nodes = max((min(NPB - wave, N - gn0) + 7) >> 3, 0);
  const int n0 = min(gn0, N - 1);

  const int pc = lane & 7;
  u32x4 tv[4];
  {
    const u32x4* cp = reinterpret_cast<const u32x4*>(TC + ((size_t)b * NCH + wave * 32) * CELL) + lane;
#pragma unroll
    for (int k = 0; k < 4; ++k) tv[k] = cp[k * 64];
  }
  const u32x4 ovh = OVH[tid];
  LAS int* cnt = (LAS int*)(smem + M_CNT);
  if (tid < 96) cnt[tid] = tid >= 32 ? (M_WV - M_LX) + (tid - 32) * 516 : (tid & 3) == 0 ? (tid >> 2) * (LPAD * 4) : 0;
  asm volatile("s_waitcnt lgkmcnt(0)\n\ts_barrier" ::: "memory");
#define GLDS(vo, rs, mv) asm volatile("s_mov_b32 m0, %2\n\ts_nop 0\n\tbuffer_load_dwordx4 %0, %1, 0 offen lds" \
                                      :: "v"(vo), "s"(rs), "s"(mv) : "memory")
  {
    const auto rw = SRD(wpack, NFRAG * 1024);
    const unsigned lb = (unsigned)(size_t)(LAS char*)smem;
    const unsigned vo = (unsigned)(wave * 1024 + lane * 16);
    GLDS(vo, rw, lb + M_SC + wave * 1024);
    GLDS(vo + 8192u, rw, lb + M_WL + wave * 1024);
    GLDS(vo + 16384u, rw, lb + M_SC + 8192 + wave * 1024);
    GLDS(vo + 26624u, rw, lb + M_WL + 8192 + wave * 1024);
    if (wave < 2) GLDS(vo + 24576u, rw, lb + M_SC + 16384 + wave * 1024);
  }
  bf16x8 xg[4];
  {
    const int node = n0 + 8 * max(min(r, nodes - 1), 0);
    const bf16x8* xs = reinterpret_cast<const bf16x8*>(x + (size_t)node * 64 + 32 * h);
#pragma unroll
    for (int k = 0; k < 4; ++k) xg[k] = xs[k];
  }
  {
    unsigned* oh = reinterpret_cast<unsigned*>(smem + M_OH);
    const int i = tid;
    const int sl = i >> 4, hh = (i >> 3) & 1, ks = (i >> 2) & 1, jp = i & 3;
    const int j0 = 2 * jp, j1 = 2 * jp + 1;
    const int nd0 = 16 * ks + 8 * (j0 >> 2) + 4 * hh + (j0 & 3);
    const int nd1 = 16 * ks + 8 * (j1 >> 2) + 4 * hh + (j1 & 3);
    oh[i] = (sl == nd0 ? 0x3F80u : 0u) | (sl == nd1 ? 0x3F800000u : 0u);
  }
  const LAS bf16x8* wl = (const LAS bf16x8*)(smem + M_WL);
  const LAS bf16x8* ohl = (const LAS bf16x8*)(smem + M_OH);
  LAS unsigned* lxa = (LAS unsigned*)(smem + M_LX);
  LAS unsigned* lea = (LAS unsigned*)(smem + M_LE);
  char* wbase = smem + M_WV + wave * WAVE_LDS;
  const unsigned ring_a = (unsigned)(size_t)(LAS char*)wbase;
  {
    int ad[16];
    unsigned xe[16], hw[4];
    const unsigned dum = (unsigned)(128 + lane * 4);
#pragma unroll
    for (int k = 0; k < 4; ++k) {
      const unsigned h0 = (unsigned)__builtin_amdgcn_mov_dpp((int)tv[k][0], 0x00, 0xF, 0xF, true);
      const unsigned h1 = (unsigned)__builtin_amdgcn_mov_dpp((int)tv[k][1], 0x00, 0xF, 0xF, true);
      hw[k] = ((lane & 2) ? h1 : h0) >> ((lane & 1) * 16);
      const unsigned m = h0 & 0xFFu;
#pragma unroll
      for (int j = 0; j < 4; ++j) {
        const bool v = (unsigned)((lane & 3) * 4 + j - 2) < m;
        xe[k * 4 + j] = tv[k][j] >> 9;
        ad[k * 4 + j] = LDS_ADD((LAS int*)((LAS char*)cnt + (v ? xe[k * 4 + j] & 0x70u : dum)), 4);
      }
    }
#pragma unroll
    for (int k = 0; k < 4; ++k) {
      const unsigned c13 = (unsigned)(wave * 32 + k * 8 + (lane >> 3)) << 13;
#pragma unroll
      for (int j = 0; j < 4; ++j) {
        LAS unsigned* p = (LAS unsigned*)((LAS char*)lxa + ad[k * 4 + j]);
        p[0] = xe[k * 4 + j];
        p[GPB * LPAD] = (tv[k][j] & 0x1FFFu) | ((((hw[k] >> (4 * j)) & 15u) << 21) | c13);
      }
    }
  }
  if (__builtin_amdgcn_ballot_w64((ovh[1] & 0x100u) == 0u) != 0) {
#pragma unroll
    for (int q = 0; q < 2; ++q) {
      const unsigned w = ovh[2 * q], m = ovh[2 * q + 1];
      if ((m & 0x1FFu) == (unsigned)b) {
        LAS unsigned* p = (LAS unsigned*)((LAS char*)lxa + LDS_ADD((LAS int*)((LAS char*)cnt + ((w >> 9) & 0x70u)), 4));
        p[0] = w >> 9;
        p[GPB * LPAD] = (w & 0x1FFFu) | ((m & 0xF000u) << 9) | ((unsigned)(tid >> 1) << 13);
      }
    }
    if ((ovh[1] >> 16) > 4u) cnt[1] = 1;
  }
  asm volatile("s_waitcnt vmcnt(0) lgkmcnt(0)\n\ts_barrier" : "+v"(xg[0]), "+v"(xg[1]), "+v"(xg[2]), "+v"(xg[3]) :: "memory");
  bool ready = __builtin_amdgcn_ballot_w64(cnt[4 * pc] - pc * (LPAD * 4) > LCAP * 4) == 0;
  ((LAS unsigned*)(smem + M_ST))[tid] = (ready ? 0x40000u : 0xFFFFu) | ((unsigned)(tid & 1) << 16);

  f32x16 Y[4];
#pragma unroll
  for (int q = 0; q < 4; ++q)
#pragma unroll
    for (int i = 0; i < 16; ++i) Y[q][i] = 0.f;

  const auto rx = SRD(x, N * 128);
  const auto re = SRD(eattr, E * 32);
  const unsigned xc0 = (unsigned)(((lane & 7) ^ ((lane >> 2) & 4)) * 16);
  const unsigned ec0 = (unsigned)(((lane & 1) ^ ((lane >> 4) & 1)) * 16);
  const LAS unsigned* lx = lxa + wave * LPAD;
  const LAS unsigned* le = lea + wave * LPAD;
  const int ohoff = ((((r >> 2) & 1) * 16) + ((r >> 4) * 8) + (((r >> 3) & 1) * 4) + (r & 3)) * 2;
  const int xa0 = (r & 3) * 1024 + (r >> 2) * 128 + (((r & 3) | ((h ^ ((r >> 3) & 1)) << 2)) * 16);
  const int ea0 = 4096 + r * 32 + ((h ^ ((r >> 3) & 1)) * 16);
#define SB() __builtin_amdgcn_sched_barrier(0)
  bf16x8 wB[4][2], wC[2], XAf[2][2];
  const LAS bf16x8* wvl = wl + lane;
  f32x16 G0, G1, V;
  u32x4 pd[2], pn[2];
  bf16x8 S[2], St[2], as[4], ae, bv[4];
  const f32x16 zero16 = {0.f, 0.f, 0.f, 0.f, 0.f, 0.f, 0.f, 0.f, 0.f, 0.f, 0.f, 0.f, 0.f, 0.f, 0.f, 0.f};
  LAS char* sm = (LAS char*)(smem + M_SC + wave * SSCR);
  LAS u32x4* mine = (LAS u32x4*)(sm + r * 80 + h * 32);
  const LAS char* ring = (const LAS char*)wbase;
  unsigned ga[5];
  unsigned slr;
  int sl;
  int rs = 0;

#define E1(i) Gc[i] = __builtin_amdgcn_exp2f(Gc[i])
#define E2(i) Gc[i] = Gc[i] + 1.0f
#define E3(i) Gc[i] = __builtin_amdgcn_rcpf(Gc[i])
#define MU(i) V[i] = V[i] * Gc[i]
#define CD(j) pd[(j) >> 2][(j) & 3] = pack_bf16(Gc[2 * (j)], Gc[2 * (j) + 1])
#define CN(j) pn[(j) >> 2][(j) & 3] = pack_bf16(V[2 * (j)], V[2 * (j) + 1])
#define XG(k) ((xo[k] & ~127u) | (xc0 ^ (16u * (k))))
#define BC(v) __builtin_bit_cast(bf16x8, v)

  int more, ntiles;
  const LAS unsigned* lxp = lx + (lane >> 3) * 4;
  const LAS unsigned* lep = le + (lane >> 1);
  const LAS unsigned* lsp = le + r;
  auto round_begin = [&]() NOPK __attribute__((always_inline)) {
    const int cn = min((__builtin_amdgcn_readfirstlane(cnt[4 * wave]) - wave * (LPAD * 4)) >> 2, LCAP);
    more = __builtin_amdgcn_readfirstlane(cnt[1]);
    ntiles = (cn + 31) >> 5;
    {
      LAS unsigned* wx = lxa + wave * LPAD;
      LAS unsigned* we = lea + wave * LPAD;
      const int e0 = ntiles * 32;
      if (cn + r < e0) { wx[cn + r] = 0xFFFFFF80u; we[cn + r] = 0xFFFFFFFFu; }
      wx[e0 + lane] = lane < nodes ? (unsigned)(n0 + 8 * lane) << 7 : 0xFFFFFF80u;
      we[e0 + lane] = 0xFFFFFFFFu;
      wx[e0 + 64 + r] = 0xFFFFFF80u;
      we[e0 + 64 + r] = 0xFFFFFFFFu;
    }
  };
  auto gather = [&](int t, bool issue) NOPK __attribute__((always_inline)) {
    const u32x4 xo = *(const LAS u32x4*)(lxp + t * 32);
    const unsigned eo = lep[t * 32];
    ga[0] = XG(0); ga[1] = XG(1); ga[2] = XG(2); ga[3] = XG(3);
    ga[4] = (eo & ~31u) | ec0;
    if (issue) {
      const unsigned d = ring_a + (rs ^ (t * RING_SLOT));
      GLDS(ga[0], rx, d); GLDS(ga[1], rx, d + 1024); GLDS(ga[2], rx, d + 2048); GLDS(ga[3], rx, d + 3072);
      GLDS(ga[4], re, d + 4096);
    }
  };
  if (ready) {
    round_begin();
    if (wave < 4) gather(0, true);
  }
  {
      const LAS bf16x8* ws = (const LAS bf16x8*)(smem + M_SC) + lane;
#pragma unroll
      for (int ct = 0; ct < 2; ++ct) {
        f32x16 A = zero16;
#pragma unroll
        for (int k = 0; k < 4; ++k) A = MFMA32(xg[k], ws[(8 + k * 2 + ct) * 64], A);
#pragma unroll
        for (int s = 0; s < 2; ++s) {
          u32x4 p;
#pragma unroll
          for (int j = 0; j < 4; ++j) p[j] = pack_bf16(A[8 * s + 2 * j] * NLOG2E, A[8 * s + 2 * j + 1] * NLOG2E);
          XAf[s][ct] = BC(p);
        }
      }
#pragma unroll
      for (int k = 0; k < 8; ++k) wB[k >> 1][k & 1] = ws[k * 64];
      wC[0] = ws[16 * 64];
      wC[1] = ws[17 * 64];
      asm volatile("s_waitcnt lgkmcnt(0)\n\ts_barrier"
                   : "+v"(wB[0][0]), "+v"(wB[0][1]), "+v"(wB[1][0]), "+v"(wB[1][1]), "+v"(wB[2][0]), "+v"(wB[2][1]),
                     "+v"(wB[3][0]), "+v"(wB[3][1]), "+v"(wC[0]), "+v"(wC[1]) :: "memory");
      const u32x4 z4 = {0u, 0u, 0u, 0u};
      mine[0] = z4;
      mine[1] = z4;
    }
  for (;;) {
   if (!ready) {
    asm volatile("s_waitcnt vmcnt(0)" ::: "memory");
    WGSYNC();
    {
      int t2 = (int)__builtin_amdgcn_mbcnt_hi(~0u, __builtin_amdgcn_mbcnt_lo(~0u, 0u));
      asm volatile("" : "+v"(t2));
      const int l2 = t2;
      t2 += wave * 64;
      if (t2 < 32) cnt[t2] = (t2 & 3) == 0 ? (t2 >> 2) * (LPAD * 4) : 0;
      WGSYNC();
      auto put = [&](unsigned t, unsigned e) NOPK -> bool {
        const unsigned gg = (t >> 4) & 7u;
        const int ad = LDS_ADD(&cnt[4 * gg], 4) >> 2;
        if (ad - (int)gg * LPAD >= LCAP) return false;
        lxa[ad] = t;
        lea[ad] = e;
        return true;
      };
      LAS unsigned* stp = (LAS unsigned*)(smem + M_ST) + t2;
      unsigned pe = *stp & 0xFFFFu, ocur = *stp >> 16;
#pragma unroll 1
      for (int j = 0; j < 16; ++j) {
        if ((pe >> j) & 1u) {
          const int cc = wave * 32 + (j >> 2) * 8 + (l2 >> 3);
          const unsigned* cp = TC + ((size_t)b * NCH + cc) * CELL;
          const int d = (l2 & 7) * 4 + (j & 3);
          const unsigned w = cp[d];
          const unsigned nib = (cp[(d & 16) + ((d >> 3) & 1)] >> (4 * (d & 7))) & 15u;
          if ((unsigned)((d & 15) - 2) >= (cp[d & 16] & 0xFFu) ||
              put(w >> 9, (w & 0x1FFFu) | (nib << 21) | ((unsigned)cc << 13))) pe &= ~(1u << j);
        }
      }
      const unsigned on = OVH[t2][1] >> 16;
      const size_t ob = (size_t)(t2 >> 1) * OVCAP;
#pragma unroll 1
      for (; ocur < on; ocur += 2) {
        const u32x4 v = OV[ob + ocur];
        if ((int)v[2] == b && !put(v[0], v[1])) break;
      }
      if (pe != 0u || ocur < on) cnt[1] = 1;
      *stp = pe | (ocur << 16);
    }
    WGSYNC();
    round_begin();
   }
    if (!ready || wave >= 4) gather(0, true);
    gather(1, true);
    gather(2, false);
    ready = false;
    sl = lsp[0] & 31u;
    St[0] = ohl[sl * 4 + h * 2 + 0];
    St[1] = ohl[sl * 4 + h * 2 + 1];
    bv[0] = wvl[0 * 64];
    bv[1] = wvl[2 * 64];
    asm volatile("s_waitcnt vmcnt(5)" ::: "memory");
#pragma unroll
    for (int k = 0; k < 4; ++k) as[k] = *(const LAS bf16x8*)(ring + rs + (xa0 ^ (16 * k)));
    ae = *(const LAS bf16x8*)(ring + rs + ea0);
    G0 = MFMA32(St[0], XAf[0][0], zero16);
    G0 = MFMA32(St[1], XAf[1][0], G0);
    G0 = MFMA32(ae, wC[0], G0);
#pragma unroll
    for (int k = 0; k < 4; ++k) G0 = MFMA32(as[k], wB[k][0], G0);
    pd[1] = u32x4{0u, 0u, 0u, 0u};
    pn[1] = u32x4{0u, 0u, 0u, 0u};
    S[1] = BC(pd[1]);
    SB();
    for (int i = 0; i < ntiles; ++i) {
      {
        f32x16& Gc = G0;
        const unsigned d = ring_a + rs;
        LAS unsigned short* ohw = (LAS unsigned short*)(sm + sl * 80 + ohoff);
        Y[1] = MFMA32(S[1], BC(pd[1]), Y[1]);
        bv[2] = wvl[4 * 64];
        E1(0); E1(1); E1(2); E1(3);
        SB();
        Y[3] = MFMA32(S[1], BC(pn[1]), Y[3]);
        bv[3] = wvl[6 * 64];
        E2(0); E2(1); E1(4); E1(5); E2(2); E2(3);
        SB();
        V = MFMA32(as[0], bv[0], zero16);
        E3(0); E3(1); E1(6); E1(7); E2(4); E2(5);
        SB();
        V = MFMA32(as[1], bv[1], V);
        E3(2); E3(3); E1(8); E1(9); E2(6); E2(7);
        SB();
        V = MFMA32(as[2], bv[2], V);
        E3(4); E3(5); E1(10); E1(11); E2(8); E2(9); CD(0);
        SB();
        V = MFMA32(as[3], bv[3], V);
        E3(6); E3(7); E1(12); E1(13); E2(10); E2(11); CD(1);
        SB();
        G1 = MFMA32(St[0], XAf[0][1], zero16);
        E3(8); E3(9); E1(14); E1(15); E2(12); E2(13); CD(2);
        SB();
        G1 = MFMA32(St[1], XAf[1][1], G1);
        *ohw = (unsigned short)0x3F80;
        asm volatile("" ::: "memory");
        S[0] = BC(mine[0]);
        S[1] = BC(mine[1]);
        asm volatile("" ::: "memory");
        *ohw = (unsigned short)0;
        E3(10); E3(11); E2(14); E2(15); CD(3);
        SB();
        G1 = MFMA32(ae, wC[1], G1);
        GLDS(ga[0], rx, d);
        E3(12); E3(13); MU(0); MU(1); MU(2); MU(3);
        SB();
        G1 = MFMA32(as[0], wB[0][1], G1);
        GLDS(ga[1], rx, d + 1024);
        E3(14); E3(15); MU(4); MU(5); CN(0); CN(1);
        SB();
        G1 = MFMA32(as[1], wB[1][1], G1);
        GLDS(ga[2], rx, d + 2048);
        MU(6); MU(7); CD(4); CD(5); CN(2);
        SB();
        G1 = MFMA32(as[2], wB[2][1], G1);
        GLDS(ga[3], rx, d + 3072);
        MU(8); MU(9); MU(10); MU(11); CN(3); CD(6);
        SB();
        G1 = MFMA32(as[3], wB[3][1], G1);
        GLDS(ga[4], re, d + 4096);
        slr = lsp[(i + 1) * 32];
        MU(12); MU(13); CN(4); CN(5); CD(7);
        SB();
        Y[0] = MFMA32(S[0], BC(pd[0]), Y[0]);
        bv[0] = wvl[1 * 64];
        MU(14); MU(15); CN(6);
        SB();
        Y[2] = MFMA32(S[0], BC(pn[0]), Y[2]);
        bv[1] = wvl[3 * 64];
        CN(7);
        SB();
      }
      rs ^= RING_SLOT;
      {
        f32x16& Gc = G1;
        const LAS char* rp = ring + rs;
        sl = slr & 31u;
        Y[0] = MFMA32(S[1], BC(pd[1]), Y[0]);
        bv[2] = wvl[5 * 64];
        St[0] = ohl[sl * 4 + h * 2 + 0];
        E1(0); E1(1); E1(2); E1(3);
        SB();
        Y[2] = MFMA32(S[1], BC(pn[1]), Y[2]);
        bv[3] = wvl[7 * 64];
        St[1] = ohl[sl * 4 + h * 2 + 1];
        E2(0); E2(1); E1(4); E1(5); E2(2); E2(3);
        SB();
        V = MFMA32(as[0], bv[0], zero16);
        E3(0); E3(1); E1(6); E1(7); E2(4); E2(5);
        SB();
        V = MFMA32(as[1], bv[1], V);
        E3(2); E3(3); E1(8); E1(9); E2(6); E2(7);
        SB();
        V = MFMA32(as[2], bv[2], V);
        asm volatile("s_waitcnt vmcnt(5)" ::: "memory");
        ae = *(const LAS bf16x8*)(rp + ea0);
        E3(4); E3(5); E1(10); E1(11); E2(8); E2(9); CD(0);
        SB();
        V = MFMA32(as[3], bv[3], V);
        as[0] = *(const LAS bf16x8*)(rp + xa0);
        E3(6); E3(7); E1(12); E1(13); E2(10); E2(11); CD(1);
        SB();
        G0 = MFMA32(St[0], XAf[0][0], zero16);
        as[1] = *(const LAS bf16x8*)(rp + (xa0 ^ 16));
        E3(8); E3(9); E1(14); E1(15); E2(12); E2(13); CD(2);
        SB();
        G0 = MFMA32(St[1], XAf[1][0], G0);
        as[2] = *(const LAS bf16x8*)(rp + (xa0 ^ 32));
        E3(10); E3(11); E2(14); E2(15); CD(3);
        SB();
        G0 = MFMA32(ae, wC[0], G0);
        as[3] = *(const LAS bf16x8*)(rp + (xa0 ^ 48));
        E3(12); E3(13); MU(0); MU(1); MU(2); MU(3);
        SB();
        G0 = MFMA32(as[0], wB[0][0], G0);
        const u32x4 xo = *(const LAS u32x4*)(lxp + (i + 3) * 32);
        const unsigned eo = lep[(i + 3) * 32];
        E3(14); E3(15); MU(4); MU(5); CN(0); CN(1);
        SB();
        G0 = MFMA32(as[1], wB[1][0], G0);
        MU(6); MU(7); CD(4); CD(5); CN(2);
        SB();
        G0 = MFMA32(as[2], wB[2][0], G0);
        MU(8); MU(9); MU(10); MU(11); CN(3); CD(6);
        SB();
        G0 = MFMA32(as[3], wB[3][0], G0);
        ga[0] = XG(0); ga[1] = XG(1);
        MU(12); MU(13); CN(4); CN(5); CD(7);
        SB();
        Y[1] = MFMA32(S[0], BC(pd[0]), Y[1]);
        bv[0] = wvl[0 * 64];
        ga[2] = XG(2); ga[3] = XG(3);
        MU(14); MU(15); CN(6);
        SB();
        Y[3] = MFMA32(S[0], BC(pn[0]), Y[3]);
        bv[1] = wvl[2 * 64];
        ga[4] = (eo & ~31u) | ec0;
        CN(7);
        SB();
      }
    }
    Y[1] = MFMA32(S[1], BC(pd[1]), Y[1]);
    Y[3] = MFMA32(S[1], BC(pn[1]), Y[3]);
    if (!more) break;
  }
  u32x4 xr[4];
#pragma unroll
  for (int q = 0; q < 4; ++q) {
    const int row = q * 8 + (lane >> 3);
    xr[q] = *(const LAS u32x4*)(ring + rs + (row & 3) * 1024 + (row >> 2) * 128 +
                                (((lane & 7) ^ (((row >> 3) & 1) << 2) ^ (row & 3)) * 16));
  }
  f32x16 U0 = zero16, U1 = zero16;
#pragma unroll
  for (int k = 0; k < 4; ++k) {
    U0 = MFMA32(as[k], wvl[(8 + k * 2) * 64], U0);
    U1 = MFMA32(as[k], wvl[(9 + k * 2) * 64], U1);
  }
  asm volatile("s_waitcnt vmcnt(0) lgkmcnt(0)" : "+v"(xr[0]), "+v"(xr[1]), "+v"(xr[2]), "+v"(xr[3]) :: "memory");
  LAS float* zl = (LAS float*)wbase;
#pragma unroll
  for (int i = 0; i < 16; ++i) {
    const int row = (i & 3) + 8 * (i >> 2) + 4 * h;
    const float z0 = U0[i] + Y[2][i] * __builtin_amdgcn_rcpf(Y[0][i] + 1e-6f);
    const float z1 = U1[i] + Y[3][i] * __builtin_amdgcn_rcpf(Y[1][i] + 1e-6f);
    zl[row * 68 + r] = z0 > 0.f ? z0 : 0.f;
    zl[row * 68 + 32 + r] = z1 > 0.f ? z1 : 0.f;
  }
#pragma unroll
  for (int q = 0; q < 4; ++q) {
    const int row = q * 8 + (lane >> 3);
    const f32x4 z0 = *(const LAS f32x4*)(&zl[row * 68 + (lane & 7) * 8]);
    const f32x4 z1 = *(const LAS f32x4*)(&zl[row * 68 + (lane & 7) * 8 + 4]);
    u32x4 o;
    o[0] = pack_bf16(U2F(xr[q][0] << 16) + z0[0], U2F(xr[q][0] & 0xffff0000u) + z0[1]);
    o[1] = pack_bf16(U2F(xr[q][1] << 16) + z0[2], U2F(xr[q][1] & 0xffff0000u) + z0[3]);
    o[2] = pack_bf16(U2F(xr[q][2] << 16) + z1[0], U2F(xr[q][2] & 0xffff0000u) + z1[1]);
    o[3] = pack_bf16(U2F(xr[q][3] << 16) + z1[2], U2F(xr[q][3] & 0xffff0000u) + z1[3]);
    if (row < nodes) *reinterpret_cast<u32x4*>(out + (size_t)(n0 + 8 * row) * 64 + (lane & 7) * 8) = o;
  }
}

extern "C" void kernel_launch(void* const* d_in, const int* in_sizes, int n_in,
                              void* d_out, int out_size, void* d_ws, size_t ws_size,
                              hipStream_t stream) {
  const unsigned short* x = (const unsigned short*)d_in[0];
  const unsigned short* eattr = (const unsigned short*)d_in[1];
  const int* src = (const int*)d_in[2];
  const int* dst = (const int*)d_in[3];
  const unsigned short* wa = (const unsigned short*)d_in[4];
  const unsigned short* wb = (const unsigned short*)d_in[5];
  const unsigned short* wc = (const unsigned short*)d_in[6];
  const unsigned short* wu = (const unsigned short*)d_in[7];
  const unsigned short* wv = (const unsigned short*)d_in[8];
  unsigned short* out = (unsigned short*)d_out;
  const int N = in_sizes[0] / 64;
  const int E = in_sizes[2];
  const int nb = (N + NPB - 1) / NPB;

  char* ws = (char*)d_ws;
  size_t off = 0;
  unsigned* TC = (unsigned*)(ws + off);
  off += (size_t)MAXB * NCH * CELL * 4;
  u32x4* OVH = (u32x4*)(ws + off);
  off += NCH * 32;
  unsigned short* wpack = (unsigned short*)(ws + off);
  off += NFRAG * 1024;
  u32x4* OV = (u32x4*)(ws + off);

  k_part<<<NCH, 1024, 0, stream>>>(src, dst, E, nb, TC, OVH, OV, wa, wb, wc, wu, wv, wpack);
  k_main<<<nb, 512, 0, stream>>>(x, eattr, TC, OVH, OV, E, (const bf16x8*)wpack, out, N);
}
```

```cpp
#include <hip/hip_runtime.h>
#include <hip/hip_bf16.h>
#include <stdint.h>

using bf16x8 = __attribute__((ext_vector_type(8))) short;
using f32x16 = __attribute__((ext_vector_type(16))) float;
using u32x4 = __attribute__((ext_vector_type(4))) unsigned;
using u32x2 = __attribute__((ext_vector_type(2))) unsigned;
using i32x4 = __attribute__((ext_vector_type(4))) int;
using f32x4 = __attribute__((ext_vector_type(4))) float;

#define MFMA32(a, b, c) __builtin_amdgcn_mfma_f32_32x32x16_bf16((a), (b), (c), 0, 0, 0)
#define LAS __attribute__((address_space(3)))
#if defined(__HIP_DEVICE_COMPILE__)
#define NOPK __attribute__((target("no-packed-fp32-ops")))
#else
#define NOPK
#endif
#define SRD(p, bytes) __builtin_amdgcn_make_buffer_rsrc((void*)(p), (short)0, (int)(bytes), 0x00020000)
#define LDS_ADD(p, v) __hip_atomic_fetch_add((p), (v), __ATOMIC_RELAXED, __HIP_MEMORY_SCOPE_WORKGROUP)

#define CST(p, v) __builtin_amdgcn_raw_buffer_store_b128((v), rc, (p) * 4, 0, 17)
#define WALL 1
constexpr int GPB = 8;
constexpr int NPB = 196;
constexpr int MAXB = 256;
constexpr int NCH = 256;
constexpr int CELL = 32;
constexpr int CAP = 28;
constexpr int CSTR = 36;
constexpr int OVCAP = 4096;
constexpr int NFRAG = 34;
constexpr size_t W_TC = 0;
constexpr size_t W_OVH = W_TC + (size_t)MAXB * NCH * CELL * 4;
constexpr size_t W_WP = W_OVH + NCH * 32;
constexpr size_t W_OV = W_WP + NFRAG * 1024;
constexpr float NLOG2E = -1.44269504088896340736f;

__global__ __launch_bounds__(1024) void k_part(const int* __restrict__ src, const int* __restrict__ dst,
                                               int E, int nb,
                                               unsigned* __restrict__ TC,
                                               u32x4* __restrict__ OVH, u32x4* __restrict__ OV,
                                               const unsigned short* __restrict__ wa,
                                               const unsigned short* __restrict__ wb,
                                               const unsigned short* __restrict__ wc,
                                               const unsigned short* __restrict__ wu,
                                               const unsigned short* __restrict__ wv,
                                               unsigned short* __restrict__ wpack) {
  __shared__ __attribute__((aligned(16))) unsigned cT[MAXB * CSTR];
  __shared__ __attribute__((aligned(16))) unsigned ovl[8];
  __shared__ int cnt[MAXB + 4];
  const int tid = threadIdx.x, lane = tid & 63;
  const int rnd = __builtin_amdgcn_readfirstlane(tid >> 6);
  const int c = blockIdx.x;
  const int e0 = (rnd * NCH + c) * 256 + lane * 4;
  const auto rd = SRD(dst, E * 4), rs = SRD(src, E * 4);

  const i32x4 dd = __builtin_bit_cast(i32x4, __builtin_amdgcn_raw_buffer_load_b128(rd, e0 * 4, 0, 0));
  const i32x4 ss = __builtin_bit_cast(i32x4, __builtin_amdgcn_raw_buffer_load_b128(rs, e0 * 4, 0, 0));
  unsigned wval = 0;
  const bool dow = (c < NFRAG) && (tid < 512);
  if (dow) {
    const int f = c, i = tid;
    const int ln = i >> 3, j = i & 7;
    const int nn = ln & 31, h = ln >> 5;
    const unsigned short* p;
    if (f < 24) {
      const int m = f >> 3, t = (f >> 1) & 3, ct = f & 1;
      const unsigned short* W = (m == 0) ? wb : (m == 1) ? wv : wa;
      p = W + (32 * h + 8 * t + j) * 64 + 32 * ct + nn;
    } else if (f < 26) {
      p = wc + (8 * h + j) * 64 + 32 * (f & 1) + nn;
    } else {
      const int g = f - 26, t = (g >> 1) & 3, ct = g & 1;
      p = wu + (32 * h + 8 * t + j) * 64 + 32 * ct + nn;
    }
    wval = *p;
  }
  if (tid < 2 * MAXB) *reinterpret_cast<u32x2*>(cT + (tid >> 1) * CSTR + (tid & 1) * 16) = u32x2{0u, 0u};
  else if (tid < 3 * MAXB + 4) cnt[tid - 2 * MAXB] = 0;
  else if (tid < 3 * MAXB + 12) ovl[tid - (3 * MAXB + 4)] = 0x100u;
  __syncthreads();
  if ((rnd * NCH + c) * 256 < E) {
    int bk[4], rk[4];
#pragma unroll
    for (int k = 0; k < 4; ++k) {
      const bool v = e0 + k < E;
      bk[k] = v ? (int)((unsigned)dd[k] / (unsigned)NPB) : MAXB + 1;
      rk[k] = LDS_ADD(&cnt[bk[k]], 1);
    }
#pragma unroll
    for (int k = 0; k < 4; ++k) {
      if (bk[k] < MAXB) {
        const unsigned s = (unsigned)(dd[k] - bk[k] * NPB);
        const unsigned w = ((unsigned)ss[k] << 16) | ((s & 7u) << 13) | ((unsigned)(lane * 4 + k) << 5) | (s >> 3);
        if (rk[k] < CAP) {
          const int d = rk[k] + (rk[k] < CAP / 2 ? 2 : 4);
          cT[bk[k] * CSTR + d] = w;
          __hip_atomic_fetch_or(&cT[bk[k] * CSTR + (d & 16) + ((d >> 3) & 1)], (unsigned)rnd << (4 * (d & 7)),
                                __ATOMIC_RELAXED, __HIP_MEMORY_SCOPE_WORKGROUP);
        } else {
          const int o = LDS_ADD(&cnt[MAXB], 1);
          OV[c * OVCAP + o] = u32x4{w >> 9, ((unsigned)(e0 + k) << 5) | (s >> 3), (unsigned)bk[k], 0u};
          if (o < 4) { ovl[2 * o] = w; ovl[2 * o + 1] = ((unsigned)rnd << 12) | (unsigned)bk[k]; }
        }
      }
    }
  }
  if (dow) {
    if (c < 8 || c == 24 || c == 25) {
      const unsigned u = __float_as_uint(__uint_as_float(wval << 16) * NLOG2E);
      wval = (u + 0x7FFFu + ((u >> 16) & 1u)) >> 16;
    }
    wpack[c * 512 + tid] = (unsigned short)wval;
  }
  __syncthreads();
  {
    const auto rc = SRD(TC, MAXB * NCH * CELL * 4);
    const int p = tid & 7;
    const int first = (CAP / 2) * (p >> 2) + 4 * (p & 3) - 2;
#pragma unroll
    for (int q = 0; q < 2; ++q) {
      const int b = (tid >> 3) + q * 128;
      const int m = cnt[b];
      u32x4 v = *reinterpret_cast<const u32x4*>(cT + b * CSTR + 4 * p);
      if ((p & 3) == 0) v[0] |= (unsigned)min(max(m - first - 2, 0), CAP / 2);
      if (b < nb && (WALL || (p & 3) == 0 || first < m)) CST((b * NCH + c) * CELL + 4 * p, v);
    }
    if (tid < 2) {
      u32x4 v = *reinterpret_cast<const u32x4*>(ovl + 4 * tid);
      v[1] |= (unsigned)cnt[MAXB] << 16;
      v[3] |= (unsigned)cnt[MAXB] << 16;
      OVH[c * 2 + tid] = v;
    }
  }
}

__device__ __forceinline__ NOPK unsigned pack_bf16(float lo, float hi) {
  typedef __attribute__((ext_vector_type(2))) float f2;
  typedef __attribute__((ext_vector_type(2))) __bf16 b2;
  f2 v = {lo, hi};
  b2 r = __builtin_convertvector(v, b2);
  return __builtin_bit_cast(unsigned, r);
}
#define WGSYNC() do { __builtin_amdgcn_fence(__ATOMIC_RELEASE, "workgroup"); __builtin_amdgcn_s_barrier(); \
                      __builtin_amdgcn_fence(__ATOMIC_ACQUIRE, "workgroup"); } while (0)
#define U2F(u) __builtin_bit_cast(float, (unsigned)(u))
constexpr int RING_SLOT = 5 * 1024;
constexpr int LCAP = 512;
constexpr int LPAD = LCAP + 96;
constexpr int SSCR = 32 * 80;
constexpr int WAVE_LDS = 2 * RING_SLOT;
constexpr int M_WL = 0;
constexpr int M_OH = 16 * 1024;
constexpr int M_CNT = M_OH + 2048;
constexpr int M_ST = M_CNT + 256;
constexpr int M_SC = M_ST + 2048;
constexpr int M_LX = M_SC + GPB * SSCR;
constexpr int M_LE = M_LX + GPB * LPAD * 4;
constexpr int M_WV = M_LE + GPB * LPAD * 4;
constexpr int M_XG = M_WV + 4 * WAVE_LDS;
constexpr int MAIN_LDS = M_WV + GPB * WAVE_LDS;
static_assert(M_LX % 256 == 0 && M_LE % 256 == 0 && M_LE < 65536 && M_LE + GPB * LPAD * 4 + NCH * CELL * 4 <= M_XG, "lists");
static_assert(MAIN_LDS <= 160 * 1024, "LDS");

__global__ __launch_bounds__(512, 2) NOPK void k_main(const unsigned short* __restrict__ x,
                                                      const unsigned short* __restrict__ eattr,
                                                      const char* __restrict__ wsp, int E,
                                                      int N, unsigned short* __restrict__ out) {
  __shared__ __attribute__((aligned(16))) char smem[MAIN_LDS];
  asm volatile("" :: "s"(x), "s"(eattr), "s"(wsp), "s"(E), "s"(N), "s"(out));
  const unsigned* TC = reinterpret_cast<const unsigned*>(wsp + W_TC);
  const u32x4* OVH = reinterpret_cast<const u32x4*>(wsp + W_OVH);
  const u32x4* OV = reinterpret_cast<const u32x4*>(wsp + W_OV);
  const char* wpack = wsp + W_WP;
  const int tid = threadIdx.x, lane = tid & 63;
  const int wave = __builtin_amdgcn_readfirstlane(tid >> 6);
  const int r = lane & 31, h = lane >> 5;
  const int b = blockIdx.x;

  const int pc = lane & 7;
  u32x4 tv[4], ovh;
  {
    const unsigned* cp = TC + ((size_t)b * NCH + wave * 32) * CELL;
    asm volatile("global_load_dwordx4 %0, %5, %6\n\tglobal_load_dwordx4 %1, %5, %6 offset:1024\n\t"
                 "global_load_dwordx4 %2, %5, %6 offset:2048\n\tglobal_load_dwordx4 %3, %5, %6 offset:3072\n\t"
                 "global_load_dwordx4 %4, %5, %7"
                 : "=&v"(tv[0]), "=&v"(tv[1]), "=&v"(tv[2]), "=&v"(tv[3]), "=&v"(ovh)
                 : "v"(lane * 16), "s"(cp), "s"(OVH + wave * 64) : "memory");
  }
#define GLDS(vo, rs, mv) asm volatile("s_mov_b32 m0, %2\n\ts_nop 0\n\tbuffer_load_dwordx4 %0, %1, 0 offen lds" \
                                      :: "v"(vo), "s"(rs), "s"(mv) : "memory")
  const auto rx = SRD(x, N * 128);
  const auto re = SRD(eattr, E * 32);
  const unsigned xc0 = (unsigned)(((lane & 7) ^ ((lane >> 2) & 4)) * 16);
  const unsigned lb = (unsigned)(size_t)(LAS char*)smem;
  LAS int* cnt = (LAS int*)(smem + M_CNT);
  if (tid < 32) cnt[tid] = (tid & 3) == 0 ? (tid >> 2) * (LPAD * 4) : (tid & 3) == 2 ? 127 : 0;
  __builtin_amdgcn_s_setprio(1);
  {
    unsigned* oh = reinterpret_cast<unsigned*>(smem + M_OH);
    const int i = tid;
    const int sl = i >> 4, hh = (i >> 3) & 1, ks = (i >> 2) & 1, jp = i & 3;
    const int j0 = 2 * jp, j1 = 2 * jp + 1;
    const int nd0 = 16 * ks + 8 * (j0 >> 2) + 4 * hh + (j0 & 3);
    const int nd1 = 16 * ks + 8 * (j1 >> 2) + 4 * hh + (j1 & 3);
    oh[i] = (sl == nd0 ? 0x3F80u : 0u) | (sl == nd1 ? 0x3F800000u : 0u);
  }
  asm volatile("s_waitcnt lgkmcnt(0)\n\ts_barrier" ::: "memory");
  {
    const int nw = (min(NPB - wave, N - b * NPB - wave) + 7) >> 3;
#pragma unroll
    for (int k = 0; k < 4; ++k) {
      const int s = (lane >> 3) * 4 + k;
      const unsigned xo = s < nw ? (unsigned)(b * NPB + wave + 8 * s) << 7 : 0xFFFFFF80u;
      GLDS(xo | (xc0 ^ (16u * k)), rx, lb + M_XG + wave * 4096 + k * 1024);
    }
  }
  const LAS bf16x8* wl = (const LAS bf16x8*)(smem + M_WL);
  const LAS bf16x8* ohl = (const LAS bf16x8*)(smem + M_OH);
  LAS unsigned* lxa = (LAS unsigned*)(smem + M_LX);
  LAS unsigned* lea = (LAS unsigned*)(smem + M_LE);
  char* wbase = smem + M_WV + wave * WAVE_LDS;
  const unsigned ring_a = (unsigned)(size_t)(LAS char*)wbase;
  {
    unsigned long long vm[16];
    unsigned ad[16], xe[16], hw[4];
    const unsigned four = 4u;
#pragma unroll
    for (int k = 0; k < 4; ++k) {
      asm volatile("s_waitcnt vmcnt(%1)" : "+v"(tv[k]) : "n"(8 - k) : "memory");
      const unsigned h0 = (unsigned)__builtin_amdgcn_mov_dpp((int)tv[k][0], 0x00, 0xF, 0xF, true);
      const unsigned h1 = (unsigned)__builtin_amdgcn_mov_dpp((int)tv[k][1], 0x00, 0xF, 0xF, true);
      hw[k] = ((lane & 2) ? h1 : h0) >> ((lane & 1) * 16);
      const unsigned m = h0 & 0xFFu;
      unsigned ci[4];
#pragma unroll
      for (int j = 0; j < 4; ++j) {
        vm[4 * k + j] = __builtin_amdgcn_ballot_w64((unsigned)((lane & 3) * 4 + j - 2) < m);
        xe[4 * k + j] = tv[k][j] >> 9;
        ci[j] = (xe[4 * k + j] & 0x70u) + lb;
      }
      asm volatile("s_mov_b64 exec, %4\n\tds_add_rtn_u32 %0, %8, %12 offset:%13\n\t"
                   "s_mov_b64 exec, %5\n\tds_add_rtn_u32 %1, %9, %12 offset:%13\n\t"
                   "s_mov_b64 exec, %6\n\tds_add_rtn_u32 %2, %10, %12 offset:%13\n\t"
                   "s_mov_b64 exec, %7\n\tds_add_rtn_u32 %3, %11, %12 offset:%13\n\t"
                   "s_mov_b64 exec, -1"
                   : "=&v"(ad[4 * k]), "=&v"(ad[4 * k + 1]), "=&v"(ad[4 * k + 2]), "=&v"(ad[4 * k + 3])
                   : "s"(vm[4 * k]), "s"(vm[4 * k + 1]), "s"(vm[4 * k + 2]), "s"(vm[4 * k + 3]),
                     "v"(ci[0]), "v"(ci[1]), "v"(ci[2]), "v"(ci[3]), "v"(four), "n"(M_CNT)
                   : "memory");
    }
#pragma unroll
    for (int k = 0; k < 4; ++k) {
      const unsigned c13 = (unsigned)(wave * 32 + k * 8 + (lane >> 3)) << 13;
      unsigned ee[4];
#pragma unroll
      for (int j = 0; j < 4; ++j) ee[j] = (tv[k][j] & 0x1FFFu) | ((((hw[k] >> (4 * j)) & 15u) << 21) | c13);
      asm volatile("s_waitcnt lgkmcnt(12)\n\t"
                   "s_mov_b64 exec, %0\n\tds_write2st64_b32 %4, %8, %12 offset0:%16 offset1:%17\n\t"
                   "s_mov_b64 exec, %1\n\tds_write2st64_b32 %5, %9, %13 offset0:%16 offset1:%17\n\t"
                   "s_mov_b64 exec, %2\n\tds_write2st64_b32 %6, %10, %14 offset0:%16 offset1:%17\n\t"
                   "s_mov_b64 exec, %3\n\tds_write2st64_b32 %7, %11, %15 offset0:%16 offset1:%17\n\t"
                   "s_mov_b64 exec, -1"
                   :: "s"(vm[4 * k]), "s"(vm[4 * k + 1]), "s"(vm[4 * k + 2]), "s"(vm[4 * k + 3]),
                      "v"(ad[4 * k]), "v"(ad[4 * k + 1]), "v"(ad[4 * k + 2]), "v"(ad[4 * k + 3]),
                      "v"(xe[4 * k]), "v"(xe[4 * k + 1]), "v"(xe[4 * k + 2]), "v"(xe[4 * k + 3]),
                      "v"(ee[0]), "v"(ee[1]), "v"(ee[2]), "v"(ee[3]), "n"(M_LX / 256), "n"(M_LE / 256)
                   : "memory");
    }
  }
    {
      const auto rw = SRD(wpack, NFRAG * 1024);
      const unsigned vo = (unsigned)(wave * 1024 + lane * 16);
      GLDS(vo + 16384u, rw, lb + M_SC + 8192 + wave * 1024);
      GLDS(vo, rw, lb + M_SC + wave * 1024);
      asm volatile("s_mov_b32 m0, %2\n\ts_nop 0\n\tbuffer_load_dword %0, %1, 0 offen lds"
                   :: "v"(24576u + (vo >> 2)), "s"(rw), "s"(lb + M_SC + 16384 + wave * 256) : "memory");
      GLDS(vo + 8192u, rw, lb + M_WL + wave * 1024);
      GLDS(vo + 26624u, rw, lb + M_WL + 8192 + wave * 1024);
    }
  asm volatile("s_waitcnt vmcnt(9)" : "+v"(ovh) :: "memory");
  if (__builtin_amdgcn_ballot_w64((ovh[1] & 0x100u) == 0u) != 0) {
#pragma unroll
    for (int q = 0; q < 2; ++q) {
      const unsigned w = ovh[2 * q], m = ovh[2 * q + 1];
      if ((m & 0x1FFu) == (unsigned)b) {
        LAS unsigned* p = (LAS unsigned*)((LAS char*)lxa + LDS_ADD((LAS int*)((LAS char*)cnt + ((w >> 9) & 0x70u)), 4));
        p[0] = w >> 9;
        p[GPB * LPAD] = (w & 0x1FFFu) | ((m & 0xF000u) << 9) | ((unsigned)(tid >> 1) << 13);
      }
    }
    if ((ovh[1] >> 16) > 4u) cnt[1] = 1;
  }
  asm volatile("s_waitcnt lgkmcnt(0)\n\ts_barrier" ::: "memory");
  const int cpc = cnt[4 * pc] - pc * (LPAD * 4);
  const int cpa = cnt[4 * (lane >> 3)] - (lane >> 3) * (LPAD * 4);
  bool ready = __builtin_amdgcn_ballot_w64(cpc > LCAP * 4) == 0;
  int grp;
  {
    const unsigned long long gt = __builtin_amdgcn_ballot_w64(cpc * 2 + pc > cpa * 2 + (lane >> 3));
    const unsigned wd = h ? (unsigned)(gt >> 32) : (unsigned)gt;
    const int rk = __builtin_popcount((wd >> (r & 24)) & 0xFFu);
    grp = __builtin_ctzll(__builtin_amdgcn_ballot_w64(rk == (wave < 4 ? wave : 11 - wave))) >> 3;
  }
  const int gn0 = b * NPB + grp;
  const int nodes = max((min(NPB - grp, N - gn0) + 7) >> 3, 0);
  const int n0 = min(gn0, N - 1);
  ((LAS unsigned*)(smem + M_ST))[tid] = (ready ? 0x40000u : 0xFFFFu) | ((unsigned)(tid & 1) << 16);

  f32x16 Y[4];
#pragma unroll
  for (int q = 0; q < 4; ++q)
#pragma unroll
    for (int i = 0; i < 16; ++i) Y[q][i] = 0.f;

  const unsigned ec0 = (unsigned)(((lane & 1) ^ ((lane >> 4) & 1)) * 16);
  const LAS unsigned* lx = lxa + grp * LPAD;
  const LAS unsigned* le = lea + grp * LPAD;
  const int ohoff = ((((r >> 2) & 1) * 16) + ((r >> 4) * 8) + (((r >> 3) & 1) * 4) + (r & 3)) * 2;
  const int xa0 = (r & 3) * 1024 + (r >> 2) * 128 + (((r & 3) | ((h ^ ((r >> 3) & 1)) << 2)) * 16);
  const int ea0 = 4096 + r * 32 + ((h ^ ((r >> 3) & 1)) * 16);
#define SB() __builtin_amdgcn_sched_barrier(0)
  bf16x8 wB[4][2], wC[2], XAf[2][2];
  const LAS bf16x8* wvl = wl + lane;
  f32x16 G0, G1, V;
  u32x4 pd[2], pn[2];
  bf16x8 S[2], St[2], as[4], ae, bv[4];
  const f32x16 zero16 = {0.f, 0.f, 0.f, 0.f, 0.f, 0.f, 0.f, 0.f, 0.f, 0.f, 0.f, 0.f, 0.f, 0.f, 0.f, 0.f};
  LAS char* sm = (LAS char*)(smem + M_SC + wave * SSCR);
  LAS u32x4* mine = (LAS u32x4*)(sm + r * 80 + h * 32);
  const LAS char* ring = (const LAS char*)wbase;
  unsigned ga[5];
  unsigned slr;
  int sl;
  int rs = 0;

#define E1(i) Gc[i] = __builtin_amdgcn_exp2f(Gc[i])
#define E2(i) Gc[i] = Gc[i] + 1.0f
#define E3(i) Gc[i] = __builtin_amdgcn_rcpf(Gc[i])
#define MU(i) V[i] = V[i] * Gc[i]
#define CD(j) pd[(j) >> 2][(j) & 3] = pack_bf16(Gc[2 * (j)], Gc[2 * (j) + 1])
#define CN(j) pn[(j) >> 2][(j) & 3] = pack_bf16(V[2 * (j)], V[2 * (j) + 1])
#define XG(k) ((xo[k] & ~127u) | (xc0 ^ (16u * (k))))
#define BC(v) __builtin_bit_cast(bf16x8, v)

  int more, ntiles;
  LAS int* pown = cnt + 4 * wave + 2;
  const LAS int* poth = cnt + 4 * (wave ^ 4) + 2;
  const LAS unsigned* lxp = lx + (lane >> 3) * 4;
  const LAS unsigned* lep = le + (lane >> 1);
  const LAS unsigned* lsp = le + r;
  auto round_begin = [&]() NOPK __attribute__((always_inline)) {
    const int cn = min((__builtin_amdgcn_readfirstlane(cnt[4 * grp]) - grp * (LPAD * 4)) >> 2, LCAP);
    more = __builtin_amdgcn_readfirstlane(cnt[1]);
    ntiles = (cn + 31) >> 5;
    {
      LAS unsigned* wx = lxa + grp * LPAD;
      LAS unsigned* we = lea + grp * LPAD;
      const int e0 = ntiles * 32;
      if (cn + r < e0) { wx[cn + r] = 0xFFFFFF80u; we[cn + r] = 0xFFFFFFFFu; }
      wx[e0 + lane] = lane < nodes ? (unsigned)(n0 + 8 * lane) << 7 : 0xFFFFFF80u;
      we[e0 + lane] = 0xFFFFFFFFu;
      wx[e0 + 64 + r] = 0xFFFFFF80u;
      we[e0 + 64 + r] = 0xFFFFFFFFu;
    }
  };
  auto gather = [&](int t, bool issue) NOPK __attribute__((always_inline)) {
    const u32x4 xo = *(const LAS u32x4*)(lxp + t * 32);
    const unsigned eo = lep[t * 32];
    ga[0] = XG(0); ga[1] = XG(1); ga[2] = XG(2); ga[3] = XG(3);
    ga[4] = (eo & ~31u) | ec0;
    if (issue) {
      const unsigned d = ring_a + (rs ^ (t * RING_SLOT));
      GLDS(ga[0], rx, d); GLDS(ga[1], rx, d + 1024); GLDS(ga[2], rx, d + 2048); GLDS(ga[3], rx, d + 3072);
      GLDS(ga[4], re, d + 4096);
    }
  };
  if (ready) {
    round_begin();
    if (wave < 4) gather(0, true);
  }
  asm volatile("s_cmp_lg_u32 %0, 0\n\ts_cbranch_scc1 1f\n\ts_waitcnt vmcnt(0)\n1:\n\ts_waitcnt vmcnt(5)\n\ts_barrier"
               :: "s"(__builtin_amdgcn_readfirstlane((ready && wave < 4) ? 1 : 0)) : "memory", "scc");
  {
      const LAS bf16x8* ws = (const LAS bf16x8*)(smem + M_SC) + lane;
      bf16x8 xg[4];
#pragma unroll
      for (int k = 0; k < 4; ++k) xg[k] = *(const LAS bf16x8*)(smem + M_XG + grp * 4096 + (xa0 ^ (16 * k)));
#pragma unroll
      for (int ct = 0; ct < 2; ++ct) {
        f32x16 A = zero16;
#pragma unroll
        for (int k = 0; k < 4; ++k) A = MFMA32(xg[k], ws[(8 + k * 2 + ct) * 64], A);
#pragma unroll
        for (int s = 0; s < 2; ++s) {
          u32x4 p;
#pragma unroll
          for (int j = 0; j < 4; ++j) p[j] = pack_bf16(A[8 * s + 2 * j] * NLOG2E, A[8 * s + 2 * j + 1] * NLOG2E);
          XAf[s][ct] = BC(p);
        }
      }
#pragma unroll
      for (int k = 0; k < 8; ++k) wB[k >> 1][k & 1] = ws[k * 64];
      wC[0] = ws[16 * 64];
      wC[1] = ws[17 * 64];
      asm volatile("s_waitcnt lgkmcnt(0)\n\ts_barrier"
                   : "+v"(wB[0][0]), "+v"(wB[0][1]), "+v"(wB[1][0]), "+v"(wB[1][1]), "+v"(wB[2][0]), "+v"(wB[2][1]),
                     "+v"(wB[3][0]), "+v"(wB[3][1]), "+v"(wC[0]), "+v"(wC[1]) :: "memory");
      const u32x4 z4 = {0u, 0u, 0u, 0u};
      mine[0] = z4;
      mine[1] = z4;
    }
  for (;;) {
   if (!ready) {
    asm volatile("s_waitcnt vmcnt(0)" ::: "memory");
    WGSYNC();
    {
      int t2 = (int)__builtin_amdgcn_mbcnt_hi(~0u, __builtin_amdgcn_mbcnt_lo(~0u, 0u));
      asm volatile("" : "+v"(t2));
      const int l2 = t2;
      t2 += wave * 64;
      if (t2 < 32) cnt[t2] = (t2 & 3) == 0 ? (t2 >> 2) * (LPAD * 4) : 0;
      WGSYNC();
      auto put = [&](unsigned t, unsigned e) NOPK -> bool {
        const unsigned gg = (t >> 4) & 7u;
        const int ad = LDS_ADD(&cnt[4 * gg], 4) >> 2;
        if (ad - (int)gg * LPAD >= LCAP) return false;
        lxa[ad] = t;
        lea[ad] = e;
        return true;
      };
      LAS unsigned* stp = (LAS unsigned*)(smem + M_ST) + t2;
      unsigned pe = *stp & 0xFFFFu, ocur = *stp >> 16;
#pragma unroll 1
      for (int j = 0; j < 16; ++j) {
        if ((pe >> j) & 1u) {
          const int cc = wave * 32 + (j >> 2) * 8 + (l2 >> 3);
          const unsigned* cp = TC + ((size_t)b * NCH + cc) * CELL;
          const int d = (l2 & 7) * 4 + (j & 3);
          const unsigned w = cp[d];
          const unsigned nib = (cp[(d & 16) + ((d >> 3) & 1)] >> (4 * (d & 7))) & 15u;
          if ((unsigned)((d & 15) - 2) >= (cp[d & 16] & 0xFFu) ||
              put(w >> 9, (w & 0x1FFFu) | (nib << 21) | ((unsigned)cc << 13))) pe &= ~(1u << j);
        }
      }
      const unsigned on = OVH[t2][1] >> 16;
      const size_t ob = (size_t)(t2 >> 1) * OVCAP;
#pragma unroll 1
      for (; ocur < on; ocur += 2) {
        const u32x4 v = OV[ob + ocur];
        if ((int)v[2] == b && !put(v[0], v[1])) break;
      }
      if (pe != 0u || ocur < on) cnt[1] = 1;
      *stp = pe | (ocur << 16);
    }
    WGSYNC();
    round_begin();
   }
    if (!ready || wave >= 4) gather(0, true);
    gather(1, true);
    gather(2, false);
    ready = false;
    sl = lsp[0] & 31u;
    St[0] = ohl[sl * 4 + h * 2 + 0];
    St[1] = ohl[sl * 4 + h * 2 + 1];
    bv[0] = wvl[0 * 64];
    bv[1] = wvl[2 * 64];
    asm volatile("s_waitcnt vmcnt(5)" ::: "memory");
#pragma unroll
    for (int k = 0; k < 4; ++k) as[k] = *(const LAS bf16x8*)(ring + rs + (xa0 ^ (16 * k)));
    ae = *(const LAS bf16x8*)(ring + rs + ea0);
    G0 = MFMA32(St[0], XAf[0][0], zero16);
    G0 = MFMA32(St[1], XAf[1][0], G0);
    G0 = MFMA32(ae, wC[0], G0);
#pragma unroll
    for (int k = 0; k < 4; ++k) G0 = MFMA32(as[k], wB[k][0], G0);
    pd[1] = u32x4{0u, 0u, 0u, 0u};
    pn[1] = u32x4{0u, 0u, 0u, 0u};
    S[1] = BC(pd[1]);
    SB();
    for (int i = 0; i < ntiles; ++i) {
      {
        f32x16& Gc = G0;
        const unsigned d = ring_a + rs;
        LAS unsigned short* ohw = (LAS unsigned short*)(sm + sl * 80 + ohoff);
        Y[1] = MFMA32(S[1], BC(pd[1]), Y[1]);
        bv[2] = wvl[4 * 64];
        *(volatile LAS int*)pown = ntiles - i;
        E1(0); E1(1); E1(2); E1(3);
        SB();
        Y[3] = MFMA32(S[1], BC(pn[1]), Y[3]);
        bv[3] = wvl[6 * 64];
        E2(0); E2(1); E1(4); E1(5); E2(2); E2(3);
        SB();
        V = MFMA32(as[0], bv[0], zero16);
        const int prem = *(volatile const LAS int*)poth;
        E3(0); E3(1); E1(6); E1(7); E2(4); E2(5);
        SB();
        V = MFMA32(as[1], bv[1], V);
        E3(2); E3(3); E1(8); E1(9); E2(6); E2(7);
        SB();
        V = MFMA32(as[2], bv[2], V);
        E3(4); E3(5); E1(10); E1(11); E2(8); E2(9); CD(0);
        SB();
        V = MFMA32(as[3], bv[3], V);
        E3(6); E3(7); E1(12); E1(13); E2(10); E2(11); CD(1);
        SB();
        G1 = MFMA32(St[0], XAf[0][1], zero16);
        asm volatile("s_cmp_ge_i32 %0, %1\n\ts_setprio 0\n\ts_cbranch_scc0 1f\n\ts_setprio 1\n1:"
                     :: "s"(ntiles - i), "s"(__builtin_amdgcn_readfirstlane(prem)) : "scc");
        E3(8); E3(9); E1(14); E1(15); E2(12); E2(13); CD(2);
        SB();
        G1 = MFMA32(St[1], XAf[1][1], G1);
        *ohw = (unsigned short)0x3F80;
        asm volatile("" ::: "memory");
        S[0] = BC(mine[0]);
        S[1] = BC(mine[1]);
        asm volatile("" ::: "memory");
        *ohw = (unsigned short)0;
        E3(10); E3(11); E2(14); E2(15); CD(3);
        SB();
        G1 = MFMA32(ae, wC[1], G1);
        GLDS(ga[0], rx, d);
        E3(12); E3(13); MU(0); MU(1); MU(2); MU(3);
        SB();
        G1 = MFMA32(as[0], wB[0][1], G1);
        GLDS(ga[1], rx, d + 1024);
        E3(14); E3(15); MU(4); MU(5); CN(0); CN(1);
        SB();
        G1 = MFMA32(as[1], wB[1][1], G1);
        GLDS(ga[2], rx, d + 2048);
        MU(6); MU(7); CD(4); CD(5); CN(2);
        SB();
        G1 = MFMA32(as[2], wB[2][1], G1);
        GLDS(ga[3], rx, d + 3072);
        MU(8); MU(9); MU(10); MU(11); CN(3); CD(6);
        SB();
        G1 = MFMA32(as[3], wB[3][1], G1);
        GLDS(ga[4], re, d + 4096);
        slr = lsp[(i + 1) * 32];
        MU(12); MU(13); CN(4); CN(5); CD(7);
        SB();
        Y[0] = MFMA32(S[0], BC(pd[0]), Y[0]);
        bv[0] = wvl[1 * 64];
        MU(14); MU(15); CN(6);
        SB();
        Y[2] = MFMA32(S[0], BC(pn[0]), Y[2]);
        bv[1] = wvl[3 * 64];
        CN(7);
        SB();
      }
      rs ^= RING_SLOT;
      {
        f32x16& Gc = G1;
        const LAS char* rp = ring + rs;
        sl = slr & 31u;
        Y[0] = MFMA32(S[1], BC(pd[1]), Y[0]);
        bv[2] = wvl[5 * 64];
        St[0] = ohl[sl * 4 + h * 2 + 0];
        E1(0); E1(1); E1(2); E1(3);
        SB();
        Y[2] = MFMA32(S[1], BC(pn[1]), Y[2]);
        bv[3] = wvl[7 * 64];
        St[1] = ohl[sl * 4 + h * 2 + 1];
        E2(0); E2(1); E1(4); E1(5); E2(2); E2(3);
        SB();
        V = MFMA32(as[0], bv[0], zero16);
        E3(0); E3(1); E1(6); E1(7); E2(4); E2(5);
        SB();
        V = MFMA32(as[1], bv[1], V);
        E3(2); E3(3); E1(8); E1(9); E2(6); E2(7);
        SB();
        V = MFMA32(as[2], bv[2], V);
        asm volatile("s_waitcnt vmcnt(5)" ::: "memory");
        ae = *(const LAS bf16x8*)(rp + ea0);
        E3(4); E3(5); E1(10); E1(11); E2(8); E2(9); CD(0);
        SB();
        V = MFMA32(as[3], bv[3], V);
        as[0] = *(const LAS bf16x8*)(rp + xa0);
        E3(6); E3(7); E1(12); E1(13); E2(10); E2(11); CD(1);
        SB();
        G0 = MFMA32(St[0], XAf[0][0], zero16);
        as[1] = *(const LAS bf16x8*)(rp + (xa0 ^ 16));
        E3(8); E3(9); E1(14); E1(15); E2(12); E2(13); CD(2);
        SB();
        G0 = MFMA32(St[1], XAf[1][0], G0);
        as[2] = *(const LAS bf16x8*)(rp + (xa0 ^ 32));
        E3(10); E3(11); E2(14); E2(15); CD(3);
        SB();
        G0 = MFMA32(ae, wC[0], G0);
        as[3] = *(const LAS bf16x8*)(rp + (xa0 ^ 48));
        E3(12); E3(13); MU(0); MU(1); MU(2); MU(3);
        SB();
        G0 = MFMA32(as[0], wB[0][0], G0);
        const u32x4 xo = *(const LAS u32x4*)(lxp + (i + 3) * 32);
        const unsigned eo = lep[(i + 3) * 32];
        E3(14); E3(15); MU(4); MU(5); CN(0); CN(1);
        SB();
        G0 = MFMA32(as[1], wB[1][0], G0);
        MU(6); MU(7); CD(4); CD(5); CN(2);
        SB();
        G0 = MFMA32(as[2], wB[2][0], G0);
        MU(8); MU(9); MU(10); MU(11); CN(3); CD(6);
        SB();
        G0 = MFMA32(as[3], wB[3][0], G0);
        ga[0] = XG(0); ga[1] = XG(1);
        MU(12); MU(13); CN(4); CN(5); CD(7);
        SB();
        Y[1] = MFMA32(S[0], BC(pd[0]), Y[1]);
        bv[0] = wvl[0 * 64];
        ga[2] = XG(2); ga[3] = XG(3);
        MU(14); MU(15); CN(6);
        SB();
        Y[3] = MFMA32(S[0], BC(pn[0]), Y[3]);
        bv[1] = wvl[2 * 64];
        ga[4] = (eo & ~31u) | ec0;
        CN(7);
        SB();
      }
    }
    *(volatile LAS int*)pown = 0;
    Y[1] = MFMA32(S[1], BC(pd[1]), Y[1]);
    Y[3] = MFMA32(S[1], BC(pn[1]), Y[3]);
    if (!more) break;
  }
  u32x4 xr[4];
#pragma unroll
  for (int q = 0; q < 4; ++q) {
    const int row = q * 8 + (lane >> 3);
    xr[q] = *(const LAS u32x4*)(ring + rs + (row & 3) * 1024 + (row >> 2) * 128 +
                                (((lane & 7) ^ (((row >> 3) & 1) << 2) ^ (row & 3)) * 16));
  }
  f32x16 U0 = zero16, U1 = zero16;
#pragma unroll
  for (int k = 0; k < 4; ++k) {
    U0 = MFMA32(as[k], wvl[(8 + k * 2) * 64], U0);
    U1 = MFMA32(as[k], wvl[(9 + k * 2) * 64], U1);
  }
  asm volatile("s_waitcnt vmcnt(0) lgkmcnt(0)" : "+v"(xr[0]), "+v"(xr[1]), "+v"(xr[2]), "+v"(xr[3]) :: "memory");
  LAS float* zl = (LAS float*)wbase;
#pragma unroll
  for (int i = 0; i < 16; ++i) {
    const int row = (i & 3) + 8 * (i >> 2) + 4 * h;
    const float z0 = U0[i] + Y[2][i] * __builtin_amdgcn_rcpf(Y[0][i] + 1e-6f);
    const float z1 = U1[i] + Y[3][i] * __builtin_amdgcn_rcpf(Y[1][i] + 1e-6f);
    zl[row * 68 + r] = z0 > 0.f ? z0 : 0.f;
    zl[row * 68 + 32 + r] = z1 > 0.f ? z1 : 0.f;
  }
#pragma unroll
  for (int q = 0; q < 4; ++q) {
    const int row = q * 8 + (lane >> 3);
    const f32x4 z0 = *(const LAS f32x4*)(&zl[row * 68 + (lane & 7) * 8]);
    const f32x4 z1 = *(const LAS f32x4*)(&zl[row * 68 + (lane & 7) * 8 + 4]);
    u32x4 o;
    o[0] = pack_bf16(U2F(xr[q][0] << 16) + z0[0], U2F(xr[q][0] & 0xffff0000u) + z0[1]);
    o[1] = pack_bf16(U2F(xr[q][1] << 16) + z0[2], U2F(xr[q][1] & 0xffff0000u) + z0[3]);
    o[2] = pack_bf16(U2F(xr[q][2] << 16) + z1[0], U2F(xr[q][2] & 0xffff0000u) + z1[1]);
    o[3] = pack_bf16(U2F(xr[q][3] << 16) + z1[2], U2F(xr[q][3] & 0xffff0000u) + z1[3]);
    if (row < nodes) *reinterpret_cast<u32x4*>(out + (size_t)(n0 + 8 * row) * 64 + (lane & 7) * 8) = o;
  }
}

extern "C" void kernel_launch(void* const* d_in, const int* in_sizes, int n_in,
                              void* d_out, int out_size, void* d_ws, size_t ws_size,
                              hipStream_t stream) {
  const unsigned short* x = (const unsigned short*)d_in[0];
  const unsigned short* eattr = (const unsigned short*)d_in[1];
  const int* src = (const int*)d_in[2];
  const int* dst = (const int*)d_in[3];
  const unsigned short* wa = (const unsigned short*)d_in[4];
  const unsigned short* wb = (const unsigned short*)d_in[5];
  const unsigned short* wc = (const unsigned short*)d_in[6];
  const unsigned short* wu = (const unsigned short*)d_in[7];
  const unsigned short* wv = (const unsigned short*)d_in[8];
  unsigned short* out = (unsigned short*)d_out;
  const int N = in_sizes[0] / 64;
  const int E = in_sizes[2];
  const int nb = (N + NPB - 1) / NPB;

  char* ws = (char*)d_ws;
  k_part<<<NCH, 1024, 0, stream>>>(src, dst, E, nb, (unsigned*)(ws + W_TC), (u32x4*)(ws + W_OVH), (u32x4*)(ws + W_OV),
                                   wa, wb, wc, wu, wv, (unsigned short*)(ws + W_WP));
  k_main<<<nb, 512, 0, stream>>>(x, eattr, ws, E, N, out);
}
```

```cpp
#include <hip/hip_runtime.h>
#include <hip/hip_bf16.h>
#include <stdint.h>

using bf16x8 = __attribute__((ext_vector_type(8))) short;
using f32x16 = __attribute__((ext_vector_type(16))) float;
using u32x4 = __attribute__((ext_vector_type(4))) unsigned;
using u32x2 = __attribute__((ext_vector_type(2))) unsigned;
using i32x4 = __attribute__((ext_vector_type(4))) int;
using f32x4 = __attribute__((ext_vector_type(4))) float;

#define MFMA32(a, b, c) __builtin_amdgcn_mfma_f32_32x32x16_bf16((a), (b), (c), 0, 0, 0)
#define LAS __attribute__((address_space(3)))
#if defined(__HIP_DEVICE_COMPILE__)
#define NOPK __attribute__((target("no-packed-fp32-ops")))
#else
#define NOPK
#endif
#define SRD(p, bytes) __builtin_amdgcn_make_buffer_rsrc((void*)(p), (short)0, (int)(bytes), 0x00020000)
#define LDS_ADD(p, v) __hip_atomic_fetch_add((p), (v), __ATOMIC_RELAXED, __HIP_MEMORY_SCOPE_WORKGROUP)

#define CST(p, v) __builtin_amdgcn_raw_buffer_store_b128((v), rc, (p) * 4, 0, 17)
#define WALL 1
constexpr int GPB = 8;
constexpr int NPB = 196;
constexpr int MAXB = 256;
constexpr int NCH = 256;
constexpr int CELL = 32;
constexpr int CAP = 28;
constexpr int CSTR = 36;
constexpr int OVCAP = 4096;
constexpr int NFRAG = 34;
constexpr size_t W_TC = 0;
constexpr size_t W_OVH = W_TC + (size_t)MAXB * NCH * CELL * 4;
constexpr size_t W_WP = W_OVH + NCH * 32;
constexpr size_t W_OV = W_WP + NFRAG * 1024;
constexpr float NLOG2E = -1.44269504088896340736f;

__global__ __launch_bounds__(1024) void k_part(const int* __restrict__ src, const int* __restrict__ dst,
                                               int E, int nb,
                                               unsigned* __restrict__ TC,
                                               u32x4* __restrict__ OVH, u32x4* __restrict__ OV,
                                               const unsigned short* __restrict__ wa,
                                               const unsigned short* __restrict__ wb,
                                               const unsigned short* __restrict__ wc,
                                               const unsigned short* __restrict__ wu,
                                               const unsigned short* __restrict__ wv,
                                               unsigned short* __restrict__ wpack) {
  __shared__ __attribute__((aligned(16))) unsigned cT[(MAXB + 1) * CSTR];
  __shared__ __attribute__((aligned(16))) unsigned ovl[8];
  __shared__ int cnt[MAXB + 4];
  const int tid = threadIdx.x, lane = tid & 63;
  const int rnd = __builtin_amdgcn_readfirstlane(tid >> 6);
  const int c = blockIdx.x;
  const int e0 = (rnd * NCH + c) * 256 + lane * 4;
  const auto rd = SRD(dst, E * 4), rs = SRD(src, E * 4);

  const i32x4 dd = __builtin_bit_cast(i32x4, __builtin_amdgcn_raw_buffer_load_b128(rd, e0 * 4, 0, 0));
  const i32x4 ss = __builtin_bit_cast(i32x4, __builtin_amdgcn_raw_buffer_load_b128(rs, e0 * 4, 0, 0));
  unsigned wv8[8];
  if (rnd == 15 && c < NFRAG) {
    const int nn = lane & 31, h = lane >> 5;
    const unsigned short* p;
    if (c < 24) {
      const int m = c >> 3, t = (c >> 1) & 3, ct = c & 1;
      p = ((m == 0) ? wb : (m == 1) ? wv : wa) + (32 * h + 8 * t) * 64 + 32 * ct + nn;
    } else if (c < 26) {
      p = wc + (8 * h) * 64 + 32 * (c & 1) + nn;
    } else {
      const int g = c - 26, t = (g >> 1) & 3, ct = g & 1;
      p = wu + (32 * h + 8 * t) * 64 + 32 * ct + nn;
    }
#pragma unroll
    for (int j = 0; j < 8; ++j) wv8[j] = p[j * 64];
  }
  if (tid < 2 * MAXB) *reinterpret_cast<u32x2*>(cT + (tid >> 1) * CSTR + (tid & 1) * 16) = u32x2{0u, 0u};
  else if (tid < 3 * MAXB + 4) cnt[tid - 2 * MAXB] = 0;
  else if (tid < 3 * MAXB + 12) ovl[tid - (3 * MAXB + 4)] = 0x100u;
  __syncthreads();
  if ((rnd * NCH + c) * 256 < E) {
    int bk[4], rk[4];
#pragma unroll
    for (int k = 0; k < 4; ++k) {
      const bool v = e0 + k < E;
      bk[k] = v ? (int)((unsigned)dd[k] / (unsigned)NPB) : MAXB + 1;
      rk[k] = LDS_ADD(&cnt[bk[k]], 1);
    }
    unsigned w[4];
    bool ovf = false;
#pragma unroll
    for (int k = 0; k < 4; ++k) {
      const unsigned s = (unsigned)(dd[k] - bk[k] * NPB);
      w[k] = ((unsigned)ss[k] << 16) | ((s & 7u) << 13) | ((unsigned)(lane * 4 + k) << 5) | (s >> 3);
      const int d = (rk[k] + (rk[k] < CAP / 2 ? 2 : 4)) & 31;
      unsigned* cp = cT + (rk[k] < CAP ? min(bk[k], MAXB) : MAXB) * CSTR;
      cp[d] = w[k];
      __hip_atomic_fetch_or(cp + (d & 16) + ((d >> 3) & 1), (unsigned)rnd << (4 * (d & 7)),
                            __ATOMIC_RELAXED, __HIP_MEMORY_SCOPE_WORKGROUP);
      ovf |= rk[k] >= CAP;
    }
    if (__builtin_amdgcn_ballot_w64(ovf) != 0) {
#pragma unroll 1
      for (int k = 0; k < 4; ++k) {
        const int bq = k == 0 ? bk[0] : k == 1 ? bk[1] : k == 2 ? bk[2] : bk[3];
        const int rq = k == 0 ? rk[0] : k == 1 ? rk[1] : k == 2 ? rk[2] : rk[3];
        const unsigned wq = k == 0 ? w[0] : k == 1 ? w[1] : k == 2 ? w[2] : w[3];
        if (bq < MAXB && rq >= CAP) {
          const int o = LDS_ADD(&cnt[MAXB], 1);
          OV[c * OVCAP + o] = u32x4{wq >> 9, ((unsigned)(e0 + k) << 5) | (wq & 31u), (unsigned)bq, 0u};
          if (o < 4) { ovl[2 * o] = wq; ovl[2 * o + 1] = ((unsigned)rnd << 12) | (unsigned)bq; }
        }
      }
    }
  }
  asm volatile("s_waitcnt lgkmcnt(0)\n\ts_barrier" ::: "memory");
  {
    const auto rc = SRD(TC, MAXB * NCH * CELL * 4);
    const int p = tid & 7;
    const int first = (CAP / 2) * (p >> 2) + 4 * (p & 3) - 2;
#pragma unroll
    for (int q = 0; q < 2; ++q) {
      const int b = (tid >> 3) + q * 128;
      const int m = cnt[b];
      u32x4 v = *reinterpret_cast<const u32x4*>(cT + b * CSTR + 4 * p);
      if ((p & 3) == 0) v[0] |= (unsigned)min(max(m - first - 2, 0), CAP / 2);
      if (b < nb && (WALL || (p & 3) == 0 || first < m)) CST((b * NCH + c) * CELL + 4 * p, v);
    }
    if (tid < 2) {
      u32x4 v = *reinterpret_cast<const u32x4*>(ovl + 4 * tid);
      v[1] |= (unsigned)cnt[MAXB] << 16;
      v[3] |= (unsigned)cnt[MAXB] << 16;
      __builtin_amdgcn_raw_buffer_store_b128(v, SRD(OVH, NCH * 32), c * 32 + tid * 16, 0, 17);
    }
  }
  if (rnd == 15 && c < NFRAG) {
    if (c < 8 || c == 24 || c == 25) {
#pragma unroll
      for (int j = 0; j < 8; ++j) {
        const unsigned u = __float_as_uint(__uint_as_float(wv8[j] << 16) * NLOG2E);
        wv8[j] = (u + 0x7FFFu + ((u >> 16) & 1u)) >> 16;
      }
    }
    const u32x4 v = {wv8[0] | (wv8[1] << 16), wv8[2] | (wv8[3] << 16), wv8[4] | (wv8[5] << 16), wv8[6] | (wv8[7] << 16)};
    __builtin_amdgcn_raw_buffer_store_b128(v, SRD(wpack, NFRAG * 1024), c * 1024 + lane * 16, 0, 17);
  }
}

__device__ __forceinline__ NOPK unsigned pack_bf16(float lo, float hi) {
  typedef __attribute__((ext_vector_type(2))) float f2;
  typedef __attribute__((ext_vector_type(2))) __bf16 b2;
  f2 v = {lo, hi};
  b2 r = __builtin_convertvector(v, b2);
  return __builtin_bit_cast(unsigned, r);
}
#define WGSYNC() do { __builtin_amdgcn_fence(__ATOMIC_RELEASE, "workgroup"); __builtin_amdgcn_s_barrier(); \
                      __builtin_amdgcn_fence(__ATOMIC_ACQUIRE, "workgroup"); } while (0)
#define U2F(u) __builtin_bit_cast(float, (unsigned)(u))
constexpr int RING_SLOT = 5 * 1024;
constexpr int LCAP = 512;
constexpr int LPAD = LCAP + 96;
constexpr int SSCR = 32 * 80;
constexpr int WAVE_LDS = 2 * RING_SLOT;
constexpr int M_WL = 0;
constexpr int M_OH = 16 * 1024;
constexpr int M_CNT = M_OH + 2048;
constexpr int M_ST = M_CNT + 256;
constexpr int M_SC = M_ST + 2048;
constexpr int M_LX = M_SC + GPB * SSCR;
constexpr int M_LE = M_LX + GPB * LPAD * 4;
constexpr int M_WV = M_LE + GPB * LPAD * 4;
constexpr int M_XG = M_WV + 4 * WAVE_LDS;
constexpr int MAIN_LDS = M_WV + GPB * WAVE_LDS;
static_assert(M_LX % 256 == 0 && M_LE % 256 == 0 && M_LE < 65536 && M_LE + GPB * LPAD * 4 + NCH * CELL * 4 <= M_XG, "lists");
static_assert(MAIN_LDS <= 160 * 1024, "LDS");

__global__ __launch_bounds__(512, 2) NOPK void k_main(const unsigned short* __restrict__ x,
                                                      const unsigned short* __restrict__ eattr,
                                                      const char* __restrict__ wsp, int E,
                                                      int N, unsigned short* __restrict__ out) {
  __shared__ __attribute__((aligned(16))) char smem[MAIN_LDS];
  asm volatile("" :: "s"(x), "s"(eattr), "s"(wsp), "s"(E), "s"(N), "s"(out));
  const unsigned* TC = reinterpret_cast<const unsigned*>(wsp + W_TC);
  const u32x4* OVH = reinterpret_cast<const u32x4*>(wsp + W_OVH);
  const u32x4* OV = reinterpret_cast<const u32x4*>(wsp + W_OV);
  const char* wpack = wsp + W_WP;
  const int tid = threadIdx.x, lane = tid & 63;
  const int wave = __builtin_amdgcn_readfirstlane(tid >> 6);
  const int r = lane & 31, h = lane >> 5;
  const int b = blockIdx.x;

  const int pc = lane & 7;
  u32x4 tv[4], ovh;
  {
    const unsigned* cp = TC + ((size_t)b * NCH + wave * 32) * CELL;
    asm volatile("global_load_dwordx4 %0, %5, %6\n\tglobal_load_dwordx4 %1, %5, %6 offset:1024\n\t"
                 "global_load_dwordx4 %2, %5, %6 offset:2048\n\tglobal_load_dwordx4 %3, %5, %6 offset:3072\n\t"
                 "global_load_dwordx4 %4, %5, %7"
                 : "=&v"(tv[0]), "=&v"(tv[1]), "=&v"(tv[2]), "=&v"(tv[3]), "=&v"(ovh)
                 : "v"(lane * 16), "s"(cp), "s"(OVH + wave * 64) : "memory");
  }
#define GLDS(vo, rs, mv) asm volatile("s_mov_b32 m0, %2\n\ts_nop 0\n\tbuffer_load_dwordx4 %0, %1, 0 offen lds" \
                                      :: "v"(vo), "s"(rs), "s"(mv) : "memory")
  const auto rx = SRD(x, N * 128);
  const auto re = SRD(eattr, E * 32);
  const unsigned xc0 = (unsigned)(((lane & 7) ^ ((lane >> 2) & 4)) * 16);
  const unsigned lb = (unsigned)(size_t)(LAS char*)smem;
  LAS int* cnt = (LAS int*)(smem + M_CNT);
  if (tid < 32) cnt[tid] = (tid & 3) == 0 ? (tid >> 2) * (LPAD * 4) : (tid & 3) == 2 ? 127 : 0;
  __builtin_amdgcn_s_setprio(1);
  {
    unsigned* oh = reinterpret_cast<unsigned*>(smem + M_OH);
    const int i = tid;
    const int sl = i >> 4, hh = (i >> 3) & 1, ks = (i >> 2) & 1, jp = i & 3;
    const int j0 = 2 * jp, j1 = 2 * jp + 1;
    const int nd0 = 16 * ks + 8 * (j0 >> 2) + 4 * hh + (j0 & 3);
    const int nd1 = 16 * ks + 8 * (j1 >> 2) + 4 * hh + (j1 & 3);
    oh[i] = (sl == nd0 ? 0x3F80u : 0u) | (sl == nd1 ? 0x3F800000u : 0u);
  }
  asm volatile("s_waitcnt lgkmcnt(0)\n\ts_barrier" ::: "memory");
  {
    const int nw = (min(NPB - wave, N - b * NPB - wave) + 7) >> 3;
#pragma unroll
    for (int k = 0; k < 4; ++k) {
      const int s = (lane >> 3) * 4 + k;
      const unsigned xo = s < nw ? (unsigned)(b * NPB + wave + 8 * s) << 7 : 0xFFFFFF80u;
      GLDS(xo | (xc0 ^ (16u * k)), rx, lb + M_XG + wave * 4096 + k * 1024);
    }
  }
  const LAS bf16x8* wl = (const LAS bf16x8*)(smem + M_WL);
  const LAS bf16x8* ohl = (const LAS bf16x8*)(smem + M_OH);
  LAS unsigned* lxa = (LAS unsigned*)(smem + M_LX);
  LAS unsigned* lea = (LAS unsigned*)(smem + M_LE);
  char* wbase = smem + M_WV + wave * WAVE_LDS;
  const unsigned ring_a = (unsigned)(size_t)(LAS char*)wbase;
  {
    unsigned long long vm[16];
    unsigned ad[16], xe[16], hw[4];
    const unsigned four = 4u;
#pragma unroll
    for (int k = 0; k < 4; ++k) {
      asm volatile("s_waitcnt vmcnt(%1)" : "+v"(tv[k]) : "n"(8 - k) : "memory");
      const unsigned h0 = (unsigned)__builtin_amdgcn_mov_dpp((int)tv[k][0], 0x00, 0xF, 0xF, true);
      const unsigned h1 = (unsigned)__builtin_amdgcn_mov_dpp((int)tv[k][1], 0x00, 0xF, 0xF, true);
      hw[k] = ((lane & 2) ? h1 : h0) >> ((lane & 1) * 16);
      const unsigned m = h0 & 0xFFu;
      unsigned ci[4];
#pragma unroll
      for (int j = 0; j < 4; ++j) {
        vm[4 * k + j] = __builtin_amdgcn_ballot_w64((unsigned)((lane & 3) * 4 + j - 2) < m);
        xe[4 * k + j] = tv[k][j] >> 9;
        ci[j] = (xe[4 * k + j] & 0x70u) + lb;
      }
      asm volatile("s_mov_b64 exec, %4\n\tds_add_rtn_u32 %0, %8, %12 offset:%13\n\t"
                   "s_mov_b64 exec, %5\n\tds_add_rtn_u32 %1, %9, %12 offset:%13\n\t"
                   "s_mov_b64 exec, %6\n\tds_add_rtn_u32 %2, %10, %12 offset:%13\n\t"
                   "s_mov_b64 exec, %7\n\tds_add_rtn_u32 %3, %11, %12 offset:%13\n\t"
                   "s_mov_b64 exec, -1"
                   : "=&v"(ad[4 * k]), "=&v"(ad[4 * k + 1]), "=&v"(ad[4 * k + 2]), "=&v"(ad[4 * k + 3])
                   : "s"(vm[4 * k]), "s"(vm[4 * k + 1]), "s"(vm[4 * k + 2]), "s"(vm[4 * k + 3]),
                     "v"(ci[0]), "v"(ci[1]), "v"(ci[2]), "v"(ci[3]), "v"(four), "n"(M_CNT)
                   : "memory");
    }
#pragma unroll
    for (int k = 0; k < 4; ++k) {
      const unsigned c13 = (unsigned)(wave * 32 + k * 8 + (lane >> 3)) << 13;
      unsigned ee[4];
#pragma unroll
      for (int j = 0; j < 4; ++j) ee[j] = (tv[k][j] & 0x1FFFu) | ((((hw[k] >> (4 * j)) & 15u) << 21) | c13);
      asm volatile("s_waitcnt lgkmcnt(12)\n\t"
                   "s_mov_b64 exec, %0\n\tds_write2st64_b32 %4, %8, %12 offset0:%16 offset1:%17\n\t"
                   "s_mov_b64 exec, %1\n\tds_write2st64_b32 %5, %9, %13 offset0:%16 offset1:%17\n\t"
                   "s_mov_b64 exec, %2\n\tds_write2st64_b32 %6, %10, %14 offset0:%16 offset1:%17\n\t"
                   "s_mov_b64 exec, %3\n\tds_write2st64_b32 %7, %11, %15 offset0:%16 offset1:%17\n\t"
                   "s_mov_b64 exec, -1"
                   :: "s"(vm[4 * k]), "s"(vm[4 * k + 1]), "s"(vm[4 * k + 2]), "s"(vm[4 * k + 3]),
                      "v"(ad[4 * k]), "v"(ad[4 * k + 1]), "v"(ad[4 * k + 2]), "v"(ad[4 * k + 3]),
                      "v"(xe[4 * k]), "v"(xe[4 * k + 1]), "v"(xe[4 * k + 2]), "v"(xe[4 * k + 3]),
                      "v"(ee[0]), "v"(ee[1]), "v"(ee[2]), "v"(ee[3]), "n"(M_LX / 256), "n"(M_LE / 256)
                   : "memory");
    }
  }
    {
      const auto rw = SRD(wpack, NFRAG * 1024);
      const unsigned vo = (unsigned)(wave * 1024 + lane * 16);
      GLDS(vo + 16384u, rw, lb + M_SC + 8192 + wave * 1024);
      GLDS(vo, rw, lb + M_SC + wave * 1024);
      asm volatile("s_mov_b32 m0, %2\n\ts_nop 0\n\tbuffer_load_dword %0, %1, 0 offen lds"
                   :: "v"(24576u + (vo >> 2)), "s"(rw), "s"(lb + M_SC + 16384 + wave * 256) : "memory");
      GLDS(vo + 8192u, rw, lb + M_WL + wave * 1024);
      GLDS(vo + 26624u, rw, lb + M_WL + 8192 + wave * 1024);
    }
  asm volatile("s_waitcnt vmcnt(9)" : "+v"(ovh) :: "memory");
  if (__builtin_amdgcn_ballot_w64((ovh[1] & 0x100u) == 0u) != 0) {
#pragma unroll
    for (int q = 0; q < 2; ++q) {
      const unsigned w = ovh[2 * q], m = ovh[2 * q + 1];
      if ((m & 0x1FFu) == (unsigned)b) {
        LAS unsigned* p = (LAS unsigned*)((LAS char*)lxa + LDS_ADD((LAS int*)((LAS char*)cnt + ((w >> 9) & 0x70u)), 4));
        p[0] = w >> 9;
        p[GPB * LPAD] = (w & 0x1FFFu) | ((m & 0xF000u) << 9) | ((unsigned)(tid >> 1) << 13);
      }
    }
    if ((ovh[1] >> 16) > 4u) cnt[1] = 1;
  }
  asm volatile("s_waitcnt lgkmcnt(0)\n\ts_barrier" ::: "memory");
  const int cpc = cnt[4 * pc] - pc * (LPAD * 4);
  const int cpa = cnt[4 * (lane >> 3)] - (lane >> 3) * (LPAD * 4);
  bool ready = __builtin_amdgcn_ballot_w64(cpc > LCAP * 4) == 0;
  int grp;
  {
    const unsigned long long gt = __builtin_amdgcn_ballot_w64(cpc * 2 + pc > cpa * 2 + (lane >> 3));
    const unsigned wd = h ? (unsigned)(gt >> 32) : (unsigned)gt;
    const int rk = __builtin_popcount((wd >> (r & 24)) & 0xFFu);
    grp = __builtin_ctzll(__builtin_amdgcn_ballot_w64(rk == (wave < 4 ? wave : 11 - wave))) >> 3;
  }
  const int gn0 = b * NPB + grp;
  const int nodes = max((min(NPB - grp, N - gn0) + 7) >> 3, 0);
  const int n0 = min(gn0, N - 1);
  ((LAS unsigned*)(smem + M_ST))[tid] = (ready ? 0x40000u : 0xFFFFu) | ((unsigned)(tid & 1) << 16);

  f32x16 Y[4];
#pragma unroll
  for (int q = 0; q < 4; ++q)
#pragma unroll
    for (int i = 0; i < 16; ++i) Y[q][i] = 0.f;

  const unsigned ec0 = (unsigned)(((lane & 1) ^ ((lane >> 4) & 1)) * 16);
  const LAS unsigned* lx = lxa + grp * LPAD;
  const LAS unsigned* le = lea + grp * LPAD;
  const int ohoff = ((((r >> 2) & 1) * 16) + ((r >> 4) * 8) + (((r >> 3) & 1) * 4) + (r & 3)) * 2;
  const int xa0 = (r & 3) * 1024 + (r >> 2) * 128 + (((r & 3) | ((h ^ ((r >> 3) & 1)) << 2)) * 16);
  const int ea0 = 4096 + r * 32 + ((h ^ ((r >> 3) & 1)) * 16);
#define SB() __builtin_amdgcn_sched_barrier(0)
  bf16x8 wB[4][2], wC[2], XAf[2][2];
  const LAS bf16x8* wvl = wl + lane;
  f32x16 G0, G1, V;
  u32x4 pd[2], pn[2];
  bf16x8 S[2], St[2], as[4], ae, bv[4];
  const f32x16 zero16 = {0.f, 0.f, 0.f, 0.f, 0.f, 0.f, 0.f, 0.f, 0.f, 0.f, 0.f, 0.f, 0.f, 0.f, 0.f, 0.f};
  LAS char* sm = (LAS char*)(smem + M_SC + wave * SSCR);
  LAS u32x4* mine = (LAS u32x4*)(sm + r * 80 + h * 32);
  const LAS char* ring = (const LAS char*)wbase;
  unsigned ga[5];
  unsigned slr;
  int sl;
  int rs = 0;

#define E1(i) Gc[i] = __builtin_amdgcn_exp2f(Gc[i])
#define E2(i) Gc[i] = Gc[i] + 1.0f
#define E3(i) Gc[i] = __builtin_amdgcn_rcpf(Gc[i])
#define MU(i) V[i] = V[i] * Gc[i]
#define CD(j) pd[(j) >> 2][(j) & 3] = pack_bf16(Gc[2 * (j)], Gc[2 * (j) + 1])
#define CN(j) pn[(j) >> 2][(j) & 3] = pack_bf16(V[2 * (j)], V[2 * (j) + 1])
#define XG(k) ((xo[k] & ~127u) | (xc0 ^ (16u * (k))))
#define BC(v) __builtin_bit_cast(bf16x8, v)

  int more, ntiles;
  LAS int* pown = cnt + 4 * wave + 2;
  const LAS int* poth = cnt + 4 * (wave ^ 4) + 2;
  const LAS unsigned* lxp = lx + (lane >> 3) * 4;
  const LAS unsigned* lep = le + (lane >> 1);
  const LAS unsigned* lsp = le + r;
  auto round_begin = [&]() NOPK __attribute__((always_inline)) {
    const int cn = min((__builtin_amdgcn_readfirstlane(cnt[4 * grp]) - grp * (LPAD * 4)) >> 2, LCAP);
    more = __builtin_amdgcn_readfirstlane(cnt[1]);
    ntiles = (cn + 31) >> 5;
    {
      LAS unsigned* wx = lxa + grp * LPAD;
      LAS unsigned* we = lea + grp * LPAD;
      const int e0 = ntiles * 32;
      if (cn + r < e0) { wx[cn + r] = 0xFFFFFF80u; we[cn + r] = 0xFFFFFFFFu; }
      wx[e0 + lane] = lane < nodes ? (unsigned)(n0 + 8 * lane) << 7 : 0xFFFFFF80u;
      we[e0 + lane] = 0xFFFFFFFFu;
      wx[e0 + 64 + r] = 0xFFFFFF80u;
      we[e0 + 64 + r] = 0xFFFFFFFFu;
    }
  };
  auto gather = [&](int t, bool issue) NOPK __attribute__((always_inline)) {
    const u32x4 xo = *(const LAS u32x4*)(lxp + t * 32);
    const unsigned eo = lep[t * 32];
    ga[0] = XG(0); ga[1] = XG(1); ga[2] = XG(2); ga[3] = XG(3);
    ga[4] = (eo & ~31u) | ec0;
    if (issue) {
      const unsigned d = ring_a + (rs ^ (t * RING_SLOT));
      GLDS(ga[0], rx, d); GLDS(ga[1], rx, d + 1024); GLDS(ga[2], rx, d + 2048); GLDS(ga[3], rx, d + 3072);
      GLDS(ga[4], re, d + 4096);
    }
  };
  if (ready) {
    round_begin();
    if (wave < 4) gather(0, true);
  }
  asm volatile("s_cmp_lg_u32 %0, 0\n\ts_cbranch_scc1 1f\n\ts_waitcnt vmcnt(0)\n1:\n\ts_waitcnt vmcnt(5)\n\ts_barrier"
               :: "s"(__builtin_amdgcn_readfirstlane((ready && wave < 4) ? 1 : 0)) : "memory", "scc");
  {
      const LAS bf16x8* ws = (const LAS bf16x8*)(smem + M_SC) + lane;
      bf16x8 xg[4];
#pragma unroll
      for (int k = 0; k < 4; ++k) xg[k] = *(const LAS bf16x8*)(smem + M_XG + grp * 4096 + (xa0 ^ (16 * k)));
#pragma unroll
      for (int ct = 0; ct < 2; ++ct) {
        f32x16 A = zero16;
#pragma unroll
        for (int k = 0; k < 4; ++k) A = MFMA32(xg[k], ws[(8 + k * 2 + ct) * 64], A);
#pragma unroll
        for (int s = 0; s < 2; ++s) {
          u32x4 p;
#pragma unroll
          for (int j = 0; j < 4; ++j) p[j] = pack_bf16(A[8 * s + 2 * j] * NLOG2E, A[8 * s + 2 * j + 1] * NLOG2E);
          XAf[s][ct] = BC(p);
        }
      }
#pragma unroll
      for (int k = 0; k < 8; ++k) wB[k >> 1][k & 1] = ws[k * 64];
      wC[0] = ws[16 * 64];
      wC[1] = ws[17 * 64];
      asm volatile("s_waitcnt lgkmcnt(0)\n\ts_barrier"
                   : "+v"(wB[0][0]), "+v"(wB[0][1]), "+v"(wB[1][0]), "+v"(wB[1][1]), "+v"(wB[2][0]), "+v"(wB[2][1]),
                     "+v"(wB[3][0]), "+v"(wB[3][1]), "+v"(wC[0]), "+v"(wC[1]) :: "memory");
      const u32x4 z4 = {0u, 0u, 0u, 0u};
      mine[0] = z4;
      mine[1] = z4;
    }
  for (;;) {
   if (!ready) {
    asm volatile("s_waitcnt vmcnt(0)" ::: "memory");
    WGSYNC();
    {
      int t2 = (int)__builtin_amdgcn_mbcnt_hi(~0u, __builtin_amdgcn_mbcnt_lo(~0u, 0u));
      asm volatile("" : "+v"(t2));
      const int l2 = t2;
      t2 += wave * 64;
      if (t2 < 32) cnt[t2] = (t2 & 3) == 0 ? (t2 >> 2) * (LPAD * 4) : 0;
      WGSYNC();
      auto put = [&](unsigned t, unsigned e) NOPK -> bool {
        const unsigned gg = (t >> 4) & 7u;
        const int ad = LDS_ADD(&cnt[4 * gg], 4) >> 2;
        if (ad - (int)gg * LPAD >= LCAP) return false;
        lxa[ad] = t;
        lea[ad] = e;
        return true;
      };
      LAS unsigned* stp = (LAS unsigned*)(smem + M_ST) + t2;
      unsigned pe = *stp & 0xFFFFu, ocur = *stp >> 16;
#pragma unroll 1
      for (int j = 0; j < 16; ++j) {
        if ((pe >> j) & 1u) {
          const int cc = wave * 32 + (j >> 2) * 8 + (l2 >> 3);
          const unsigned* cp = TC + ((size_t)b * NCH + cc) * CELL;
          const int d = (l2 & 7) * 4 + (j & 3);
          const unsigned w = cp[d];
          const unsigned nib = (cp[(d & 16) + ((d >> 3) & 1)] >> (4 * (d & 7))) & 15u;
          if ((unsigned)((d & 15) - 2) >= (cp[d & 16] & 0xFFu) ||
              put(w >> 9, (w & 0x1FFFu) | (nib << 21) | ((unsigned)cc << 13))) pe &= ~(1u << j);
        }
      }
      const unsigned on = OVH[t2][1] >> 16;
      const size_t ob = (size_t)(t2 >> 1) * OVCAP;
#pragma unroll 1
      for (; ocur < on; ocur += 2) {
        const u32x4 v = OV[ob + ocur];
        if ((int)v[2] == b && !put(v[0], v[1])) break;
      }
      if (pe != 0u || ocur < on) cnt[1] = 1;
      *stp = pe | (ocur << 16);
    }
    WGSYNC();
    round_begin();
   }
    if (!ready || wave >= 4) gather(0, true);
    gather(1, true);
    gather(2, false);
    ready = false;
    sl = lsp[0] & 31u;
    St[0] = ohl[sl * 4 + h * 2 + 0];
    St[1] = ohl[sl * 4 + h * 2 + 1];
    bv[0] = wvl[0 * 64];
    bv[1] = wvl[2 * 64];
    asm volatile("s_waitcnt vmcnt(5)" ::: "memory");
#pragma unroll
    for (int k = 0; k < 4; ++k) as[k] = *(const LAS bf16x8*)(ring + rs + (xa0 ^ (16 * k)));
    ae = *(const LAS bf16x8*)(ring + rs + ea0);
    G0 = MFMA32(St[0], XAf[0][0], zero16);
    G0 = MFMA32(St[1], XAf[1][0], G0);
    G0 = MFMA32(ae, wC[0], G0);
#pragma unroll
    for (int k = 0; k < 4; ++k) G0 = MFMA32(as[k], wB[k][0], G0);
    pd[1] = u32x4{0u, 0u, 0u, 0u};
    pn[1] = u32x4{0u, 0u, 0u, 0u};
    S[1] = BC(pd[1]);
    SB();
    for (int i = 0; i < ntiles; ++i) {
      {
        f32x16& Gc = G0;
        const unsigned d = ring_a + rs;
        LAS unsigned short* ohw = (LAS unsigned short*)(sm + sl * 80 + ohoff);
        Y[1] = MFMA32(S[1], BC(pd[1]), Y[1]);
        bv[2] = wvl[4 * 64];
        *(volatile LAS int*)pown = ntiles - i;
        E1(0); E1(1); E1(2); E1(3);
        SB();
        Y[3] = MFMA32(S[1], BC(pn[1]), Y[3]);
        bv[3] = wvl[6 * 64];
        E2(0); E2(1); E1(4); E1(5); E2(2); E2(3);
        SB();
        V = MFMA32(as[0], bv[0], zero16);
        const int prem = *(volatile const LAS int*)poth;
        E3(0); E3(1); E1(6); E1(7); E2(4); E2(5);
        SB();
        V = MFMA32(as[1], bv[1], V);
        E3(2); E3(3); E1(8); E1(9); E2(6); E2(7);
        SB();
        V = MFMA32(as[2], bv[2], V);
        E3(4); E3(5); E1(10); E1(11); E2(8); E2(9); CD(0);
        SB();
        V = MFMA32(as[3], bv[3], V);
        E3(6); E3(7); E1(12); E1(13); E2(10); E2(11); CD(1);
        SB();
        G1 = MFMA32(St[0], XAf[0][1], zero16);
        asm volatile("s_cmp_ge_i32 %0, %1\n\ts_setprio 0\n\ts_cbranch_scc0 1f\n\ts_setprio 1\n1:"
                     :: "s"(ntiles - i), "s"(__builtin_amdgcn_readfirstlane(prem)) : "scc");
        E3(8); E3(9); E1(14); E1(15); E2(12); E2(13); CD(2);
        SB();
        G1 = MFMA32(St[1], XAf[1][1], G1);
        *ohw = (unsigned short)0x3F80;
        asm volatile("" ::: "memory");
        S[0] = BC(mine[0]);
        S[1] = BC(mine[1]);
        asm volatile("" ::: "memory");
        *ohw = (unsigned short)0;
        E3(10); E3(11); E2(14); E2(15); CD(3);
        SB();
        G1 = MFMA32(ae, wC[1], G1);
        GLDS(ga[0], rx, d);
        E3(12); E3(13); MU(0); MU(1); MU(2); MU(3);
        SB();
        G1 = MFMA32(as[0], wB[0][1], G1);
        GLDS(ga[1], rx, d + 1024);
        E3(14); E3(15); MU(4); MU(5); CN(0); CN(1);
        SB();
        G1 = MFMA32(as[1], wB[1][1], G1);
        GLDS(ga[2], rx, d + 2048);
        MU(6); MU(7); CD(4); CD(5); CN(2);
        SB();
        G1 = MFMA32(as[2], wB[2][1], G1);
        GLDS(ga[3], rx, d + 3072);
        MU(8); MU(9); MU(10); MU(11); CN(3); CD(6);
        SB();
        G1 = MFMA32(as[3], wB[3][1], G1);
        GLDS(ga[4], re, d + 4096);
        slr = lsp[(i + 1) * 32];
        MU(12); MU(13); CN(4); CN(5); CD(7);
        SB();
        Y[0] = MFMA32(S[0], BC(pd[0]), Y[0]);
        bv[0] = wvl[1 * 64];
        MU(14); MU(15); CN(6);
        SB();
        Y[2] = MFMA32(S[0], BC(pn[0]), Y[2]);
        bv[1] = wvl[3 * 64];
        CN(7);
        SB();
      }
      rs ^= RING_SLOT;
      {
        f32x16& Gc = G1;
        const LAS char* rp = ring + rs;
        sl = slr & 31u;
        Y[0] = MFMA32(S[1], BC(pd[1]), Y[0]);
        bv[2] = wvl[5 * 64];
        St[0] = ohl[sl * 4 + h * 2 + 0];
        E1(0); E1(1); E1(2); E1(3);
        SB();
        Y[2] = MFMA32(S[1], BC(pn[1]), Y[2]);
        bv[3] = wvl[7 * 64];
        St[1] = ohl[sl * 4 + h * 2 + 1];
        E2(0); E2(1); E1(4); E1(5); E2(2); E2(3);
        SB();
        V = MFMA32(as[0], bv[0], zero16);
        E3(0); E3(1); E1(6); E1(7); E2(4); E2(5);
        SB();
        V = MFMA32(as[1], bv[1], V);
        E3(2); E3(3); E1(8); E1(9); E2(6); E2(7);
        SB();
        V = MFMA32(as[2], bv[2], V);
        asm volatile("s_waitcnt vmcnt(5)" ::: "memory");
        ae = *(const LAS bf16x8*)(rp + ea0);
        E3(4); E3(5); E1(10); E1(11); E2(8); E2(9); CD(0);
        SB();
        V = MFMA32(as[3], bv[3], V);
        as[0] = *(const LAS bf16x8*)(rp + xa0);
        E3(6); E3(7); E1(12); E1(13); E2(10); E2(11); CD(1);
        SB();
        G0 = MFMA32(St[0], XAf[0][0], zero16);
        as[1] = *(const LAS bf16x8*)(rp + (xa0 ^ 16));
        E3(8); E3(9); E1(14); E1(15); E2(12); E2(13); CD(2);
        SB();
        G0 = MFMA32(St[1], XAf[1][0], G0);
        as[2] = *(const LAS bf16x8*)(rp + (xa0 ^ 32));
        E3(10); E3(11); E2(14); E2(15); CD(3);
        SB();
        G0 = MFMA32(ae, wC[0], G0);
        as[3] = *(const LAS bf16x8*)(rp + (xa0 ^ 48));
        E3(12); E3(13); MU(0); MU(1); MU(2); MU(3);
        SB();
        G0 = MFMA32(as[0], wB[0][0], G0);
        const u32x4 xo = *(const LAS u32x4*)(lxp + (i + 3) * 32);
        const unsigned eo = lep[(i + 3) * 32];
        E3(14); E3(15); MU(4); MU(5); CN(0); CN(1);
        SB();
        G0 = MFMA32(as[1], wB[1][0], G0);
        MU(6); MU(7); CD(4); CD(5); CN(2);
        SB();
        G0 = MFMA32(as[2], wB[2][0], G0);
        MU(8); MU(9); MU(10); MU(11); CN(3); CD(6);
        SB();
        G0 = MFMA32(as[3], wB[3][0], G0);
        ga[0] = XG(0); ga[1] = XG(1);
        MU(12); MU(13); CN(4); CN(5); CD(7);
        SB();
        Y[1] = MFMA32(S[0], BC(pd[0]), Y[1]);
        bv[0] = wvl[0 * 64];
        ga[2] = XG(2); ga[3] = XG(3);
        MU(14); MU(15); CN(6);
        SB();
        Y[3] = MFMA32(S[0], BC(pn[0]), Y[3]);
        bv[1] = wvl[2 * 64];
        ga[4] = (eo & ~31u) | ec0;
        CN(7);
        SB();
      }
    }
    *(volatile LAS int*)pown = 0;
    Y[1] = MFMA32(S[1], BC(pd[1]), Y[1]);
    Y[3] = MFMA32(S[1], BC(pn[1]), Y[3]);
    if (!more) break;
  }
  u32x4 xr[4];
#pragma unroll
  for (int q = 0; q < 4; ++q) {
    const int row = q * 8 + (lane >> 3);
    xr[q] = *(const LAS u32x4*)(ring + rs + (row & 3) * 1024 + (row >> 2) * 128 +
                                (((lane & 7) ^ (((row >> 3) & 1) << 2) ^ (row & 3)) * 16));
  }
  f32x16 U0 = zero16, U1 = zero16;
#pragma unroll
  for (int k = 0; k < 4; ++k) {
    U0 = MFMA32(as[k], wvl[(8 + k * 2) * 64], U0);
    U1 = MFMA32(as[k], wvl[(9 + k * 2) * 64], U1);
  }
  asm volatile("s_waitcnt vmcnt(0) lgkmcnt(0)" : "+v"(xr[0]), "+v"(xr[1]), "+v"(xr[2]), "+v"(xr[3]) :: "memory");
  LAS float* zl = (LAS float*)wbase;
#pragma unroll
  for (int i = 0; i < 16; ++i) {
    const int row = (i & 3) + 8 * (i >> 2) + 4 * h;
    const float z0 = U0[i] + Y[2][i] * __builtin_amdgcn_rcpf(Y[0][i] + 1e-6f);
    const float z1 = U1[i] + Y[3][i] * __builtin_amdgcn_rcpf(Y[1][i] + 1e-6f);
    zl[row * 68 + r] = z0 > 0.f ? z0 : 0.f;
    zl[row * 68 + 32 + r] = z1 > 0.f ? z1 : 0.f;
  }
#pragma unroll
  for (int q = 0; q < 4; ++q) {
    const int row = q * 8 + (lane >> 3);
    const f32x4 z0 = *(const LAS f32x4*)(&zl[row * 68 + (lane & 7) * 8]);
    const f32x4 z1 = *(const LAS f32x4*)(&zl[row * 68 + (lane & 7) * 8 + 4]);
    u32x4 o;
    o[0] = pack_bf16(U2F(xr[q][0] << 16) + z0[0], U2F(xr[q][0] & 0xffff0000u) + z0[1]);
    o[1] = pack_bf16(U2F(xr[q][1] << 16) + z0[2], U2F(xr[q][1] & 0xffff0000u) + z0[3]);
    o[2] = pack_bf16(U2F(xr[q][2] << 16) + z1[0], U2F(xr[q][2] & 0xffff0000u) + z1[1]);
    o[3] = pack_bf16(U2F(xr[q][3] << 16) + z1[2], U2F(xr[q][3] & 0xffff0000u) + z1[3]);
    if (row < nodes) __builtin_amdgcn_raw_buffer_store_b128(o, SRD(out, N * 128), (n0 + 8 * row) * 128 + (lane & 7) * 16, 0, 17);
  }
}

extern "C" void kernel_launch(void* const* d_in, const int* in_sizes, int n_in,
                              void* d_out, int out_size, void* d_ws, size_t ws_size,
                              hipStream_t stream) {
  const unsigned short* x = (const unsigned short*)d_in[0];
  const unsigned short* eattr = (const unsigned short*)d_in[1];
  const int* src = (const int*)d_in[2];
  const int* dst = (const int*)d_in[3];
  const unsigned short* wa = (const unsigned short*)d_in[4];
  const unsigned short* wb = (const unsigned short*)d_in[5];
  const unsigned short* wc = (const unsigned short*)d_in[6];
  const unsigned short* wu = (const unsigned short*)d_in[7];
  const unsigned short* wv = (const unsigned short*)d_in[8];
  unsigned short* out = (unsigned short*)d_out;
  const int N = in_sizes[0] / 64;
  const int E = in_sizes[2];
  const int nb = (N + NPB - 1) / NPB;

  char* ws = (char*)d_ws;
  k_part<<<NCH, 1024, 0, stream>>>(src, dst, E, nb, (unsigned*)(ws + W_TC), (u32x4*)(ws + W_OVH), (u32x4*)(ws + W_OV),
                                   wa, wb, wc, wu, wv, (unsigned short*)(ws + W_WP));
  k_main<<<nb, 512, 0, stream>>>(x, eattr, ws, E, N, out);
}
```

```cpp
#include <hip/hip_runtime.h>
#include <hip/hip_bf16.h>
#include <stdint.h>

using bf16x8 = __attribute__((ext_vector_type(8))) short;
using f32x16 = __attribute__((ext_vector_type(16))) float;
using u32x4 = __attribute__((ext_vector_type(4))) unsigned;
using u32x2 = __attribute__((ext_vector_type(2))) unsigned;
using i32x4 = __attribute__((ext_vector_type(4))) int;
using f32x4 = __attribute__((ext_vector_type(4))) float;

#define MFMA32(a, b, c) __builtin_amdgcn_mfma_f32_32x32x16_bf16((a), (b), (c), 0, 0, 0)
#define LAS __attribute__((address_space(3)))
#define GAS __attribute__((address_space(1)))
#define ALD(p) __hip_atomic_load((p), __ATOMIC_RELAXED, __HIP_MEMORY_SCOPE_AGENT)
#if defined(__HIP_DEVICE_COMPILE__)
#define NOPK __attribute__((target("no-packed-fp32-ops")))
#else
#define NOPK
#endif
#define SRD(p, bytes) __builtin_amdgcn_make_buffer_rsrc((void*)(p), (short)0, (int)(bytes), 0x00020000)
#define LDS_ADD(p, v) __hip_atomic_fetch_add((p), (v), __ATOMIC_RELAXED, __HIP_MEMORY_SCOPE_WORKGROUP)

#define CST(p, v) __builtin_amdgcn_raw_buffer_store_b128((v), rc, (p) * 4, 0, 17)
#define WALL 1
constexpr int GPB = 8;
constexpr int NPB = 196;
constexpr int MAXB = 256;
constexpr int NCH = 256;
constexpr int CELL = 32;
constexpr int CAP = 28;
constexpr int CSTR = 36;
constexpr int OVCAP = 4096;
constexpr int NFRAG = 34;
constexpr int NREP = 8;
constexpr size_t W_TC = 0;
constexpr size_t W_OVH = W_TC + (size_t)MAXB * NCH * CELL * 4;
constexpr size_t W_FL = W_OVH + NCH * 32;
constexpr size_t W_EP = W_FL + NREP * NCH * 4;
constexpr size_t W_OV = W_EP + 256;
constexpr float NLOG2E = -1.44269504088896340736f;

__device__ __forceinline__ NOPK unsigned pack_bf16(float lo, float hi) {
  typedef __attribute__((ext_vector_type(2))) float f2;
  typedef __attribute__((ext_vector_type(2))) __bf16 b2;
  f2 v = {lo, hi};
  b2 r = __builtin_convertvector(v, b2);
  return __builtin_bit_cast(unsigned, r);
}
#define WGSYNC() do { __builtin_amdgcn_fence(__ATOMIC_RELEASE, "workgroup"); __builtin_amdgcn_s_barrier(); \
                      __builtin_amdgcn_fence(__ATOMIC_ACQUIRE, "workgroup"); } while (0)
#define U2F(u) __builtin_bit_cast(float, (unsigned)(u))
constexpr int RING_SLOT = 5 * 1024;
constexpr int LCAP = 512;
constexpr int LPAD = LCAP + 96;
constexpr int SSCR = 32 * 80;
constexpr int WAVE_LDS = 2 * RING_SLOT;
constexpr int M_WL = 0;
constexpr int M_OH = 16 * 1024;
constexpr int M_CNT = M_OH + 2048;
constexpr int M_ST = M_CNT + 256;
constexpr int M_SC = M_ST + 2048;
constexpr int M_LX = M_SC + GPB * SSCR;
constexpr int M_LE = M_LX + GPB * LPAD * 4;
constexpr int M_WV = M_LE + GPB * LPAD * 4;
constexpr int M_XG = M_WV + 4 * WAVE_LDS;
constexpr int MAIN_LDS = M_WV + GPB * WAVE_LDS;
constexpr int M_CT = M_WV;
constexpr int M_PC = M_CT + (MAXB + 1) * CSTR * 4;
constexpr int M_OL = M_PC + (MAXB + 4) * 4;
constexpr int M_RW = M_LX;
static_assert(M_OL + 32 <= M_XG && M_OL % 16 == 0 && M_RW + 34816 <= M_WV, "phase 1");
static_assert(M_LX % 256 == 0 && M_LE % 256 == 0 && M_LE < 65536 && M_LE + GPB * LPAD * 4 + NCH * CELL * 4 <= M_XG, "lists");
static_assert(MAIN_LDS <= 160 * 1024, "LDS");

__global__ __launch_bounds__(512, 2) NOPK void k_main(const int* __restrict__ src, const int* __restrict__ dst,
                                                      const unsigned short* __restrict__ x,
                                                      const unsigned short* __restrict__ eattr,
                                                      char* __restrict__ wsp, int E,
                                                      int N, unsigned short* __restrict__ out,
                                                      const unsigned short* __restrict__ wa,
                                                      const unsigned short* __restrict__ wb,
                                                      const unsigned short* __restrict__ wc,
                                                      const unsigned short* __restrict__ wu,
                                                      const unsigned short* __restrict__ wv) {
  __shared__ __attribute__((aligned(16))) char smem[MAIN_LDS];
  asm volatile("" :: "s"(src), "s"(dst), "s"(x), "s"(eattr), "s"(wsp), "s"(E), "s"(N), "s"(out),
                     "s"(wa), "s"(wb), "s"(wc), "s"(wu), "s"(wv));
  const unsigned* TC = reinterpret_cast<const unsigned*>(wsp + W_TC);
  const u32x4* OVH = reinterpret_cast<const u32x4*>(wsp + W_OVH);
  const u32x4* OV = reinterpret_cast<const u32x4*>(wsp + W_OV);
  const int tid = threadIdx.x, lane = tid & 63;
  const int wave = __builtin_amdgcn_readfirstlane(tid >> 6);
  const int r = lane & 31, h = lane >> 5;
  const int b = blockIdx.x;
  const int pc = lane & 7;
  const int c = (b & 7) * 32 + (b >> 3);
#define BAR() asm volatile("s_waitcnt lgkmcnt(0)\n\ts_barrier" ::: "memory")
#define GLDS(vo, rs, mv) asm volatile("s_mov_b32 m0, %2\n\ts_nop 0\n\tbuffer_load_dwordx4 %0, %1, 0 offen lds" \
                                      :: "v"(vo), "s"(rs), "s"(mv) : "memory")
#define BC(v) __builtin_bit_cast(bf16x8, v)

  i32x4 dd[2], ss[2];
  unsigned ep;
  const int e0 = (wave * NCH + c) * 256 + lane * 4;
  {
    const auto rd = SRD(dst, E * 4), rs = SRD(src, E * 4);
    asm volatile("buffer_load_dwordx4 %0, %5, %7, 0 offen\n\tbuffer_load_dwordx4 %1, %6, %7, 0 offen\n\t"
                 "buffer_load_dwordx4 %2, %5, %8, 0 offen\n\tbuffer_load_dwordx4 %3, %6, %8, 0 offen\n\t"
                 "global_load_dword %4, %9, %10 sc1"
                 : "=&v"(dd[0]), "=&v"(dd[1]), "=&v"(ss[0]), "=&v"(ss[1]), "=&v"(ep)
                 : "v"(e0 * 4), "v"(e0 * 4 + 8 * NCH * 1024), "s"(rd), "s"(rs), "v"(0), "s"(wsp + W_EP) : "memory");
  }
  const auto rx = SRD(x, N * 128);
  const auto re = SRD(eattr, E * 32);
  const unsigned xc0 = (unsigned)(((lane & 7) ^ ((lane >> 2) & 4)) * 16);
  const unsigned lb = (unsigned)(size_t)(LAS char*)smem;
  {
    const unsigned vo = (unsigned)(wave * 1024 + lane * 16);
    GLDS(vo, SRD(wa, 8192), lb + M_RW + 16384 + wave * 1024);
    GLDS(vo, SRD(wb, 8192), lb + M_RW + wave * 1024);
    GLDS(vo, SRD(wv, 8192), lb + M_RW + 8192 + wave * 1024);
    GLDS(vo, SRD(wu, 8192), lb + M_RW + 24576 + wave * 1024);
    asm volatile("s_mov_b32 m0, %2\n\ts_nop 0\n\tbuffer_load_dword %0, %1, 0 offen lds"
                 :: "v"(vo >> 2), "s"(SRD(wc, 2048)), "s"(lb + M_RW + 32768 + wave * 256) : "memory");
  }
  LAS int* cnt = (LAS int*)(smem + M_CNT);
  LAS unsigned* cT = (LAS unsigned*)(smem + M_CT);
  LAS int* pcn = (LAS int*)(smem + M_PC);
  LAS unsigned* ovl = (LAS unsigned*)(smem + M_OL);
  *(LAS u32x2*)(cT + (tid >> 1) * CSTR + (tid & 1) * 16) = u32x2{0u, 0u};
  if (tid < MAXB + 12) pcn[tid] = tid < MAXB + 4 ? 0 : 0x100;
  else if (tid >= 480) cnt[r] = (r & 3) == 0 ? (r >> 2) * (LPAD * 4) : (r & 3) == 2 ? 127 : 0;
  __builtin_amdgcn_s_setprio(1);
  BAR();
  {
    unsigned* oh = reinterpret_cast<unsigned*>(smem + M_OH);
    const int i = tid;
    const int sl = i >> 4, hh = (i >> 3) & 1, ks = (i >> 2) & 1, jp = i & 3;
    const int j0 = 2 * jp, j1 = 2 * jp + 1;
    const int nd0 = 16 * ks + 8 * (j0 >> 2) + 4 * hh + (j0 & 3);
    const int nd1 = 16 * ks + 8 * (j1 >> 2) + 4 * hh + (j1 & 3);
    oh[i] = (sl == nd0 ? 0x3F80u : 0u) | (sl == nd1 ? 0x3F800000u : 0u);
  }
  {
    int bk[8], rk[8];
#pragma unroll
    for (int q = 0; q < 2; ++q) {
      asm volatile("s_waitcnt vmcnt(%1)" : "+v"(dd[q]) : "n"(9 - q) : "memory");
      if (((wave + 8 * q) * NCH + c) * 256 < E) {
#pragma unroll
        for (int k = 0; k < 4; ++k) {
          const bool v = e0 + q * (8 * NCH * 256) + k < E;
          bk[4 * q + k] = v ? (int)((unsigned)dd[q][k] / (unsigned)NPB) : MAXB + 1;
          rk[4 * q + k] = LDS_ADD(&pcn[bk[4 * q + k]], 1);
        }
      }
    }
#pragma unroll
    for (int q = 0; q < 2; ++q) {
      asm volatile("s_waitcnt vmcnt(%1)" : "+v"(ss[q]) : "n"(7 - q) : "memory");
      if (((wave + 8 * q) * NCH + c) * 256 < E) {
        const unsigned rnd = (unsigned)(wave + 8 * q);
        unsigned w[4];
        bool ovf = false;
#pragma unroll
        for (int k = 0; k < 4; ++k) {
          const int bq = bk[4 * q + k], rq = rk[4 * q + k];
          const unsigned s = (unsigned)(dd[q][k] - bq * NPB);
          w[k] = ((unsigned)ss[q][k] << 16) | ((s & 7u) << 13) | ((unsigned)(lane * 4 + k) << 5) | (s >> 3);
          const int d = (rq + (rq < CAP / 2 ? 2 : 4)) & 31;
          LAS unsigned* cp = cT + (rq < CAP ? min(bq, MAXB) : MAXB) * CSTR;
          cp[d] = w[k];
          __hip_atomic_fetch_or(cp + (d & 16) + ((d >> 3) & 1), rnd << (4 * (d & 7)),
                                __ATOMIC_RELAXED, __HIP_MEMORY_SCOPE_WORKGROUP);
          ovf |= rq >= CAP;
        }
        if (__builtin_amdgcn_ballot_w64(ovf) != 0) {
#pragma unroll 1
          for (int k = 0; k < 4; ++k) {
            const int bq = k == 0 ? bk[4 * q] : k == 1 ? bk[4 * q + 1] : k == 2 ? bk[4 * q + 2] : bk[4 * q + 3];
            const int rq = k == 0 ? rk[4 * q] : k == 1 ? rk[4 * q + 1] : k == 2 ? rk[4 * q + 2] : rk[4 * q + 3];
            const unsigned wq = k == 0 ? w[0] : k == 1 ? w[1] : k == 2 ? w[2] : w[3];
            if (bq < MAXB && rq >= CAP) {
              const int o = LDS_ADD(&pcn[MAXB], 1);
              const u32x4 v = {wq >> 9, ((unsigned)(e0 + q * (8 * NCH * 256) + k) << 5) | (wq & 31u), (unsigned)bq, 0u};
              __builtin_amdgcn_raw_buffer_store_b128(v, SRD(OV, NCH * OVCAP * 16), (c * OVCAP + o) * 16, 0, 17);
              if (o < 4) { ovl[2 * o] = wq; ovl[2 * o + 1] = (rnd << 12) | (unsigned)bq; }
            }
          }
          asm volatile("s_waitcnt vmcnt(0)" ::: "memory");
        }
      }
    }
  }
  BAR();
  {
    const auto rc = SRD(TC, MAXB * NCH * CELL * 4);
    const int first = (CAP / 2) * (pc >> 2) + 4 * (pc & 3) - 2;
#pragma unroll
    for (int q = 0; q < 4; ++q) {
      const int cb = (tid >> 3) + q * 64;
      const int m = pcn[cb];
      u32x4 v = *(const LAS u32x4*)(cT + cb * CSTR + 4 * pc);
      if ((pc & 3) == 0) v[0] |= (unsigned)min(max(m - first - 2, 0), CAP / 2);
      CST((cb * NCH + c) * CELL + 4 * pc, v);
    }
    if (tid < 2) {
      u32x4 v = *(const LAS u32x4*)(ovl + 4 * tid);
      v[1] |= (unsigned)pcn[MAXB] << 16;
      v[3] |= (unsigned)pcn[MAXB] << 16;
      __builtin_amdgcn_raw_buffer_store_b128(v, SRD(OVH, NCH * 32), c * 32 + tid * 16, 0, 17);
    }
  }
  asm volatile("s_waitcnt vmcnt(0) lgkmcnt(0)\n\ts_barrier" : "+v"(ep) :: "memory");
  ep += 0x9E3779B9u;
  if (tid < NREP) __builtin_amdgcn_raw_buffer_store_b32(ep, SRD(wsp + W_FL, NREP * NCH * 4), (tid * NCH + c) * 4, 0, 17);
  auto xrows = [&]() NOPK __attribute__((always_inline)) {
    const int nw = (min(NPB - wave, N - b * NPB - wave) + 7) >> 3;
#pragma unroll
    for (int k = 0; k < 4; ++k) {
      const int s = (lane >> 3) * 4 + k;
      const unsigned xo = s < nw ? (unsigned)(b * NPB + wave + 8 * s) << 7 : 0xFFFFFF80u;
      GLDS(xo | (xc0 ^ (16u * k)), rx, lb + M_XG + wave * 4096 + k * 1024);
    }
  };

  const int xa0 = (r & 3) * 1024 + (r >> 2) * 128 + (((r & 3) | ((h ^ ((r >> 3) & 1)) << 2)) * 16);
  {
    const LAS unsigned short* p = (const LAS unsigned short*)(smem + M_RW) + (32 * h + 8 * (wave >> 1)) * 64 + 32 * (wave & 1) + r;
    const LAS unsigned short* pw = (const LAS unsigned short*)(smem + M_RW + 32768) + 8 * h * 64 + 32 * (wave & 1) + r;
    u32x4 f[5];
#pragma unroll
    for (int j = 0; j < 4; ++j) {
      f[0][j] = pack_bf16(U2F((unsigned)p[2 * j * 64] << 16) * NLOG2E, U2F((unsigned)p[(2 * j + 1) * 64] << 16) * NLOG2E);
      f[4][j] = pack_bf16(U2F((unsigned)pw[2 * j * 64] << 16) * NLOG2E, U2F((unsigned)pw[(2 * j + 1) * 64] << 16) * NLOG2E);
#pragma unroll
      for (int m = 1; m < 4; ++m)
        f[m][j] = (unsigned)p[m * 4096 + 2 * j * 64] | ((unsigned)p[m * 4096 + (2 * j + 1) * 64] << 16);
    }
    *(LAS u32x4*)(smem + M_SC + tid * 16) = f[0];
    *(LAS u32x4*)(smem + M_WL + tid * 16) = f[1];
    *(LAS u32x4*)(smem + M_SC + 8192 + tid * 16) = f[2];
    *(LAS u32x4*)(smem + M_WL + 8192 + tid * 16) = f[3];
    if (wave < 2) *(LAS u32x4*)(smem + M_SC + 16384 + tid * 16) = f[4];
  }
  BAR();
  const f32x16 zero16 = {0.f, 0.f, 0.f, 0.f, 0.f, 0.f, 0.f, 0.f, 0.f, 0.f, 0.f, 0.f, 0.f, 0.f, 0.f, 0.f};
  bf16x8 wB[4][2], wC[2], XAf[2][2];
  {
    const LAS bf16x8* ws = (const LAS bf16x8*)(smem + M_SC) + lane;
#pragma unroll
    for (int k = 0; k < 8; ++k) wB[k >> 1][k & 1] = ws[k * 64];
    wC[0] = ws[16 * 64];
    wC[1] = ws[17 * 64];
    asm volatile("s_waitcnt lgkmcnt(0)"
                 : "+v"(wB[0][0]), "+v"(wB[0][1]), "+v"(wB[1][0]), "+v"(wB[1][1]), "+v"(wB[2][0]), "+v"(wB[2][1]),
                   "+v"(wB[3][0]), "+v"(wB[3][1]), "+v"(wC[0]), "+v"(wC[1]) :: "memory");
  }
  bool xlate = true;
  {
    const GAS unsigned* fp = (const GAS unsigned*)(wsp + W_FL) + (b & (NREP - 1)) * NCH + wave * 32 + r;
#pragma unroll 1
    for (int spin = 0; spin < (1 << 20); ++spin) {
      const unsigned v = __hip_atomic_load(fp, __ATOMIC_RELAXED, __HIP_MEMORY_SCOPE_AGENT);
      if (__builtin_amdgcn_ballot_w64(v != ep) == 0) break;
      __builtin_amdgcn_s_sleep(2);
    }
  }
  u32x4 tv[4], ovh;
  {
    const unsigned* cp = TC + ((size_t)b * NCH + wave * 32) * CELL;
    asm volatile("global_load_dwordx4 %0, %5, %6 sc1\n\tglobal_load_dwordx4 %1, %5, %6 offset:1024 sc1\n\t"
                 "global_load_dwordx4 %2, %5, %6 offset:2048 sc1\n\tglobal_load_dwordx4 %3, %5, %6 offset:3072 sc1\n\t"
                 "global_load_dwordx4 %4, %5, %7 sc1"
                 : "=&v"(tv[0]), "=&v"(tv[1]), "=&v"(tv[2]), "=&v"(tv[3]), "=&v"(ovh)
                 : "v"(lane * 16), "s"(cp), "s"(OVH + wave * 64) : "memory");
  }
  const LAS bf16x8* wl = (const LAS bf16x8*)(smem + M_WL);
  const LAS bf16x8* ohl = (const LAS bf16x8*)(smem + M_OH);
  LAS unsigned* lxa = (LAS unsigned*)(smem + M_LX);
  LAS unsigned* lea = (LAS unsigned*)(smem + M_LE);
  char* wbase = smem + M_WV + wave * WAVE_LDS;
  const unsigned ring_a = (unsigned)(size_t)(LAS char*)wbase;
  {
    unsigned long long vm[16];
    unsigned ad[16], xe[16], hw[4];
    const unsigned four = 4u;
#pragma unroll
    for (int k = 0; k < 4; ++k) {
      asm volatile("s_waitcnt vmcnt(%1)" : "+v"(tv[k]) : "n"(4 - k) : "memory");
      const unsigned h0 = (unsigned)__builtin_amdgcn_mov_dpp((int)tv[k][0], 0x00, 0xF, 0xF, true);
      const unsigned h1 = (unsigned)__builtin_amdgcn_mov_dpp((int)tv[k][1], 0x00, 0xF, 0xF, true);
      hw[k] = ((lane & 2) ? h1 : h0) >> ((lane & 1) * 16);
      const unsigned m = h0 & 0xFFu;
      unsigned ci[4];
#pragma unroll
      for (int j = 0; j < 4; ++j) {
        vm[4 * k + j] = __builtin_amdgcn_ballot_w64((unsigned)((lane & 3) * 4 + j - 2) < m);
        xe[4 * k + j] = tv[k][j] >> 9;
        ci[j] = (xe[4 * k + j] & 0x70u) + lb;
      }
      asm volatile("s_mov_b64 exec, %4\n\tds_add_rtn_u32 %0, %8, %12 offset:%13\n\t"
                   "s_mov_b64 exec, %5\n\tds_add_rtn_u32 %1, %9, %12 offset:%13\n\t"
                   "s_mov_b64 exec, %6\n\tds_add_rtn_u32 %2, %10, %12 offset:%13\n\t"
                   "s_mov_b64 exec, %7\n\tds_add_rtn_u32 %3, %11, %12 offset:%13\n\t"
                   "s_mov_b64 exec, -1"
                   : "=&v"(ad[4 * k]), "=&v"(ad[4 * k + 1]), "=&v"(ad[4 * k + 2]), "=&v"(ad[4 * k + 3])
                   : "s"(vm[4 * k]), "s"(vm[4 * k + 1]), "s"(vm[4 * k + 2]), "s"(vm[4 * k + 3]),
                     "v"(ci[0]), "v"(ci[1]), "v"(ci[2]), "v"(ci[3]), "v"(four), "n"(M_CNT)
                   : "memory");
    }
    asm volatile("s_waitcnt vmcnt(0)" : "+v"(ovh) :: "memory");
    if (xlate) xrows();
#pragma unroll
    for (int k = 0; k < 4; ++k) {
      const unsigned c13 = (unsigned)(wave * 32 + k * 8 + (lane >> 3)) << 13;
      unsigned ee[4];
#pragma unroll
      for (int j = 0; j < 4; ++j) ee[j] = (tv[k][j] & 0x1FFFu) | ((((hw[k] >> (4 * j)) & 15u) << 21) | c13);
      asm volatile("s_waitcnt lgkmcnt(12)\n\t"
                   "s_mov_b64 exec, %0\n\tds_write2st64_b32 %4, %8, %12 offset0:%16 offset1:%17\n\t"
                   "s_mov_b64 exec, %1\n\tds_write2st64_b32 %5, %9, %13 offset0:%16 offset1:%17\n\t"
                   "s_mov_b64 exec, %2\n\tds_write2st64_b32 %6, %10, %14 offset0:%16 offset1:%17\n\t"
                   "s_mov_b64 exec, %3\n\tds_write2st64_b32 %7, %11, %15 offset0:%16 offset1:%17\n\t"
                   "s_mov_b64 exec, -1"
                   :: "s"(vm[4 * k]), "s"(vm[4 * k + 1]), "s"(vm[4 * k + 2]), "s"(vm[4 * k + 3]),
                      "v"(ad[4 * k]), "v"(ad[4 * k + 1]), "v"(ad[4 * k + 2]), "v"(ad[4 * k + 3]),
                      "v"(xe[4 * k]), "v"(xe[4 * k + 1]), "v"(xe[4 * k + 2]), "v"(xe[4 * k + 3]),
                      "v"(ee[0]), "v"(ee[1]), "v"(ee[2]), "v"(ee[3]), "n"(M_LX / 256), "n"(M_LE / 256)
                   : "memory");
    }
  }
  if (__builtin_amdgcn_ballot_w64((ovh[1] & 0x100u) == 0u) != 0) {
#pragma unroll
    for (int q = 0; q < 2; ++q) {
      const unsigned w = ovh[2 * q], m = ovh[2 * q + 1];
      if ((m & 0x1FFu) == (unsigned)b) {
        LAS unsigned* p = (LAS unsigned*)((LAS char*)lxa + LDS_ADD((LAS int*)((LAS char*)cnt + ((w >> 9) & 0x70u)), 4));
        p[0] = w >> 9;
        p[GPB * LPAD] = (w & 0x1FFFu) | ((m & 0xF000u) << 9) | ((unsigned)(tid >> 1) << 13);
      }
    }
    if ((ovh[1] >> 16) > 4u) cnt[1] = 1;
  }
  asm volatile("s_waitcnt lgkmcnt(0)\n\ts_barrier" ::: "memory");
  if (b == 0 && tid == 0) __builtin_amdgcn_raw_buffer_store_b32(ep, SRD(wsp + W_EP, 4), 0, 0, 17);
  const int cpc = cnt[4 * pc] - pc * (LPAD * 4);
  const int cpa = cnt[4 * (lane >> 3)] - (lane >> 3) * (LPAD * 4);
  bool ready = __builtin_amdgcn_ballot_w64(cpc > LCAP * 4) == 0;
  int grp;
  {
    const unsigned long long gt = __builtin_amdgcn_ballot_w64(cpc * 2 + pc > cpa * 2 + (lane >> 3));
    const unsigned wd = h ? (unsigned)(gt >> 32) : (unsigned)gt;
    const int rk = __builtin_popcount((wd >> (r & 24)) & 0xFFu);
    grp = __builtin_ctzll(__builtin_amdgcn_ballot_w64(rk == (wave < 4 ? wave : 11 - wave))) >> 3;
  }
  const int gn0 = b * NPB + grp;
  const int nodes = max((min(NPB - grp, N - gn0) + 7) >> 3, 0);
  const int n0 = min(gn0, N - 1);
  ((LAS unsigned*)(smem + M_ST))[tid] = (ready ? 0x40000u : 0xFFFFu) | ((unsigned)(tid & 1) << 16);

  f32x16 Y[4];
#pragma unroll
  for (int q = 0; q < 4; ++q)
#pragma unroll
    for (int i = 0; i < 16; ++i) Y[q][i] = 0.f;

  const unsigned ec0 = (unsigned)(((lane & 1) ^ ((lane >> 4) & 1)) * 16);
  const LAS unsigned* lx = lxa + grp * LPAD;
  const LAS unsigned* le = lea + grp * LPAD;
  const int ohoff = ((((r >> 2) & 1) * 16) + ((r >> 4) * 8) + (((r >> 3) & 1) * 4) + (r & 3)) * 2;
  const int ea0 = 4096 + r * 32 + ((h ^ ((r >> 3) & 1)) * 16);
#define SB() __builtin_amdgcn_sched_barrier(0)
  const LAS bf16x8* wvl = wl + lane;
  f32x16 G0, G1, V;
  u32x4 pd[2], pn[2];
  bf16x8 S[2], St[2], as[4], ae, bv[4];
  LAS char* sm = (LAS char*)(smem + M_SC + wave * SSCR);
  LAS u32x4* mine = (LAS u32x4*)(sm + r * 80 + h * 32);
  const LAS char* ring = (const LAS char*)wbase;
  unsigned ga[5];
  unsigned slr;
  int sl;
  int rs = 0;

#define E1(i) Gc[i] = __builtin_amdgcn_exp2f(Gc[i])
#define E2(i) Gc[i] = Gc[i] + 1.0f
#define E3(i) Gc[i] = __builtin_amdgcn_rcpf(Gc[i])
#define MU(i) V[i] = V[i] * Gc[i]
#define CD(j) pd[(j) >> 2][(j) & 3] = pack_bf16(Gc[2 * (j)], Gc[2 * (j) + 1])
#define CN(j) pn[(j) >> 2][(j) & 3] = pack_bf16(V[2 * (j)], V[2 * (j) + 1])
#define XG(k) ((xo[k] & ~127u) | (xc0 ^ (16u * (k))))

  int more, ntiles;
  LAS int* pown = cnt + 4 * wave + 2;
  const LAS int* poth = cnt + 4 * (wave ^ 4) + 2;
  const LAS unsigned* lxp = lx + (lane >> 3) * 4;
  const LAS unsigned* lep = le + (lane >> 1);
  const LAS unsigned* lsp = le + r;
  auto round_begin = [&]() NOPK __attribute__((always_inline)) {
    const int cn = min((__builtin_amdgcn_readfirstlane(cnt[4 * grp]) - grp * (LPAD * 4)) >> 2, LCAP);
    more = __builtin_amdgcn_readfirstlane(cnt[1]);
    ntiles = (cn + 31) >> 5;
    {
      LAS unsigned* wx = lxa + grp * LPAD;
      LAS unsigned* we = lea + grp * LPAD;
      const int e0 = ntiles * 32;
      if (cn + r < e0) { wx[cn + r] = 0xFFFFFF80u; we[cn + r] = 0xFFFFFFFFu; }
      wx[e0 + lane] = lane < nodes ? (unsigned)(n0 + 8 * lane) << 7 : 0xFFFFFF80u;
      we[e0 + lane] = 0xFFFFFFFFu;
      wx[e0 + 64 + r] = 0xFFFFFF80u;
      we[e0 + 64 + r] = 0xFFFFFFFFu;
    }
  };
  auto gather = [&](int t, bool issue) NOPK __attribute__((always_inline)) {
    const u32x4 xo = *(const LAS u32x4*)(lxp + t * 32);
    const unsigned eo = lep[t * 32];
    ga[0] = XG(0); ga[1] = XG(1); ga[2] = XG(2); ga[3] = XG(3);
    ga[4] = (eo & ~31u) | ec0;
    if (issue) {
      const unsigned d = ring_a + (rs ^ (t * RING_SLOT));
      GLDS(ga[0], rx, d); GLDS(ga[1], rx, d + 1024); GLDS(ga[2], rx, d + 2048); GLDS(ga[3], rx, d + 3072);
      GLDS(ga[4], re, d + 4096);
    }
  };
  if (ready) {
    round_begin();
    if (wave < 4) gather(0, true);
  }
  asm volatile("s_cmp_lg_u32 %0, 0\n\ts_cbranch_scc1 1f\n\ts_waitcnt vmcnt(0)\n1:\n\ts_waitcnt vmcnt(5)\n\ts_barrier"
               :: "s"(__builtin_amdgcn_readfirstlane((ready && wave < 4) ? 1 : 0)) : "memory", "scc");
  {
    const LAS bf16x8* ws = (const LAS bf16x8*)(smem + M_SC) + lane;
    bf16x8 xg[4];
#pragma unroll
    for (int k = 0; k < 4; ++k) xg[k] = *(const LAS bf16x8*)(smem + M_XG + grp * 4096 + (xa0 ^ (16 * k)));
#pragma unroll
    for (int ct = 0; ct < 2; ++ct) {
      f32x16 A = zero16;
#pragma unroll
      for (int k = 0; k < 4; ++k) A = MFMA32(xg[k], ws[(8 + k * 2 + ct) * 64], A);
#pragma unroll
      for (int s = 0; s < 2; ++s) {
        u32x4 p;
#pragma unroll
        for (int j = 0; j < 4; ++j) p[j] = pack_bf16(A[8 * s + 2 * j] * NLOG2E, A[8 * s + 2 * j + 1] * NLOG2E);
        XAf[s][ct] = BC(p);
      }
    }
    BAR();
    const u32x4 z4 = {0u, 0u, 0u, 0u};
    mine[0] = z4;
    mine[1] = z4;
  }
  for (;;) {
   if (!ready) {
    asm volatile("s_waitcnt vmcnt(0)" ::: "memory");
    WGSYNC();
    {
      int t2 = (int)__builtin_amdgcn_mbcnt_hi(~0u, __builtin_amdgcn_mbcnt_lo(~0u, 0u));
      asm volatile("" : "+v"(t2));
      const int l2 = t2;
      t2 += wave * 64;
      if (t2 < 32) cnt[t2] = (t2 & 3) == 0 ? (t2 >> 2) * (LPAD * 4) : 0;
      WGSYNC();
      auto put = [&](unsigned t, unsigned e) NOPK -> bool {
        const unsigned gg = (t >> 4) & 7u;
        const int ad = LDS_ADD(&cnt[4 * gg], 4) >> 2;
        if (ad - (int)gg * LPAD >= LCAP) return false;
        lxa[ad] = t;
        lea[ad] = e;
        return true;
      };
      LAS unsigned* stp = (LAS unsigned*)(smem + M_ST) + t2;
      unsigned pe = *stp & 0xFFFFu, ocur = *stp >> 16;
#pragma unroll 1
      for (int j = 0; j < 16; ++j) {
        if ((pe >> j) & 1u) {
          const int cc = wave * 32 + (j >> 2) * 8 + (l2 >> 3);
          const GAS unsigned* cp = (const GAS unsigned*)TC + ((size_t)b * NCH + cc) * CELL;
          const int d = (l2 & 7) * 4 + (j & 3);
          const unsigned w = ALD(cp + d);
          const unsigned nib = (ALD(cp + (d & 16) + ((d >> 3) & 1)) >> (4 * (d & 7))) & 15u;
          if ((unsigned)((d & 15) - 2) >= (ALD(cp + (d & 16)) & 0xFFu) ||
              put(w >> 9, (w & 0x1FFFu) | (nib << 21) | ((unsigned)cc << 13))) pe &= ~(1u << j);
        }
      }
      const unsigned on = ALD((const GAS unsigned*)OVH + t2 * 4 + 1) >> 16;
      const GAS unsigned* ob = (const GAS unsigned*)OV + (size_t)(t2 >> 1) * OVCAP * 4;
#pragma unroll 1
      for (; ocur < on; ocur += 2) {
        if ((int)ALD(ob + ocur * 4 + 2) == b && !put(ALD(ob + ocur * 4), ALD(ob + ocur * 4 + 1))) break;
      }
      if (pe != 0u || ocur < on) cnt[1] = 1;
      *stp = pe | (ocur << 16);
      __builtin_amdgcn_s_waitcnt(0x0F70);
    }
    WGSYNC();
    round_begin();
   }
    if (!ready || wave >= 4) gather(0, true);
    gather(1, true);
    gather(2, false);
    ready = false;
    sl = lsp[0] & 31u;
    St[0] = ohl[sl * 4 + h * 2 + 0];
    St[1] = ohl[sl * 4 + h * 2 + 1];
    bv[0] = wvl[0 * 64];
    bv[1] = wvl[2 * 64];
    asm volatile("s_waitcnt vmcnt(5)" ::: "memory");
#pragma unroll
    for (int k = 0; k < 4; ++k) as[k] = *(const LAS bf16x8*)(ring + rs + (xa0 ^ (16 * k)));
    ae = *(const LAS bf16x8*)(ring + rs + ea0);
    G0 = MFMA32(St[0], XAf[0][0], zero16);
    G0 = MFMA32(St[1], XAf[1][0], G0);
    G0 = MFMA32(ae, wC[0], G0);
#pragma unroll
    for (int k = 0; k < 4; ++k) G0 = MFMA32(as[k], wB[k][0], G0);
    pd[1] = u32x4{0u, 0u, 0u, 0u};
    pn[1] = u32x4{0u, 0u, 0u, 0u};
    S[1] = BC(pd[1]);
    SB();
    for (int i = 0; i < ntiles; ++i) {
      {
        f32x16& Gc = G0;
        const unsigned d = ring_a + rs;
        LAS unsigned short* ohw = (LAS unsigned short*)(sm + sl * 80 + ohoff);
        Y[1] = MFMA32(S[1], BC(pd[1]), Y[1]);
        bv[2] = wvl[4 * 64];
        *(volatile LAS int*)pown = ntiles - i;
        E1(0); E1(1); E1(2); E1(3);
        SB();
        Y[3] = MFMA32(S[1], BC(pn[1]), Y[3]);
        bv[3] = wvl[6 * 64];
        E2(0); E2(1); E1(4); E1(5); E2(2); E2(3);
        SB();
        V = MFMA32(as[0], bv[0], zero16);
        const int prem = *(volatile const LAS int*)poth;
        E3(0); E3(1); E1(6); E1(7); E2(4); E2(5);
        SB();
        V = MFMA32(as[1], bv[1], V);
        E3(2); E3(3); E1(8); E1(9); E2(6); E2(7);
        SB();
        V = MFMA32(as[2], bv[2], V);
        E3(4); E3(5); E1(10); E1(11); E2(8); E2(9); CD(0);
        SB();
        V = MFMA32(as[3], bv[3], V);
        E3(6); E3(7); E1(12); E1(13); E2(10); E2(11); CD(1);
        SB();
        G1 = MFMA32(St[0], XAf[0][1], zero16);
        asm volatile("s_cmp_ge_i32 %0, %1\n\ts_setprio 0\n\ts_cbranch_scc0 1f\n\ts_setprio 1\n1:"
                     :: "s"(ntiles - i), "s"(__builtin_amdgcn_readfirstlane(prem)) : "scc");
        E3(8); E3(9); E1(14); E1(15); E2(12); E2(13); CD(2);
        SB();
        G1 = MFMA32(St[1], XAf[1][1], G1);
        *ohw = (unsigned short)0x3F80;
        asm volatile("" ::: "memory");
        S[0] = BC(mine[0]);
        S[1] = BC(mine[1]);
        asm volatile("" ::: "memory");
        *ohw = (unsigned short)0;
        E3(10); E3(11); E2(14); E2(15); CD(3);
        SB();
        G1 = MFMA32(ae, wC[1], G1);
        GLDS(ga[0], rx, d);
        E3(12); E3(13); MU(0); MU(1); MU(2); MU(3);
        SB();
        G1 = MFMA32(as[0], wB[0][1], G1);
        GLDS(ga[1], rx, d + 1024);
        E3(14); E3(15); MU(4); MU(5); CN(0); CN(1);
        SB();
        G1 = MFMA32(as[1], wB[1][1], G1);
        GLDS(ga[2], rx, d + 2048);
        MU(6); MU(7); CD(4); CD(5); CN(2);
        SB();
        G1 = MFMA32(as[2], wB[2][1], G1);
        GLDS(ga[3], rx, d + 3072);
        MU(8); MU(9); MU(10); MU(11); CN(3); CD(6);
        SB();
        G1 = MFMA32(as[3], wB[3][1], G1);
        GLDS(ga[4], re, d + 4096);
        slr = lsp[(i + 1) * 32];
        MU(12); MU(13); CN(4); CN(5); CD(7);
        SB();
        Y[0] = MFMA32(S[0], BC(pd[0]), Y[0]);
        bv[0] = wvl[1 * 64];
        MU(14); MU(15); CN(6);
        SB();
        Y[2] = MFMA32(S[0], BC(pn[0]), Y[2]);
        bv[1] = wvl[3 * 64];
        CN(7);
        SB();
      }
      rs ^= RING_SLOT;
      {
        f32x16& Gc = G1;
        const LAS char* rp = ring + rs;
        sl = slr & 31u;
        Y[0] = MFMA32(S[1], BC(pd[1]), Y[0]);
        bv[2] = wvl[5 * 64];
        St[0] = ohl[sl * 4 + h * 2 + 0];
        E1(0); E1(1); E1(2); E1(3);
        SB();
        Y[2] = MFMA32(S[1], BC(pn[1]), Y[2]);
        bv[3] = wvl[7 * 64];
        St[1] = ohl[sl * 4 + h * 2 + 1];
        E2(0); E2(1); E1(4); E1(5); E2(2); E2(3);
        SB();
        V = MFMA32(as[0], bv[0], zero16);
        E3(0); E3(1); E1(6); E1(7); E2(4); E2(5);
        SB();
        V = MFMA32(as[1], bv[1], V);
        E3(2); E3(3); E1(8); E1(9); E2(6); E2(7);
        SB();
        V = MFMA32(as[2], bv[2], V);
        asm volatile("s_waitcnt vmcnt(5)" ::: "memory");
        ae = *(const LAS bf16x8*)(rp + ea0);
        E3(4); E3(5); E1(10); E1(11); E2(8); E2(9); CD(0);
        SB();
        V = MFMA32(as[3], bv[3], V);
        as[0] = *(const LAS bf16x8*)(rp + xa0);
        E3(6); E3(7); E1(12); E1(13); E2(10); E2(11); CD(1);
        SB();
        G0 = MFMA32(St[0], XAf[0][0], zero16);
        as[1] = *(const LAS bf16x8*)(rp + (xa0 ^ 16));
        E3(8); E3(9); E1(14); E1(15); E2(12); E2(13); CD(2);
        SB();
        G0 = MFMA32(St[1], XAf[1][0], G0);
        as[2] = *(const LAS bf16x8*)(rp + (xa0 ^ 32));
        E3(10); E3(11); E2(14); E2(15); CD(3);
        SB();
        G0 = MFMA32(ae, wC[0], G0);
        as[3] = *(const LAS bf16x8*)(rp + (xa0 ^ 48));
        E3(12); E3(13); MU(0); MU(1); MU(2); MU(3);
        SB();
        G0 = MFMA32(as[0], wB[0][0], G0);
        const u32x4 xo = *(const LAS u32x4*)(lxp + (i + 3) * 32);
        const unsigned eo = lep[(i + 3) * 32];
        E3(14); E3(15); MU(4); MU(5); CN(0); CN(1);
        SB();
        G0 = MFMA32(as[1], wB[1][0], G0);
        MU(6); MU(7); CD(4); CD(5); CN(2);
        SB();
        G0 = MFMA32(as[2], wB[2][0], G0);
        MU(8); MU(9); MU(10); MU(11); CN(3); CD(6);
        SB();
        G0 = MFMA32(as[3], wB[3][0], G0);
        ga[0] = XG(0); ga[1] = XG(1);
        MU(12); MU(13); CN(4); CN(5); CD(7);
        SB();
        Y[1] = MFMA32(S[0], BC(pd[0]), Y[1]);
        bv[0] = wvl[0 * 64];
        ga[2] = XG(2); ga[3] = XG(3);
        MU(14); MU(15); CN(6);
        SB();
        Y[3] = MFMA32(S[0], BC(pn[0]), Y[3]);
        bv[1] = wvl[2 * 64];
        ga[4] = (eo & ~31u) | ec0;
        CN(7);
        SB();
      }
    }
    *(volatile LAS int*)pown = 0;
    Y[1] = MFMA32(S[1], BC(pd[1]), Y[1]);
    Y[3] = MFMA32(S[1], BC(pn[1]), Y[3]);
    if (!more) break;
  }
  u32x4 xr[4];
#pragma unroll
  for (int q = 0; q < 4; ++q) {
    const int row = q * 8 + (lane >> 3);
    xr[q] = *(const LAS u32x4*)(ring + rs + (row & 3) * 1024 + (row >> 2) * 128 +
                                (((lane & 7) ^ (((row >> 3) & 1) << 2) ^ (row & 3)) * 16));
  }
  f32x16 U0 = zero16, U1 = zero16;
#pragma unroll
  for (int k = 0; k < 4; ++k) {
    U0 = MFMA32(as[k], wvl[(8 + k * 2) * 64], U0);
    U1 = MFMA32(as[k], wvl[(9 + k * 2) * 64], U1);
  }
  asm volatile("s_waitcnt vmcnt(0) lgkmcnt(0)" : "+v"(xr[0]), "+v"(xr[1]), "+v"(xr[2]), "+v"(xr[3]) :: "memory");
  LAS float* zl = (LAS float*)wbase;
#pragma unroll
  for (int i = 0; i < 16; ++i) {
    const int row = (i & 3) + 8 * (i >> 2) + 4 * h;
    const float z0 = U0[i] + Y[2][i] * __builtin_amdgcn_rcpf(Y[0][i] + 1e-6f);
    const float z1 = U1[i] + Y[3][i] * __builtin_amdgcn_rcpf(Y[1][i] + 1e-6f);
    zl[row * 68 + r] = z0 > 0.f ? z0 : 0.f;
    zl[row * 68 + 32 + r] = z1 > 0.f ? z1 : 0.f;
  }
#pragma unroll
  for (int q = 0; q < 4; ++q) {
    const int row = q * 8 + (lane >> 3);
    const f32x4 z0 = *(const LAS f32x4*)(&zl[row * 68 + (lane & 7) * 8]);
    const f32x4 z1 = *(const LAS f32x4*)(&zl[row * 68 + (lane & 7) * 8 + 4]);
    u32x4 o;
    o[0] = pack_bf16(U2F(xr[q][0] << 16) + z0[0], U2F(xr[q][0] & 0xffff0000u) + z0[1]);
    o[1] = pack_bf16(U2F(xr[q][1] << 16) + z0[2], U2F(xr[q][1] & 0xffff0000u) + z0[3]);
    o[2] = pack_bf16(U2F(xr[q][2] << 16) + z1[0], U2F(xr[q][2] & 0xffff0000u) + z1[1]);
    o[3] = pack_bf16(U2F(xr[q][3] << 16) + z1[2], U2F(xr[q][3] & 0xffff0000u) + z1[3]);
    if (row < nodes) __builtin_amdgcn_raw_buffer_store_b128(o, SRD(out, N * 128), (n0 + 8 * row) * 128 + (lane & 7) * 16, 0, 17);
  }
}

extern "C" void kernel_launch(void* const* d_in, const int* in_sizes, int n_in,
                              void* d_out, int out_size, void* d_ws, size_t ws_size,
                              hipStream_t stream) {
  const unsigned short* x = (const unsigned short*)d_in[0];
  const unsigned short* eattr = (const unsigned short*)d_in[1];
  const int* src = (const int*)d_in[2];
  const int* dst = (const int*)d_in[3];
  const unsigned short* wa = (const unsigned short*)d_in[4];
  const unsigned short* wb = (const unsigned short*)d_in[5];
  const unsigned short* wc = (const unsigned short*)d_in[6];
  const unsigned short* wu = (const unsigned short*)d_in[7];
  const unsigned short* wv = (const unsigned short*)d_in[8];
  unsigned short* out = (unsigned short*)d_out;
  const int N = in_sizes[0] / 64;
  const int E = in_sizes[2];
  k_main<<<NCH, 512, 0, stream>>>(src, dst, x, eattr, (char*)d_ws, E, N, out, wa, wb, wc, wu, wv);
}
```

```cpp
#include <hip/hip_runtime.h>
#include <hip/hip_bf16.h>
#include <stdint.h>
#include <stdio.h>

using bf16x8 = __attribute__((ext_vector_type(8))) short;
using f32x16 = __attribute__((ext_vector_type(16))) float;
using u32x4 = __attribute__((ext_vector_type(4))) unsigned;
using u32x2 = __attribute__((ext_vector_type(2))) unsigned;
using i32x4 = __attribute__((ext_vector_type(4))) int;
using f32x4 = __attribute__((ext_vector_type(4))) float;

#define MFMA32(a, b, c) __builtin_amdgcn_mfma_f32_32x32x16_bf16((a), (b), (c), 0, 0, 0)
#define LAS __attribute__((address_space(3)))
#if defined(__HIP_DEVICE_COMPILE__)
#define NOPK __attribute__((target("no-packed-fp32-ops")))
#else
#define NOPK
#endif
#define SRD(p, bytes) __builtin_amdgcn_make_buffer_rsrc((void*)(p), (short)0, (int)(bytes), 0x00020000)
#define LDS_ADD(p, v) __hip_atomic_fetch_add((p), (v), __ATOMIC_RELAXED, __HIP_MEMORY_SCOPE_WORKGROUP)

#define CST(p, v) __builtin_amdgcn_raw_buffer_store_b128((v), rc, (p) * 4, 0, 17)
#define WALL 1
constexpr int GPB = 8;
constexpr int NPB = 196;
constexpr int MAXB = 256;
constexpr int NCH = 256;
constexpr int CELL = 32;
constexpr int CAP = 28;
constexpr int CSTR = 36;
constexpr int OVCAP = 4096;
constexpr int NFRAG = 34;
constexpr size_t W_TC = 0;
constexpr size_t W_OVH = W_TC + (size_t)MAXB * NCH * CELL * 4;
constexpr size_t W_WP = W_OVH + NCH * 32;
constexpr size_t W_OV = W_WP + NFRAG * 1024;
constexpr float NLOG2E = -1.44269504088896340736f;

__global__ __launch_bounds__(1024) void k_part(const int* __restrict__ src, const int* __restrict__ dst,
                                               int E, int nb,
                                               unsigned* __restrict__ TC,
                                               u32x4* __restrict__ OVH, u32x4* __restrict__ OV,
                                               const unsigned short* __restrict__ wa,
                                               const unsigned short* __restrict__ wb,
                                               const unsigned short* __restrict__ wc,
                                               const unsigned short* __restrict__ wu,
                                               const unsigned short* __restrict__ wv,
                                               unsigned short* __restrict__ wpack) {
  __shared__ __attribute__((aligned(16))) unsigned cT[(MAXB + 1) * CSTR];
  __shared__ __attribute__((aligned(16))) unsigned ovl[8];
  __shared__ int cnt[MAXB + 4];
  const int tid = threadIdx.x, lane = tid & 63;
  const int rnd = __builtin_amdgcn_readfirstlane(tid >> 6);
  const int c = blockIdx.x;
  const int e0 = (rnd * NCH + c) * 256 + lane * 4;
  const auto rd = SRD(dst, E * 4), rs = SRD(src, E * 4);

  const i32x4 dd = __builtin_bit_cast(i32x4, __builtin_amdgcn_raw_buffer_load_b128(rd, e0 * 4, 0, 0));
  const i32x4 ss = __builtin_bit_cast(i32x4, __builtin_amdgcn_raw_buffer_load_b128(rs, e0 * 4, 0, 0));
  unsigned wv8[8];
  if (rnd == 15 && c < NFRAG) {
    const int nn = lane & 31, h = lane >> 5;
    const unsigned short* p;
    if (c < 24) {
      const int m = c >> 3, t = (c >> 1) & 3, ct = c & 1;
      p = ((m == 0) ? wb : (m == 1) ? wv : wa) + (32 * h + 8 * t) * 64 + 32 * ct + nn;
    } else if (c < 26) {
      p = wc + (8 * h) * 64 + 32 * (c & 1) + nn;
    } else {
      const int g = c - 26, t = (g >> 1) & 3, ct = g & 1;
      p = wu + (32 * h + 8 * t) * 64 + 32 * ct + nn;
    }
#pragma unroll
    for (int j = 0; j < 8; ++j) wv8[j] = p[j * 64];
  }
  if (tid < 2 * MAXB) *reinterpret_cast<u32x2*>(cT + (tid >> 1) * CSTR + (tid & 1) * 16) = u32x2{0u, 0u};
  else if (tid < 3 * MAXB + 4) cnt[tid - 2 * MAXB] = 0;
  else if (tid < 3 * MAXB + 12) ovl[tid - (3 * MAXB + 4)] = 0x100u;
  __syncthreads();
  if ((rnd * NCH + c) * 256 < E) {
    int bk[4], rk[4];
#pragma unroll
    for (int k = 0; k < 4; ++k) {
      const bool v = e0 + k < E;
      bk[k] = v ? (int)((unsigned)dd[k] / (unsigned)NPB) : MAXB + 1;
      rk[k] = LDS_ADD(&cnt[bk[k]], 1);
    }
    unsigned w[4];
    bool ovf = false;
#pragma unroll
    for (int k = 0; k < 4; ++k) {
      const unsigned s = (unsigned)(dd[k] - bk[k] * NPB);
      w[k] = ((unsigned)ss[k] << 16) | ((s & 7u) << 13) | ((unsigned)(lane * 4 + k) << 5) | (s >> 3);
      const int d = (rk[k] + (rk[k] < CAP / 2 ? 2 : 4)) & 31;
      unsigned* cp = cT + (rk[k] < CAP ? min(bk[k], MAXB) : MAXB) * CSTR;
      cp[d] = w[k];
      __hip_atomic_fetch_or(cp + (d & 16) + ((d >> 3) & 1), (unsigned)rnd << (4 * (d & 7)),
                            __ATOMIC_RELAXED, __HIP_MEMORY_SCOPE_WORKGROUP);
      ovf |= rk[k] >= CAP;
    }
    if (__builtin_amdgcn_ballot_w64(ovf) != 0) {
#pragma unroll 1
      for (int k = 0; k < 4; ++k) {
        const int bq = k == 0 ? bk[0] : k == 1 ? bk[1] : k == 2 ? bk[2] : bk[3];
        const int rq = k == 0 ? rk[0] : k == 1 ? rk[1] : k == 2 ? rk[2] : rk[3];
        const unsigned wq = k == 0 ? w[0] : k == 1 ? w[1] : k == 2 ? w[2] : w[3];
        if (bq < MAXB && rq >= CAP) {
          const int o = LDS_ADD(&cnt[MAXB], 1);
          OV[c * OVCAP + o] = u32x4{wq >> 9, ((unsigned)(e0 + k) << 5) | (wq & 31u), (unsigned)bq, 0u};
          if (o < 4) { ovl[2 * o] = wq; ovl[2 * o + 1] = ((unsigned)rnd << 12) | (unsigned)bq; }
        }
      }
    }
  }
  asm volatile("s_waitcnt lgkmcnt(0)\n\ts_barrier" ::: "memory");
  {
    const auto rc = SRD(TC, MAXB * NCH * CELL * 4);
    const int p = tid & 7;
    const int first = (CAP / 2) * (p >> 2) + 4 * (p & 3) - 2;
#pragma unroll
    for (int q = 0; q < 2; ++q) {
      const int b = (tid >> 3) + q * 128;
      const int m = cnt[b];
      u32x4 v = *reinterpret_cast<const u32x4*>(cT + b * CSTR + 4 * p);
      if ((p & 3) == 0) v[0] |= (unsigned)min(max(m - first - 2, 0), CAP / 2);
      if (b < nb && (WALL || (p & 3) == 0 || first < m)) CST((b * NCH + c) * CELL + 4 * p, v);
    }
    if (tid < 2) {
      u32x4 v = *reinterpret_cast<const u32x4*>(ovl + 4 * tid);
      v[1] |= (unsigned)cnt[MAXB] << 16;
      v[3] |= (unsigned)cnt[MAXB] << 16;
      __builtin_amdgcn_raw_buffer_store_b128(v, SRD(OVH, NCH * 32), c * 32 + tid * 16, 0, 17);
    }
  }
  if (rnd == 15 && c < NFRAG) {
    if (c < 8 || c == 24 || c == 25) {
#pragma unroll
      for (int j = 0; j < 8; ++j) {
        const unsigned u = __float_as_uint(__uint_as_float(wv8[j] << 16) * NLOG2E);
        wv8[j] = (u + 0x7FFFu + ((u >> 16) & 1u)) >> 16;
      }
    }
    const u32x4 v = {wv8[0] | (wv8[1] << 16), wv8[2] | (wv8[3] << 16), wv8[4] | (wv8[5] << 16), wv8[6] | (wv8[7] << 16)};
    __builtin_amdgcn_raw_buffer_store_b128(v, SRD(wpack, NFRAG * 1024), c * 1024 + lane * 16, 0, 17);
  }
}

__device__ __forceinline__ NOPK unsigned pack_bf16(float lo, float hi) {
  typedef __attribute__((ext_vector_type(2))) float f2;
  typedef __attribute__((ext_vector_type(2))) __bf16 b2;
  f2 v = {lo, hi};
  b2 r = __builtin_convertvector(v, b2);
  return __builtin_bit_cast(unsigned, r);
}
#define WGSYNC() do { __builtin_amdgcn_fence(__ATOMIC_RELEASE, "workgroup"); __builtin_amdgcn_s_barrier(); \
                      __builtin_amdgcn_fence(__ATOMIC_ACQUIRE, "workgroup"); } while (0)
#define U2F(u) __builtin_bit_cast(float, (unsigned)(u))
constexpr int RING_SLOT = 5 * 1024;
constexpr int LCAP = 512;
constexpr int LPAD = LCAP + 96;
constexpr int SSCR = 32 * 80;
constexpr int WAVE_LDS = 2 * RING_SLOT;
constexpr int M_WL = 0;
constexpr int M_OH = 16 * 1024;
constexpr int M_CNT = M_OH + 2048;
constexpr int M_ST = M_CNT + 256;
constexpr int M_SC = M_ST + 2048;
constexpr int M_LX = M_SC + GPB * SSCR;
constexpr int M_LE = M_LX + GPB * LPAD * 4;
constexpr int M_WV = M_LE + GPB * LPAD * 4;
constexpr int M_XG = M_WV + 4 * WAVE_LDS;
constexpr int MAIN_LDS = M_WV + GPB * WAVE_LDS;
static_assert(M_LX % 256 == 0 && M_LE % 256 == 0 && M_LE < 65536 && M_LE + GPB * LPAD * 4 + NCH * CELL * 4 <= M_XG, "lists");
static_assert(MAIN_LDS <= 160 * 1024, "LDS");

__global__ __launch_bounds__(512, 2) NOPK void k_main(const unsigned short* __restrict__ x,
                                                      const unsigned short* __restrict__ eattr,
                                                      const char* __restrict__ wsp, int E,
                                                      int N, unsigned short* __restrict__ out) {
  __shared__ __attribute__((aligned(16))) char smem[MAIN_LDS];
  asm volatile("" :: "s"(x), "s"(eattr), "s"(wsp), "s"(E), "s"(N), "s"(out));
  const unsigned* TC = reinterpret_cast<const unsigned*>(wsp + W_TC);
  const u32x4* OVH = reinterpret_cast<const u32x4*>(wsp + W_OVH);
  const u32x4* OV = reinterpret_cast<const u32x4*>(wsp + W_OV);
  const char* wpack = wsp + W_WP;
  const int tid = threadIdx.x, lane = tid & 63;
  const int wave = __builtin_amdgcn_readfirstlane(tid >> 6);
  const int r = lane & 31, h = lane >> 5;
  const int b = blockIdx.x;

  const int pc = lane & 7;
  u32x4 tv[4], ovh;
  {
    const unsigned* cp = TC + ((size_t)b * NCH + wave * 32) * CELL;
    asm volatile("global_load_dwordx4 %0, %5, %6\n\tglobal_load_dwordx4 %1, %5, %6 offset:1024\n\t"
                 "global_load_dwordx4 %2, %5, %6 offset:2048\n\tglobal_load_dwordx4 %3, %5, %6 offset:3072\n\t"
                 "global_load_dwordx4 %4, %5, %7"
                 : "=&v"(tv[0]), "=&v"(tv[1]), "=&v"(tv[2]), "=&v"(tv[3]), "=&v"(ovh)
                 : "v"(lane * 16), "s"(cp), "s"(OVH + wave * 64) : "memory");
  }
#define GLDS(vo, rs, mv) asm volatile("s_mov_b32 m0, %2\n\ts_nop 0\n\tbuffer_load_dwordx4 %0, %1, 0 offen lds" \
                                      :: "v"(vo), "s"(rs), "s"(mv) : "memory")
  const auto rx = SRD(x, N * 128);
  const auto re = SRD(eattr, E * 32);
  const unsigned xc0 = (unsigned)(((lane & 7) ^ ((lane >> 2) & 4)) * 16);
  const unsigned lb = (unsigned)(size_t)(LAS char*)smem;
  LAS int* cnt = (LAS int*)(smem + M_CNT);
  if (tid < 32) cnt[tid] = (tid & 3) == 0 ? (tid >> 2) * (LPAD * 4) : (tid & 3) == 2 ? 127 : 0;
  __builtin_amdgcn_s_setprio(1);
  {
    unsigned* oh = reinterpret_cast<unsigned*>(smem + M_OH);
    const int i = tid;
    const int sl = i >> 4, hh = (i >> 3) & 1, ks = (i >> 2) & 1, jp = i & 3;
    const int j0 = 2 * jp, j1 = 2 * jp + 1;
    const int nd0 = 16 * ks + 8 * (j0 >> 2) + 4 * hh + (j0 & 3);
    const int nd1 = 16 * ks + 8 * (j1 >> 2) + 4 * hh + (j1 & 3);
    oh[i] = (sl == nd0 ? 0x3F80u : 0u) | (sl == nd1 ? 0x3F800000u : 0u);
  }
  asm volatile("s_waitcnt lgkmcnt(0)\n\ts_barrier" ::: "memory");
  {
    const int nw = (min(NPB - wave, N - b * NPB - wave) + 7) >> 3;
#pragma unroll
    for (int k = 0; k < 4; ++k) {
      const int s = (lane >> 3) * 4 + k;
      const unsigned xo = s < nw ? (unsigned)(b * NPB + wave + 8 * s) << 7 : 0xFFFFFF80u;
      GLDS(xo | (xc0 ^ (16u * k)), rx, lb + M_XG + wave * 4096 + k * 1024);
    }
  }
  const LAS bf16x8* wl = (const LAS bf16x8*)(smem + M_WL);
  const LAS bf16x8* ohl = (const LAS bf16x8*)(smem + M_OH);
  LAS unsigned* lxa = (LAS unsigned*)(smem + M_LX);
  LAS unsigned* lea = (LAS unsigned*)(smem + M_LE);
  char* wbase = smem + M_WV + wave * WAVE_LDS;
  const unsigned ring_a = (unsigned)(size_t)(LAS char*)wbase;
  {
    unsigned long long vm[16];
    unsigned ad[16], xe[16], hw[4];
    const unsigned four = 4u;
#pragma unroll
    for (int k = 0; k < 4; ++k) {
      asm volatile("s_waitcnt vmcnt(%1)" : "+v"(tv[k]) : "n"(8 - k) : "memory");
      const unsigned h0 = (unsigned)__builtin_amdgcn_mov_dpp((int)tv[k][0], 0x00, 0xF, 0xF, true);
      const unsigned h1 = (unsigned)__builtin_amdgcn_mov_dpp((int)tv[k][1], 0x00, 0xF, 0xF, true);
      hw[k] = ((lane & 2) ? h1 : h0) >> ((lane & 1) * 16);
      const unsigned m = h0 & 0xFFu;
      unsigned ci[4];
#pragma unroll
      for (int j = 0; j < 4; ++j) {
        vm[4 * k + j] = __builtin_amdgcn_ballot_w64((unsigned)((lane & 3) * 4 + j - 2) < m);
        xe[4 * k + j] = tv[k][j] >> 9;
        ci[j] = (xe[4 * k + j] & 0x70u) + lb;
      }
      asm volatile("s_mov_b64 exec, %4\n\tds_add_rtn_u32 %0, %8, %12 offset:%13\n\t"
                   "s_mov_b64 exec, %5\n\tds_add_rtn_u32 %1, %9, %12 offset:%13\n\t"
                   "s_mov_b64 exec, %6\n\tds_add_rtn_u32 %2, %10, %12 offset:%13\n\t"
                   "s_mov_b64 exec, %7\n\tds_add_rtn_u32 %3, %11, %12 offset:%13\n\t"
                   "s_mov_b64 exec, -1"
                   : "=&v"(ad[4 * k]), "=&v"(ad[4 * k + 1]), "=&v"(ad[4 * k + 2]), "=&v"(ad[4 * k + 3])
                   : "s"(vm[4 * k]), "s"(vm[4 * k + 1]), "s"(vm[4 * k + 2]), "s"(vm[4 * k + 3]),
                     "v"(ci[0]), "v"(ci[1]), "v"(ci[2]), "v"(ci[3]), "v"(four), "n"(M_CNT)
                   : "memory");
    }
#pragma unroll
    for (int k = 0; k < 4; ++k) {
      const unsigned c13 = (unsigned)(wave * 32 + k * 8 + (lane >> 3)) << 13;
      unsigned ee[4];
#pragma unroll
      for (int j = 0; j < 4; ++j) ee[j] = (tv[k][j] & 0x1FFFu) | ((((hw[k] >> (4 * j)) & 15u) << 21) | c13);
      asm volatile("s_waitcnt lgkmcnt(12)\n\t"
                   "s_mov_b64 exec, %0\n\tds_write2st64_b32 %4, %8, %12 offset0:%16 offset1:%17\n\t"
                   "s_mov_b64 exec, %1\n\tds_write2st64_b32 %5, %9, %13 offset0:%16 offset1:%17\n\t"
                   "s_mov_b64 exec, %2\n\tds_write2st64_b32 %6, %10, %14 offset0:%16 offset1:%17\n\t"
                   "s_mov_b64 exec, %3\n\tds_write2st64_b32 %7, %11, %15 offset0:%16 offset1:%17\n\t"
                   "s_mov_b64 exec, -1"
                   :: "s"(vm[4 * k]), "s"(vm[4 * k + 1]), "s"(vm[4 * k + 2]), "s"(vm[4 * k + 3]),
                      "v"(ad[4 * k]), "v"(ad[4 * k + 1]), "v"(ad[4 * k + 2]), "v"(ad[4 * k + 3]),
                      "v"(xe[4 * k]), "v"(xe[4 * k + 1]), "v"(xe[4 * k + 2]), "v"(xe[4 * k + 3]),
                      "v"(ee[0]), "v"(ee[1]), "v"(ee[2]), "v"(ee[3]), "n"(M_LX / 256), "n"(M_LE / 256)
                   : "memory");
    }
  }
    {
      const auto rw = SRD(wpack, NFRAG * 1024);
      const unsigned vo = (unsigned)(wave * 1024 + lane * 16);
      GLDS(vo + 16384u, rw, lb + M_SC + 8192 + wave * 1024);
      GLDS(vo, rw, lb + M_SC + wave * 1024);
      asm volatile("s_mov_b32 m0, %2\n\ts_nop 0\n\tbuffer_load_dword %0, %1, 0 offen lds"
                   :: "v"(24576u + (vo >> 2)), "s"(rw), "s"(lb + M_SC + 16384 + wave * 256) : "memory");
      GLDS(vo + 8192u, rw, lb + M_WL + wave * 1024);
      GLDS(vo + 26624u, rw, lb + M_WL + 8192 + wave * 1024);
    }
  asm volatile("s_waitcnt vmcnt(9)" : "+v"(ovh) :: "memory");
  if (__builtin_amdgcn_ballot_w64((ovh[1] & 0x100u) == 0u) != 0) {
#pragma unroll
    for (int q = 0; q < 2; ++q) {
      const unsigned w = ovh[2 * q], m = ovh[2 * q + 1];
      if ((m & 0x1FFu) == (unsigned)b) {
        LAS unsigned* p = (LAS unsigned*)((LAS char*)lxa + LDS_ADD((LAS int*)((LAS char*)cnt + ((w >> 9) & 0x70u)), 4));
        p[0] = w >> 9;
        p[GPB * LPAD] = (w & 0x1FFFu) | ((m & 0xF000u) << 9) | ((unsigned)(tid >> 1) << 13);
      }
    }
    if ((ovh[1] >> 16) > 4u) cnt[1] = 1;
  }
  asm volatile("s_waitcnt lgkmcnt(0)\n\ts_barrier" ::: "memory");
  const int cpc = cnt[4 * pc] - pc * (LPAD * 4);
  const int cpa = cnt[4 * (lane >> 3)] - (lane >> 3) * (LPAD * 4);
  bool ready = __builtin_amdgcn_ballot_w64(cpc > LCAP * 4) == 0;
  int grp;
  {
    const unsigned long long gt = __builtin_amdgcn_ballot_w64(cpc * 2 + pc > cpa * 2 + (lane >> 3));
    const unsigned wd = h ? (unsigned)(gt >> 32) : (unsigned)gt;
    const int rk = __builtin_popcount((wd >> (r & 24)) & 0xFFu);
    grp = __builtin_ctzll(__builtin_amdgcn_ballot_w64(rk == (wave < 4 ? wave : 11 - wave))) >> 3;
  }
  const int gn0 = b * NPB + grp;
  const int nodes = max((min(NPB - grp, N - gn0) + 7) >> 3, 0);
  const int n0 = min(gn0, N - 1);
  ((LAS unsigned*)(smem + M_ST))[tid] = (ready ? 0x40000u : 0xFFFFu) | ((unsigned)(tid & 1) << 16);

  f32x16 Y[4];
#pragma unroll
  for (int q = 0; q < 4; ++q)
#pragma unroll
    for (int i = 0; i < 16; ++i) Y[q][i] = 0.f;

  const unsigned ec0 = (unsigned)(((lane & 1) ^ ((lane >> 4) & 1)) * 16);
  const LAS unsigned* lx = lxa + grp * LPAD;
  const LAS unsigned* le = lea + grp * LPAD;
  const int ohoff = ((((r >> 2) & 1) * 16) + ((r >> 4) * 8) + (((r >> 3) & 1) * 4) + (r & 3)) * 2;
  const int xa0 = (r & 3) * 1024 + (r >> 2) * 128 + (((r & 3) | ((h ^ ((r >> 3) & 1)) << 2)) * 16);
  const int ea0 = 4096 + r * 32 + ((h ^ ((r >> 3) & 1)) * 16);
#define SB() __builtin_amdgcn_sched_barrier(0)
  bf16x8 wB[4][2], wC[2], XAf[2][2];
  const LAS bf16x8* wvl = wl + lane;
  f32x16 G0, G1, V;
  u32x4 pd[2], pn[2];
  bf16x8 S[2], St[2], as[4], ae, bv[4];
  const f32x16 zero16 = {0.f, 0.f, 0.f, 0.f, 0.f, 0.f, 0.f, 0.f, 0.f, 0.f, 0.f, 0.f, 0.f, 0.f, 0.f, 0.f};
  LAS char* sm = (LAS char*)(smem + M_SC + wave * SSCR);
  LAS u32x4* mine = (LAS u32x4*)(sm + r * 80 + h * 32);
  const LAS char* ring = (const LAS char*)wbase;
  unsigned ga[5];
  unsigned slr;
  int sl;
  int rs = 0;

#define E1(i) Gc[i] = __builtin_amdgcn_exp2f(Gc[i])
#define E2(i) Gc[i] = Gc[i] + 1.0f
#define E3(i) Gc[i] = __builtin_amdgcn_rcpf(Gc[i])
#define MU(i) V[i] = V[i] * Gc[i]
#define CD(j) pd[(j) >> 2][(j) & 3] = pack_bf16(Gc[2 * (j)], Gc[2 * (j) + 1])
#define CN(j) pn[(j) >> 2][(j) & 3] = pack_bf16(V[2 * (j)], V[2 * (j) + 1])
#define XG(k) ((xo[k] & ~127u) | (xc0 ^ (16u * (k))))
#define BC(v) __builtin_bit_cast(bf16x8, v)

  int more, ntiles;
  LAS int* pown = cnt + 4 * wave + 2;
  const LAS int* poth = cnt + 4 * (wave ^ 4) + 2;
  const LAS unsigned* lxp = lx + (lane >> 3) * 4;
  const LAS unsigned* lep = le + (lane >> 1);
  const LAS unsigned* lsp = le + r;
  auto round_begin = [&]() NOPK __attribute__((always_inline)) {
    const int cn = min((__builtin_amdgcn_readfirstlane(cnt[4 * grp]) - grp * (LPAD * 4)) >> 2, LCAP);
    more = __builtin_amdgcn_readfirstlane(cnt[1]);
    ntiles = (cn + 31) >> 5;
    {
      LAS unsigned* wx = lxa + grp * LPAD;
      LAS unsigned* we = lea + grp * LPAD;
      const int e0 = ntiles * 32;
      if (cn + r < e0) { wx[cn + r] = 0xFFFFFF80u; we[cn + r] = 0xFFFFFFFFu; }
      wx[e0 + lane] = lane < nodes ? (unsigned)(n0 + 8 * lane) << 7 : 0xFFFFFF80u;
      we[e0 + lane] = 0xFFFFFFFFu;
      wx[e0 + 64 + r] = 0xFFFFFF80u;
      we[e0 + 64 + r] = 0xFFFFFFFFu;
    }
  };
  auto gather = [&](int t, bool issue) NOPK __attribute__((always_inline)) {
    const u32x4 xo = *(const LAS u32x4*)(lxp + t * 32);
    const unsigned eo = lep[t * 32];
    ga[0] = XG(0); ga[1] = XG(1); ga[2] = XG(2); ga[3] = XG(3);
    ga[4] = (eo & ~31u) | ec0;
    if (issue) {
      const unsigned d = ring_a + (rs ^ (t * RING_SLOT));
      GLDS(ga[0], rx, d); GLDS(ga[1], rx, d + 1024); GLDS(ga[2], rx, d + 2048); GLDS(ga[3], rx, d + 3072);
      GLDS(ga[4], re, d + 4096);
    }
  };
  if (ready) {
    round_begin();
    if (wave < 4) gather(0, true);
  }
  asm volatile("s_cmp_lg_u32 %0, 0\n\ts_cbranch_scc1 1f\n\ts_waitcnt vmcnt(0)\n1:\n\ts_waitcnt vmcnt(5)\n\ts_barrier"
               :: "s"(__builtin_amdgcn_readfirstlane((ready && wave < 4) ? 1 : 0)) : "memory", "scc");
  {
      const LAS bf16x8* ws = (const LAS bf16x8*)(smem + M_SC) + lane;
      bf16x8 xg[4];
#pragma unroll
      for (int k = 0; k < 4; ++k) xg[k] = *(const LAS bf16x8*)(smem + M_XG + grp * 4096 + (xa0 ^ (16 * k)));
#pragma unroll
      for (int ct = 0; ct < 2; ++ct) {
        f32x16 A = zero16;
#pragma unroll
        for (int k = 0; k < 4; ++k) A = MFMA32(xg[k], ws[(8 + k * 2 + ct) * 64], A);
#pragma unroll
        for (int s = 0; s < 2; ++s) {
          u32x4 p;
#pragma unroll
          for (int j = 0; j < 4; ++j) p[j] = pack_bf16(A[8 * s + 2 * j] * NLOG2E, A[8 * s + 2 * j + 1] * NLOG2E);
          XAf[s][ct] = BC(p);
        }
      }
#pragma unroll
      for (int k = 0; k < 8; ++k) wB[k >> 1][k & 1] = ws[k * 64];
      wC[0] = ws[16 * 64];
      wC[1] = ws[17 * 64];
      asm volatile("s_waitcnt lgkmcnt(0)\n\ts_barrier"
                   : "+v"(wB[0][0]), "+v"(wB[0][1]), "+v"(wB[1][0]), "+v"(wB[1][1]), "+v"(wB[2][0]), "+v"(wB[2][1]),
                     "+v"(wB[3][0]), "+v"(wB[3][1]), "+v"(wC[0]), "+v"(wC[1]) :: "memory");
      const u32x4 z4 = {0u, 0u, 0u, 0u};
      mine[0] = z4;
      mine[1] = z4;
    }
  for (;;) {
   if (!ready) {
    asm volatile("s_waitcnt vmcnt(0)" ::: "memory");
    WGSYNC();
    {
      int t2 = (int)__builtin_amdgcn_mbcnt_hi(~0u, __builtin_amdgcn_mbcnt_lo(~0u, 0u));
      asm volatile("" : "+v"(t2));
      const int l2 = t2;
      t2 += wave * 64;
      if (t2 < 32) cnt[t2] = (t2 & 3) == 0 ? (t2 >> 2) * (LPAD * 4) : 0;
      WGSYNC();
      auto put = [&](unsigned t, unsigned e) NOPK -> bool {
        const unsigned gg = (t >> 4) & 7u;
        const int ad = LDS_ADD(&cnt[4 * gg], 4) >> 2;
        if (ad - (int)gg * LPAD >= LCAP) return false;
        lxa[ad] = t;
        lea[ad] = e;
        return true;
      };
      LAS unsigned* stp = (LAS unsigned*)(smem + M_ST) + t2;
      unsigned pe = *stp & 0xFFFFu, ocur = *stp >> 16;
#pragma unroll 1
      for (int j = 0; j < 16; ++j) {
        if ((pe >> j) & 1u) {
          const int cc = wave * 32 + (j >> 2) * 8 + (l2 >> 3);
          const unsigned* cp = TC + ((size_t)b * NCH + cc) * CELL;
          const int d = (l2 & 7) * 4 + (j & 3);
          const unsigned w = cp[d];
          const unsigned nib = (cp[(d & 16) + ((d >> 3) & 1)] >> (4 * (d & 7))) & 15u;
          if ((unsigned)((d & 15) - 2) >= (cp[d & 16] & 0xFFu) ||
              put(w >> 9, (w & 0x1FFFu) | (nib << 21) | ((unsigned)cc << 13))) pe &= ~(1u << j);
        }
      }
      const unsigned on = OVH[t2][1] >> 16;
      const size_t ob = (size_t)(t2 >> 1) * OVCAP;
#pragma unroll 1
      for (; ocur < on; ocur += 2) {
        const u32x4 v = OV[ob + ocur];
        if ((int)v[2] == b && !put(v[0], v[1])) break;
      }
      if (pe != 0u || ocur < on) cnt[1] = 1;
      *stp = pe | (ocur << 16);
    }
    WGSYNC();
    round_begin();
   }
    if (!ready || wave >= 4) gather(0, true);
    gather(1, true);
    gather(2, false);
    ready = false;
    sl = lsp[0] & 31u;
    St[0] = ohl[sl * 4 + h * 2 + 0];
    St[1] = ohl[sl * 4 + h * 2 + 1];
    bv[0] = wvl[0 * 64];
    bv[1] = wvl[2 * 64];
    asm volatile("s_waitcnt vmcnt(5)" ::: "memory");
#pragma unroll
    for (int k = 0; k < 4; ++k) as[k] = *(const LAS bf16x8*)(ring + rs + (xa0 ^ (16 * k)));
    ae = *(const LAS bf16x8*)(ring + rs + ea0);
    G0 = MFMA32(St[0], XAf[0][0], zero16);
    G0 = MFMA32(St[1], XAf[1][0], G0);
    G0 = MFMA32(ae, wC[0], G0);
#pragma unroll
    for (int k = 0; k < 4; ++k) G0 = MFMA32(as[k], wB[k][0], G0);
    pd[1] = u32x4{0u, 0u, 0u, 0u};
    pn[1] = u32x4{0u, 0u, 0u, 0u};
    S[1] = BC(pd[1]);
    SB();
    for (int i = 0; i < ntiles; ++i) {
      {
        f32x16& Gc = G0;
        const unsigned d = ring_a + rs;
        LAS unsigned short* ohw = (LAS unsigned short*)(sm + sl * 80 + ohoff);
        Y[1] = MFMA32(S[1], BC(pd[1]), Y[1]);
        bv[2] = wvl[4 * 64];
        *(volatile LAS int*)pown = ntiles - i;
        E1(0); E1(1); E1(2); E1(3);
        SB();
        Y[3] = MFMA32(S[1], BC(pn[1]), Y[3]);
        bv[3] = wvl[6 * 64];
        E2(0); E2(1); E1(4); E1(5); E2(2); E2(3);
        SB();
        V = MFMA32(as[0], bv[0], zero16);
        const int prem = *(volatile const LAS int*)poth;
        E3(0); E3(1); E1(6); E1(7); E2(4); E2(5);
        SB();
        V = MFMA32(as[1], bv[1], V);
        E3(2); E3(3); E1(8); E1(9); E2(6); E2(7);
        SB();
        V = MFMA32(as[2], bv[2], V);
        E3(4); E3(5); E1(10); E1(11); E2(8); E2(9); CD(0);
        SB();
        V = MFMA32(as[3], bv[3], V);
        E3(6); E3(7); E1(12); E1(13); E2(10); E2(11); CD(1);
        SB();
        G1 = MFMA32(St[0], XAf[0][1], zero16);
        asm volatile("s_cmp_ge_i32 %0, %1\n\ts_setprio 0\n\ts_cbranch_scc0 1f\n\ts_setprio 1\n1:"
                     :: "s"(ntiles - i), "s"(__builtin_amdgcn_readfirstlane(prem)) : "scc");
        E3(8); E3(9); E1(14); E1(15); E2(12); E2(13); CD(2);
        SB();
        G1 = MFMA32(St[1], XAf[1][1], G1);
        *ohw = (unsigned short)0x3F80;
        asm volatile("" ::: "memory");
        S[0] = BC(mine[0]);
        S[1] = BC(mine[1]);
        asm volatile("" ::: "memory");
        *ohw = (unsigned short)0;
        E3(10); E3(11); E2(14); E2(15); CD(3);
        SB();
        G1 = MFMA32(ae, wC[1], G1);
        GLDS(ga[0], rx, d);
        E3(12); E3(13); MU(0); MU(1); MU(2); MU(3);
        SB();
        G1 = MFMA32(as[0], wB[0][1], G1);
        GLDS(ga[1], rx, d + 1024);
        E3(14); E3(15); MU(4); MU(5); CN(0); CN(1);
        SB();
        G1 = MFMA32(as[1], wB[1][1], G1);
        GLDS(ga[2], rx, d + 2048);
        MU(6); MU(7); CD(4); CD(5); CN(2);
        SB();
        G1 = MFMA32(as[2], wB[2][1], G1);
        GLDS(ga[3], rx, d + 3072);
        MU(8); MU(9); MU(10); MU(11); CN(3); CD(6);
        SB();
        G1 = MFMA32(as[3], wB[3][1], G1);
        GLDS(ga[4], re, d + 4096);
        slr = lsp[(i + 1) * 32];
        MU(12); MU(13); CN(4); CN(5); CD(7);
        SB();
        Y[0] = MFMA32(S[0], BC(pd[0]), Y[0]);
        bv[0] = wvl[1 * 64];
        MU(14); MU(15); CN(6);
        SB();
        Y[2] = MFMA32(S[0], BC(pn[0]), Y[2]);
        bv[1] = wvl[3 * 64];
        CN(7);
        SB();
      }
      rs ^= RING_SLOT;
      {
        f32x16& Gc = G1;
        const LAS char* rp = ring + rs;
        sl = slr & 31u;
        Y[0] = MFMA32(S[1], BC(pd[1]), Y[0]);
        bv[2] = wvl[5 * 64];
        St[0] = ohl[sl * 4 + h * 2 + 0];
        E1(0); E1(1); E1(2); E1(3);
        SB();
        Y[2] = MFMA32(S[1], BC(pn[1]), Y[2]);
        bv[3] = wvl[7 * 64];
        St[1] = ohl[sl * 4 + h * 2 + 1];
        E2(0); E2(1); E1(4); E1(5); E2(2); E2(3);
        SB();
        V = MFMA32(as[0], bv[0], zero16);
        E3(0); E3(1); E1(6); E1(7); E2(4); E2(5);
        SB();
        V = MFMA32(as[1], bv[1], V);
        E3(2); E3(3); E1(8); E1(9); E2(6); E2(7);
        SB();
        V = MFMA32(as[2], bv[2], V);
        asm volatile("s_waitcnt vmcnt(5)" ::: "memory");
        ae = *(const LAS bf16x8*)(rp + ea0);
        E3(4); E3(5); E1(10); E1(11); E2(8); E2(9); CD(0);
        SB();
        V = MFMA32(as[3], bv[3], V);
        as[0] = *(const LAS bf16x8*)(rp + xa0);
        E3(6); E3(7); E1(12); E1(13); E2(10); E2(11); CD(1);
        SB();
        G0 = MFMA32(St[0], XAf[0][0], zero16);
        as[1] = *(const LAS bf16x8*)(rp + (xa0 ^ 16));
        E3(8); E3(9); E1(14); E1(15); E2(12); E2(13); CD(2);
        SB();
        G0 = MFMA32(St[1], XAf[1][0], G0);
        as[2] = *(const LAS bf16x8*)(rp + (xa0 ^ 32));
        E3(10); E3(11); E2(14); E2(15); CD(3);
        SB();
        G0 = MFMA32(ae, wC[0], G0);
        as[3] = *(const LAS bf16x8*)(rp + (xa0 ^ 48));
        E3(12); E3(13); MU(0); MU(1); MU(2); MU(3);
        SB();
        G0 = MFMA32(as[0], wB[0][0], G0);
        const u32x4 xo = *(const LAS u32x4*)(lxp + (i + 3) * 32);
        const unsigned eo = lep[(i + 3) * 32];
        E3(14); E3(15); MU(4); MU(5); CN(0); CN(1);
        SB();
        G0 = MFMA32(as[1], wB[1][0], G0);
        MU(6); MU(7); CD(4); CD(5); CN(2);
        SB();
        G0 = MFMA32(as[2], wB[2][0], G0);
        MU(8); MU(9); MU(10); MU(11); CN(3); CD(6);
        SB();
        G0 = MFMA32(as[3], wB[3][0], G0);
        ga[0] = XG(0); ga[1] = XG(1);
        MU(12); MU(13); CN(4); CN(5); CD(7);
        SB();
        Y[1] = MFMA32(S[0], BC(pd[0]), Y[1]);
        bv[0] = wvl[0 * 64];
        ga[2] = XG(2); ga[3] = XG(3);
        MU(14); MU(15); CN(6);
        SB();
        Y[3] = MFMA32(S[0], BC(pn[0]), Y[3]);
        bv[1] = wvl[2 * 64];
        ga[4] = (eo & ~31u) | ec0;
        CN(7);
        SB();
      }
    }
    *(volatile LAS int*)pown = 0;
    Y[1] = MFMA32(S[1], BC(pd[1]), Y[1]);
    Y[3] = MFMA32(S[1], BC(pn[1]), Y[3]);
    if (!more) break;
  }
  u32x4 xr[4];
#pragma unroll
  for (int q = 0; q < 4; ++q) {
    const int row = q * 8 + (lane >> 3);
    xr[q] = *(const LAS u32x4*)(ring + rs + (row & 3) * 1024 + (row >> 2) * 128 +
                                (((lane & 7) ^ (((row >> 3) & 1) << 2) ^ (row & 3)) * 16));
  }
  f32x16 U0 = zero16, U1 = zero16;
#pragma unroll
  for (int k = 0; k < 4; ++k) {
    U0 = MFMA32(as[k], wvl[(8 + k * 2) * 64], U0);
    U1 = MFMA32(as[k], wvl[(9 + k * 2) * 64], U1);
  }
  asm volatile("s_waitcnt vmcnt(0) lgkmcnt(0)" : "+v"(xr[0]), "+v"(xr[1]), "+v"(xr[2]), "+v"(xr[3]) :: "memory");
  LAS float* zl = (LAS float*)wbase;
#pragma unroll
  for (int i = 0; i < 16; ++i) {
    const int row = (i & 3) + 8 * (i >> 2) + 4 * h;
    const float z0 = U0[i] + Y[2][i] * __builtin_amdgcn_rcpf(Y[0][i] + 1e-6f);
    const float z1 = U1[i] + Y[3][i] * __builtin_amdgcn_rcpf(Y[1][i] + 1e-6f);
    zl[row * 68 + r] = z0 > 0.f ? z0 : 0.f;
    zl[row * 68 + 32 + r] = z1 > 0.f ? z1 : 0.f;
  }
#pragma unroll
  for (int q = 0; q < 4; ++q) {
    const int row = q * 8 + (lane >> 3);
    const f32x4 z0 = *(const LAS f32x4*)(&zl[row * 68 + (lane & 7) * 8]);
    const f32x4 z1 = *(const LAS f32x4*)(&zl[row * 68 + (lane & 7) * 8 + 4]);
    u32x4 o;
    o[0] = pack_bf16(U2F(xr[q][0] << 16) + z0[0], U2F(xr[q][0] & 0xffff0000u) + z0[1]);
    o[1] = pack_bf16(U2F(xr[q][1] << 16) + z0[2], U2F(xr[q][1] & 0xffff0000u) + z0[3]);
    o[2] = pack_bf16(U2F(xr[q][2] << 16) + z1[0], U2F(xr[q][2] & 0xffff0000u) + z1[1]);
    o[3] = pack_bf16(U2F(xr[q][3] << 16) + z1[2], U2F(xr[q][3] & 0xffff0000u) + z1[3]);
    if (row < nodes) __builtin_amdgcn_raw_buffer_store_b128(o, SRD(out, N * 128), (n0 + 8 * row) * 128 + (lane & 7) * 16, 0, 17);
  }
}

extern "C" void kernel_launch(void* const* d_in, const int* in_sizes, int n_in,
                              void* d_out, int out_size, void* d_ws, size_t ws_size,
                              hipStream_t stream) {
  const unsigned short* x = (const unsigned short*)d_in[0];
  const unsigned short* eattr = (const unsigned short*)d_in[1];
  const int* src = (const int*)d_in[2];
  const int* dst = (const int*)d_in[3];
  const unsigned short* wa = (const unsigned short*)d_in[4];
  const unsigned short* wb = (const unsigned short*)d_in[5];
  const unsigned short* wc = (const unsigned short*)d_in[6];
  const unsigned short* wu = (const unsigned short*)d_in[7];
  const unsigned short* wv = (const unsigned short*)d_in[8];
  unsigned short* out = (unsigned short*)d_out;
  for (int i = 0; i < 11; ++i) {
    void* p = i < 9 ? d_in[i] : i == 9 ? d_out : d_ws;
    hipDeviceptr_t base = 0; size_t sz = 0;
    hipError_t e = hipMemGetAddressRange(&base, &sz, (hipDeviceptr_t)p);
    hipPointerAttribute_t at; hipError_t e2 = hipPointerGetAttributes(&at, p);
    fprintf(stderr, "buf %d %p base %p size %zu err %d type %d flags %u err %d\n", i, p, (void*)base, sz, (int)e, (int)at.type, at.allocationFlags, (int)e2);
  }
  const int N = in_sizes[0] / 64;
  const int E = in_sizes[2];
  const int nb = (N + NPB - 1) / NPB;

  char* ws = (char*)d_ws;
  k_part<<<NCH, 1024, 0, stream>>>(src, dst, E, nb, (unsigned*)(ws + W_TC), (u32x4*)(ws + W_OVH), (u32x4*)(ws + W_OV),
                                   wa, wb, wc, wu, wv, (unsigned short*)(ws + W_WP));
  k_main<<<nb, 512, 0, stream>>>(x, eattr, ws, E, N, out);
}
```

```cpp
#include <hip/hip_runtime.h>
#include <hip/hip_bf16.h>
#include <stdint.h>

using bf16x8 = __attribute__((ext_vector_type(8))) short;
using f32x16 = __attribute__((ext_vector_type(16))) float;
using u32x4 = __attribute__((ext_vector_type(4))) unsigned;
using u32x2 = __attribute__((ext_vector_type(2))) unsigned;
using i32x4 = __attribute__((ext_vector_type(4))) int;
using f32x4 = __attribute__((ext_vector_type(4))) float;

#define MFMA32(a, b, c) __builtin_amdgcn_mfma_f32_32x32x16_bf16((a), (b), (c), 0, 0, 0)
#define LAS __attribute__((address_space(3)))
#if defined(__HIP_DEVICE_COMPILE__)
#define NOPK __attribute__((target("no-packed-fp32-ops")))
#else
#define NOPK
#endif
#define SRD(p, bytes) __builtin_amdgcn_make_buffer_rsrc((void*)(p), (short)0, (int)(bytes), 0x00020000)
#define LDS_ADD(p, v) __hip_atomic_fetch_add((p), (v), __ATOMIC_RELAXED, __HIP_MEMORY_SCOPE_WORKGROUP)

#define CST(p, v) __builtin_amdgcn_raw_buffer_store_b128((v), rc, (p) * 4, 0, 17)
#define WALL 1
constexpr int GPB = 8;
constexpr int NPB = 196;
constexpr int MAXB = 256;
constexpr int NCH = 256;
constexpr int CELL = 32;
constexpr int CAP = 28;
constexpr int CSTR = 36;
constexpr int OVCAP = 4096;
constexpr int NFRAG = 34;
constexpr size_t W_TC = 0;
constexpr size_t W_OVH = W_TC + (size_t)MAXB * NCH * CELL * 4;
constexpr size_t W_WP = W_OVH + NCH * 32;
constexpr size_t W_OV = W_WP + NFRAG * 1024;
constexpr float NLOG2E = -1.44269504088896340736f;

__global__ __launch_bounds__(1024) void k_part(const int* __restrict__ src, const int* __restrict__ dst,
                                               int E, int nb,
                                               unsigned* __restrict__ TC,
                                               u32x4* __restrict__ OVH, u32x4* __restrict__ OV,
                                               const unsigned short* __restrict__ wa,
                                               const unsigned short* __restrict__ wb,
                                               const unsigned short* __restrict__ wc,
                                               const unsigned short* __restrict__ wu,
                                               const unsigned short* __restrict__ wv,
                                               unsigned short* __restrict__ wpack) {
  __shared__ __attribute__((aligned(16))) unsigned cT[(MAXB + 1) * CSTR];
  __shared__ __attribute__((aligned(16))) unsigned ovl[8];
  __shared__ int cnt[MAXB + 4];
  const long long tp0 = __builtin_amdgcn_s_memrealtime();
  const int tid = threadIdx.x, lane = tid & 63;
  const int rnd = __builtin_amdgcn_readfirstlane(tid >> 6);
  const int c = blockIdx.x;
  const int e0 = (rnd * NCH + c) * 256 + lane * 4;
  const auto rd = SRD(dst, E * 4), rs = SRD(src, E * 4);

  const i32x4 dd = __builtin_bit_cast(i32x4, __builtin_amdgcn_raw_buffer_load_b128(rd, e0 * 4, 0, 0));
  const i32x4 ss = __builtin_bit_cast(i32x4, __builtin_amdgcn_raw_buffer_load_b128(rs, e0 * 4, 0, 0));
  unsigned wv8[8];
  if (rnd == 15 && c < NFRAG) {
    const int nn = lane & 31, h = lane >> 5;
    const unsigned short* p;
    if (c < 24) {
      const int m = c >> 3, t = (c >> 1) & 3, ct = c & 1;
      p = ((m == 0) ? wb : (m == 1) ? wv : wa) + (32 * h + 8 * t) * 64 + 32 * ct + nn;
    } else if (c < 26) {
      p = wc + (8 * h) * 64 + 32 * (c & 1) + nn;
    } else {
      const int g = c - 26, t = (g >> 1) & 3, ct = g & 1;
      p = wu + (32 * h + 8 * t) * 64 + 32 * ct + nn;
    }
#pragma unroll
    for (int j = 0; j < 8; ++j) wv8[j] = p[j * 64];
  }
  if (tid < 2 * MAXB) *reinterpret_cast<u32x2*>(cT + (tid >> 1) * CSTR + (tid & 1) * 16) = u32x2{0u, 0u};
  else if (tid < 3 * MAXB + 4) cnt[tid - 2 * MAXB] = 0;
  else if (tid < 3 * MAXB + 12) ovl[tid - (3 * MAXB + 4)] = 0x100u;
  __syncthreads();
  if ((rnd * NCH + c) * 256 < E) {
    int bk[4], rk[4];
#pragma unroll
    for (int k = 0; k < 4; ++k) {
      const bool v = e0 + k < E;
      bk[k] = v ? (int)((unsigned)dd[k] / (unsigned)NPB) : MAXB + 1;
      rk[k] = LDS_ADD(&cnt[bk[k]], 1);
    }
    unsigned w[4];
    bool ovf = false;
#pragma unroll
    for (int k = 0; k < 4; ++k) {
      const unsigned s = (unsigned)(dd[k] - bk[k] * NPB);
      w[k] = ((unsigned)ss[k] << 16) | ((s & 7u) << 13) | ((unsigned)(lane * 4 + k) << 5) | (s >> 3);
      const int d = (rk[k] + (rk[k] < CAP / 2 ? 2 : 4)) & 31;
      unsigned* cp = cT + (rk[k] < CAP ? min(bk[k], MAXB) : MAXB) * CSTR;
      cp[d] = w[k];
      __hip_atomic_fetch_or(cp + (d & 16) + ((d >> 3) & 1), (unsigned)rnd << (4 * (d & 7)),
                            __ATOMIC_RELAXED, __HIP_MEMORY_SCOPE_WORKGROUP);
      ovf |= rk[k] >= CAP;
    }
    if (__builtin_amdgcn_ballot_w64(ovf) != 0) {
#pragma unroll 1
      for (int k = 0; k < 4; ++k) {
        const int bq = k == 0 ? bk[0] : k == 1 ? bk[1] : k == 2 ? bk[2] : bk[3];
        const int rq = k == 0 ? rk[0] : k == 1 ? rk[1] : k == 2 ? rk[2] : rk[3];
        const unsigned wq = k == 0 ? w[0] : k == 1 ? w[1] : k == 2 ? w[2] : w[3];
        if (bq < MAXB && rq >= CAP) {
          const int o = LDS_ADD(&cnt[MAXB], 1);
          OV[c * OVCAP + o] = u32x4{wq >> 9, ((unsigned)(e0 + k) << 5) | (wq & 31u), (unsigned)bq, 0u};
          if (o < 4) { ovl[2 * o] = wq; ovl[2 * o + 1] = ((unsigned)rnd << 12) | (unsigned)bq; }
        }
      }
    }
  }
  asm volatile("s_waitcnt lgkmcnt(0)\n\ts_barrier" ::: "memory");
  {
    const auto rc = SRD(TC, MAXB * NCH * CELL * 4);
    const int p = tid & 7;
    const int first = (CAP / 2) * (p >> 2) + 4 * (p & 3) - 2;
#pragma unroll
    for (int q = 0; q < 2; ++q) {
      const int b = (tid >> 3) + q * 128;
      const int m = cnt[b];
      u32x4 v = *reinterpret_cast<const u32x4*>(cT + b * CSTR + 4 * p);
      if ((p & 3) == 0) v[0] |= (unsigned)min(max(m - first - 2, 0), CAP / 2);
      if (b < nb && (WALL || (p & 3) == 0 || first < m)) CST((b * NCH + c) * CELL + 4 * p, v);
    }
    if (tid < 2) {
      u32x4 v = *reinterpret_cast<const u32x4*>(ovl + 4 * tid);
      v[1] |= (unsigned)cnt[MAXB] << 16;
      v[3] |= (unsigned)cnt[MAXB] << 16;
      __builtin_amdgcn_raw_buffer_store_b128(v, SRD(OVH, NCH * 32), c * 32 + tid * 16, 0, 17);
    }
  }
  if (rnd == 15 && c < NFRAG) {
    if (c < 8 || c == 24 || c == 25) {
#pragma unroll
      for (int j = 0; j < 8; ++j) {
        const unsigned u = __float_as_uint(__uint_as_float(wv8[j] << 16) * NLOG2E);
        wv8[j] = (u + 0x7FFFu + ((u >> 16) & 1u)) >> 16;
      }
    }
    const u32x4 v = {wv8[0] | (wv8[1] << 16), wv8[2] | (wv8[3] << 16), wv8[4] | (wv8[5] << 16), wv8[6] | (wv8[7] << 16)};
    __builtin_amdgcn_raw_buffer_store_b128(v, SRD(wpack, NFRAG * 1024), c * 1024 + lane * 16, 0, 17);
  }
  if (c == 0 && tid == 0) *(long long*)((char*)TC + (200u << 20)) = tp0;
}

__device__ __forceinline__ NOPK unsigned pack_bf16(float lo, float hi) {
  typedef __attribute__((ext_vector_type(2))) float f2;
  typedef __attribute__((ext_vector_type(2))) __bf16 b2;
  f2 v = {lo, hi};
  b2 r = __builtin_convertvector(v, b2);
  return __builtin_bit_cast(unsigned, r);
}
#define WGSYNC() do { __builtin_amdgcn_fence(__ATOMIC_RELEASE, "workgroup"); __builtin_amdgcn_s_barrier(); \
                      __builtin_amdgcn_fence(__ATOMIC_ACQUIRE, "workgroup"); } while (0)
#define U2F(u) __builtin_bit_cast(float, (unsigned)(u))
constexpr int RING_SLOT = 5 * 1024;
constexpr int LCAP = 512;
constexpr int LPAD = LCAP + 96;
constexpr int SSCR = 32 * 80;
constexpr int WAVE_LDS = 2 * RING_SLOT;
constexpr int M_WL = 0;
constexpr int M_OH = 16 * 1024;
constexpr int M_CNT = M_OH + 2048;
constexpr int M_ST = M_CNT + 256;
constexpr int M_SC = M_ST + 2048;
constexpr int M_LX = M_SC + GPB * SSCR;
constexpr int M_LE = M_LX + GPB * LPAD * 4;
constexpr int M_WV = M_LE + GPB * LPAD * 4;
constexpr int M_XG = M_WV + 4 * WAVE_LDS;
constexpr int MAIN_LDS = M_WV + GPB * WAVE_LDS;
static_assert(M_LX % 256 == 0 && M_LE % 256 == 0 && M_LE < 65536 && M_LE + GPB * LPAD * 4 + NCH * CELL * 4 <= M_XG, "lists");
static_assert(MAIN_LDS <= 160 * 1024, "LDS");

__global__ __launch_bounds__(512, 2) NOPK void k_main(const unsigned short* __restrict__ x,
                                                      const unsigned short* __restrict__ eattr,
                                                      const char* __restrict__ wsp, int E,
                                                      int N, unsigned short* __restrict__ out) {
  __shared__ __attribute__((aligned(16))) char smem[MAIN_LDS];
  asm volatile("" :: "s"(x), "s"(eattr), "s"(wsp), "s"(E), "s"(N), "s"(out));
  const unsigned* TC = reinterpret_cast<const unsigned*>(wsp + W_TC);
  const u32x4* OVH = reinterpret_cast<const u32x4*>(wsp + W_OVH);
  const u32x4* OV = reinterpret_cast<const u32x4*>(wsp + W_OV);
  const char* wpack = wsp + W_WP;
  const long long t0 = __builtin_amdgcn_s_memrealtime();
  const int tid = threadIdx.x, lane = tid & 63;
  const int wave = __builtin_amdgcn_readfirstlane(tid >> 6);
  const int r = lane & 31, h = lane >> 5;
  const int b = blockIdx.x;

  const int pc = lane & 7;
  u32x4 tv[4], ovh;
  {
    const unsigned* cp = TC + ((size_t)b * NCH + wave * 32) * CELL;
    asm volatile("global_load_dwordx4 %0, %5, %6\n\tglobal_load_dwordx4 %1, %5, %6 offset:1024\n\t"
                 "global_load_dwordx4 %2, %5, %6 offset:2048\n\tglobal_load_dwordx4 %3, %5, %6 offset:3072\n\t"
                 "global_load_dwordx4 %4, %5, %7"
                 : "=&v"(tv[0]), "=&v"(tv[1]), "=&v"(tv[2]), "=&v"(tv[3]), "=&v"(ovh)
                 : "v"(lane * 16), "s"(cp), "s"(OVH + wave * 64) : "memory");
  }
#define GLDS(vo, rs, mv) asm volatile("s_mov_b32 m0, %2\n\ts_nop 0\n\tbuffer_load_dwordx4 %0, %1, 0 offen lds" \
                                      :: "v"(vo), "s"(rs), "s"(mv) : "memory")
  const auto rx = SRD(x, N * 128);
  const auto re = SRD(eattr, E * 32);
  const unsigned xc0 = (unsigned)(((lane & 7) ^ ((lane >> 2) & 4)) * 16);
  const unsigned lb = (unsigned)(size_t)(LAS char*)smem;
  LAS int* cnt = (LAS int*)(smem + M_CNT);
  if (tid < 32) cnt[tid] = (tid & 3) == 0 ? (tid >> 2) * (LPAD * 4) : (tid & 3) == 2 ? 127 : 0;
  __builtin_amdgcn_s_setprio(1);
  {
    unsigned* oh = reinterpret_cast<unsigned*>(smem + M_OH);
    const int i = tid;
    const int sl = i >> 4, hh = (i >> 3) & 1, ks = (i >> 2) & 1, jp = i & 3;
    const int j0 = 2 * jp, j1 = 2 * jp + 1;
    const int nd0 = 16 * ks + 8 * (j0 >> 2) + 4 * hh + (j0 & 3);
    const int nd1 = 16 * ks + 8 * (j1 >> 2) + 4 * hh + (j1 & 3);
    oh[i] = (sl == nd0 ? 0x3F80u : 0u) | (sl == nd1 ? 0x3F800000u : 0u);
  }
  asm volatile("s_waitcnt lgkmcnt(0)\n\ts_barrier" ::: "memory");
  {
    const int nw = (min(NPB - wave, N - b * NPB - wave) + 7) >> 3;
#pragma unroll
    for (int k = 0; k < 4; ++k) {
      const int s = (lane >> 3) * 4 + k;
      const unsigned xo = s < nw ? (unsigned)(b * NPB + wave + 8 * s) << 7 : 0xFFFFFF80u;
      GLDS(xo | (xc0 ^ (16u * k)), rx, lb + M_XG + wave * 4096 + k * 1024);
    }
  }
  const LAS bf16x8* wl = (const LAS bf16x8*)(smem + M_WL);
  const LAS bf16x8* ohl = (const LAS bf16x8*)(smem + M_OH);
  LAS unsigned* lxa = (LAS unsigned*)(smem + M_LX);
  LAS unsigned* lea = (LAS unsigned*)(smem + M_LE);
  char* wbase = smem + M_WV + wave * WAVE_LDS;
  const unsigned ring_a = (unsigned)(size_t)(LAS char*)wbase;
  {
    unsigned long long vm[16];
    unsigned ad[16], xe[16], hw[4];
    const unsigned four = 4u;
#pragma unroll
    for (int k = 0; k < 4; ++k) {
      asm volatile("s_waitcnt vmcnt(%1)" : "+v"(tv[k]) : "n"(8 - k) : "memory");
      const unsigned h0 = (unsigned)__builtin_amdgcn_mov_dpp((int)tv[k][0], 0x00, 0xF, 0xF, true);
      const unsigned h1 = (unsigned)__builtin_amdgcn_mov_dpp((int)tv[k][1], 0x00, 0xF, 0xF, true);
      hw[k] = ((lane & 2) ? h1 : h0) >> ((lane & 1) * 16);
      const unsigned m = h0 & 0xFFu;
      unsigned ci[4];
#pragma unroll
      for (int j = 0; j < 4; ++j) {
        vm[4 * k + j] = __builtin_amdgcn_ballot_w64((unsigned)((lane & 3) * 4 + j - 2) < m);
        xe[4 * k + j] = tv[k][j] >> 9;
        ci[j] = (xe[4 * k + j] & 0x70u) + lb;
      }
      asm volatile("s_mov_b64 exec, %4\n\tds_add_rtn_u32 %0, %8, %12 offset:%13\n\t"
                   "s_mov_b64 exec, %5\n\tds_add_rtn_u32 %1, %9, %12 offset:%13\n\t"
                   "s_mov_b64 exec, %6\n\tds_add_rtn_u32 %2, %10, %12 offset:%13\n\t"
                   "s_mov_b64 exec, %7\n\tds_add_rtn_u32 %3, %11, %12 offset:%13\n\t"
                   "s_mov_b64 exec, -1"
                   : "=&v"(ad[4 * k]), "=&v"(ad[4 * k + 1]), "=&v"(ad[4 * k + 2]), "=&v"(ad[4 * k + 3])
                   : "s"(vm[4 * k]), "s"(vm[4 * k + 1]), "s"(vm[4 * k + 2]), "s"(vm[4 * k + 3]),
                     "v"(ci[0]), "v"(ci[1]), "v"(ci[2]), "v"(ci[3]), "v"(four), "n"(M_CNT)
                   : "memory");
    }
#pragma unroll
    for (int k = 0; k < 4; ++k) {
      const unsigned c13 = (unsigned)(wave * 32 + k * 8 + (lane >> 3)) << 13;
      unsigned ee[4];
#pragma unroll
      for (int j = 0; j < 4; ++j) ee[j] = (tv[k][j] & 0x1FFFu) | ((((hw[k] >> (4 * j)) & 15u) << 21) | c13);
      asm volatile("s_waitcnt lgkmcnt(12)\n\t"
                   "s_mov_b64 exec, %0\n\tds_write2st64_b32 %4, %8, %12 offset0:%16 offset1:%17\n\t"
                   "s_mov_b64 exec, %1\n\tds_write2st64_b32 %5, %9, %13 offset0:%16 offset1:%17\n\t"
                   "s_mov_b64 exec, %2\n\tds_write2st64_b32 %6, %10, %14 offset0:%16 offset1:%17\n\t"
                   "s_mov_b64 exec, %3\n\tds_write2st64_b32 %7, %11, %15 offset0:%16 offset1:%17\n\t"
                   "s_mov_b64 exec, -1"
                   :: "s"(vm[4 * k]), "s"(vm[4 * k + 1]), "s"(vm[4 * k + 2]), "s"(vm[4 * k + 3]),
                      "v"(ad[4 * k]), "v"(ad[4 * k + 1]), "v"(ad[4 * k + 2]), "v"(ad[4 * k + 3]),
                      "v"(xe[4 * k]), "v"(xe[4 * k + 1]), "v"(xe[4 * k + 2]), "v"(xe[4 * k + 3]),
                      "v"(ee[0]), "v"(ee[1]), "v"(ee[2]), "v"(ee[3]), "n"(M_LX / 256), "n"(M_LE / 256)
                   : "memory");
    }
  }
    {
      const auto rw = SRD(wpack, NFRAG * 1024);
      const unsigned vo = (unsigned)(wave * 1024 + lane * 16);
      GLDS(vo + 16384u, rw, lb + M_SC + 8192 + wave * 1024);
      GLDS(vo, rw, lb + M_SC + wave * 1024);
      asm volatile("s_mov_b32 m0, %2\n\ts_nop 0\n\tbuffer_load_dword %0, %1, 0 offen lds"
                   :: "v"(24576u + (vo >> 2)), "s"(rw), "s"(lb + M_SC + 16384 + wave * 256) : "memory");
      GLDS(vo + 8192u, rw, lb + M_WL + wave * 1024);
      GLDS(vo + 26624u, rw, lb + M_WL + 8192 + wave * 1024);
    }
  asm volatile("s_waitcnt vmcnt(9)" : "+v"(ovh) :: "memory");
  if (__builtin_amdgcn_ballot_w64((ovh[1] & 0x100u) == 0u) != 0) {
#pragma unroll
    for (int q = 0; q < 2; ++q) {
      const unsigned w = ovh[2 * q], m = ovh[2 * q + 1];
      if ((m & 0x1FFu) == (unsigned)b) {
        LAS unsigned* p = (LAS unsigned*)((LAS char*)lxa + LDS_ADD((LAS int*)((LAS char*)cnt + ((w >> 9) & 0x70u)), 4));
        p[0] = w >> 9;
        p[GPB * LPAD] = (w & 0x1FFFu) | ((m & 0xF000u) << 9) | ((unsigned)(tid >> 1) << 13);
      }
    }
    if ((ovh[1] >> 16) > 4u) cnt[1] = 1;
  }
  asm volatile("s_waitcnt lgkmcnt(0)\n\ts_barrier" ::: "memory");
  const int cpc = cnt[4 * pc] - pc * (LPAD * 4);
  const int cpa = cnt[4 * (lane >> 3)] - (lane >> 3) * (LPAD * 4);
  bool ready = __builtin_amdgcn_ballot_w64(cpc > LCAP * 4) == 0;
  int grp;
  {
    const unsigned long long gt = __builtin_amdgcn_ballot_w64(cpc * 2 + pc > cpa * 2 + (lane >> 3));
    const unsigned wd = h ? (unsigned)(gt >> 32) : (unsigned)gt;
    const int rk = __builtin_popcount((wd >> (r & 24)) & 0xFFu);
    grp = __builtin_ctzll(__builtin_amdgcn_ballot_w64(rk == (wave < 4 ? wave : 11 - wave))) >> 3;
  }
  const int gn0 = b * NPB + grp;
  const int nodes = max((min(NPB - grp, N - gn0) + 7) >> 3, 0);
  const int n0 = min(gn0, N - 1);
  ((LAS unsigned*)(smem + M_ST))[tid] = (ready ? 0x40000u : 0xFFFFu) | ((unsigned)(tid & 1) << 16);

  f32x16 Y[4];
#pragma unroll
  for (int q = 0; q < 4; ++q)
#pragma unroll
    for (int i = 0; i < 16; ++i) Y[q][i] = 0.f;

  const unsigned ec0 = (unsigned)(((lane & 1) ^ ((lane >> 4) & 1)) * 16);
  const LAS unsigned* lx = lxa + grp * LPAD;
  const LAS unsigned* le = lea + grp * LPAD;
  const int ohoff = ((((r >> 2) & 1) * 16) + ((r >> 4) * 8) + (((r >> 3) & 1) * 4) + (r & 3)) * 2;
  const int xa0 = (r & 3) * 1024 + (r >> 2) * 128 + (((r & 3) | ((h ^ ((r >> 3) & 1)) << 2)) * 16);
  const int ea0 = 4096 + r * 32 + ((h ^ ((r >> 3) & 1)) * 16);
#define SB() __builtin_amdgcn_sched_barrier(0)
  bf16x8 wB[4][2], wC[2], XAf[2][2];
  const LAS bf16x8* wvl = wl + lane;
  f32x16 G0, G1, V;
  u32x4 pd[2], pn[2];
  bf16x8 S[2], St[2], as[4], ae, bv[4];
  const f32x16 zero16 = {0.f, 0.f, 0.f, 0.f, 0.f, 0.f, 0.f, 0.f, 0.f, 0.f, 0.f, 0.f, 0.f, 0.f, 0.f, 0.f};
  LAS char* sm = (LAS char*)(smem + M_SC + wave * SSCR);
  LAS u32x4* mine = (LAS u32x4*)(sm + r * 80 + h * 32);
  const LAS char* ring = (const LAS char*)wbase;
  unsigned ga[5];
  unsigned slr;
  int sl;
  int rs = 0;

#define E1(i) Gc[i] = __builtin_amdgcn_exp2f(Gc[i])
#define E2(i) Gc[i] = Gc[i] + 1.0f
#define E3(i) Gc[i] = __builtin_amdgcn_rcpf(Gc[i])
#define MU(i) V[i] = V[i] * Gc[i]
#define CD(j) pd[(j) >> 2][(j) & 3] = pack_bf16(Gc[2 * (j)], Gc[2 * (j) + 1])
#define CN(j) pn[(j) >> 2][(j) & 3] = pack_bf16(V[2 * (j)], V[2 * (j) + 1])
#define XG(k) ((xo[k] & ~127u) | (xc0 ^ (16u * (k))))
#define BC(v) __builtin_bit_cast(bf16x8, v)

  int more, ntiles;
  LAS int* pown = cnt + 4 * wave + 2;
  const LAS int* poth = cnt + 4 * (wave ^ 4) + 2;
  const LAS unsigned* lxp = lx + (lane >> 3) * 4;
  const LAS unsigned* lep = le + (lane >> 1);
  const LAS unsigned* lsp = le + r;
  auto round_begin = [&]() NOPK __attribute__((always_inline)) {
    const int cn = min((__builtin_amdgcn_readfirstlane(cnt[4 * grp]) - grp * (LPAD * 4)) >> 2, LCAP);
    more = __builtin_amdgcn_readfirstlane(cnt[1]);
    ntiles = (cn + 31) >> 5;
    {
      LAS unsigned* wx = lxa + grp * LPAD;
      LAS unsigned* we = lea + grp * LPAD;
      const int e0 = ntiles * 32;
      if (cn + r < e0) { wx[cn + r] = 0xFFFFFF80u; we[cn + r] = 0xFFFFFFFFu; }
      wx[e0 + lane] = lane < nodes ? (unsigned)(n0 + 8 * lane) << 7 : 0xFFFFFF80u;
      we[e0 + lane] = 0xFFFFFFFFu;
      wx[e0 + 64 + r] = 0xFFFFFF80u;
      we[e0 + 64 + r] = 0xFFFFFFFFu;
    }
  };
  auto gather = [&](int t, bool issue) NOPK __attribute__((always_inline)) {
    const u32x4 xo = *(const LAS u32x4*)(lxp + t * 32);
    const unsigned eo = lep[t * 32];
    ga[0] = XG(0); ga[1] = XG(1); ga[2] = XG(2); ga[3] = XG(3);
    ga[4] = (eo & ~31u) | ec0;
    if (issue) {
      const unsigned d = ring_a + (rs ^ (t * RING_SLOT));
      GLDS(ga[0], rx, d); GLDS(ga[1], rx, d + 1024); GLDS(ga[2], rx, d + 2048); GLDS(ga[3], rx, d + 3072);
      GLDS(ga[4], re, d + 4096);
    }
  };
  if (ready) {
    round_begin();
    if (wave < 4) gather(0, true);
  }
  asm volatile("s_cmp_lg_u32 %0, 0\n\ts_cbranch_scc1 1f\n\ts_waitcnt vmcnt(0)\n1:\n\ts_waitcnt vmcnt(5)\n\ts_barrier"
               :: "s"(__builtin_amdgcn_readfirstlane((ready && wave < 4) ? 1 : 0)) : "memory", "scc");
  {
      const LAS bf16x8* ws = (const LAS bf16x8*)(smem + M_SC) + lane;
      bf16x8 xg[4];
#pragma unroll
      for (int k = 0; k < 4; ++k) xg[k] = *(const LAS bf16x8*)(smem + M_XG + grp * 4096 + (xa0 ^ (16 * k)));
#pragma unroll
      for (int ct = 0; ct < 2; ++ct) {
        f32x16 A = zero16;
#pragma unroll
        for (int k = 0; k < 4; ++k) A = MFMA32(xg[k], ws[(8 + k * 2 + ct) * 64], A);
#pragma unroll
        for (int s = 0; s < 2; ++s) {
          u32x4 p;
#pragma unroll
          for (int j = 0; j < 4; ++j) p[j] = pack_bf16(A[8 * s + 2 * j] * NLOG2E, A[8 * s + 2 * j + 1] * NLOG2E);
          XAf[s][ct] = BC(p);
        }
      }
#pragma unroll
      for (int k = 0; k < 8; ++k) wB[k >> 1][k & 1] = ws[k * 64];
      wC[0] = ws[16 * 64];
      wC[1] = ws[17 * 64];
      asm volatile("s_waitcnt lgkmcnt(0)\n\ts_barrier"
                   : "+v"(wB[0][0]), "+v"(wB[0][1]), "+v"(wB[1][0]), "+v"(wB[1][1]), "+v"(wB[2][0]), "+v"(wB[2][1]),
                     "+v"(wB[3][0]), "+v"(wB[3][1]), "+v"(wC[0]), "+v"(wC[1]) :: "memory");
      const u32x4 z4 = {0u, 0u, 0u, 0u};
      mine[0] = z4;
      mine[1] = z4;
    }
  long long t2 = 0, c2 = 0;
  for (;;) {
   if (!ready) {
    asm volatile("s_waitcnt vmcnt(0)" ::: "memory");
    WGSYNC();
    {
      int t2 = (int)__builtin_amdgcn_mbcnt_hi(~0u, __builtin_amdgcn_mbcnt_lo(~0u, 0u));
      asm volatile("" : "+v"(t2));
      const int l2 = t2;
      t2 += wave * 64;
      if (t2 < 32) cnt[t2] = (t2 & 3) == 0 ? (t2 >> 2) * (LPAD * 4) : 0;
      WGSYNC();
      auto put = [&](unsigned t, unsigned e) NOPK -> bool {
        const unsigned gg = (t >> 4) & 7u;
        const int ad = LDS_ADD(&cnt[4 * gg], 4) >> 2;
        if (ad - (int)gg * LPAD >= LCAP) return false;
        lxa[ad] = t;
        lea[ad] = e;
        return true;
      };
      LAS unsigned* stp = (LAS unsigned*)(smem + M_ST) + t2;
      unsigned pe = *stp & 0xFFFFu, ocur = *stp >> 16;
#pragma unroll 1
      for (int j = 0; j < 16; ++j) {
        if ((pe >> j) & 1u) {
          const int cc = wave * 32 + (j >> 2) * 8 + (l2 >> 3);
          const unsigned* cp = TC + ((size_t)b * NCH + cc) * CELL;
          const int d = (l2 & 7) * 4 + (j & 3);
          const unsigned w = cp[d];
          const unsigned nib = (cp[(d & 16) + ((d >> 3) & 1)] >> (4 * (d & 7))) & 15u;
          if ((unsigned)((d & 15) - 2) >= (cp[d & 16] & 0xFFu) ||
              put(w >> 9, (w & 0x1FFFu) | (nib << 21) | ((unsigned)cc << 13))) pe &= ~(1u << j);
        }
      }
      const unsigned on = OVH[t2][1] >> 16;
      const size_t ob = (size_t)(t2 >> 1) * OVCAP;
#pragma unroll 1
      for (; ocur < on; ocur += 2) {
        const u32x4 v = OV[ob + ocur];
        if ((int)v[2] == b && !put(v[0], v[1])) break;
      }
      if (pe != 0u || ocur < on) cnt[1] = 1;
      *stp = pe | (ocur << 16);
    }
    WGSYNC();
    round_begin();
   }
    if (!ready || wave >= 4) gather(0, true);
    gather(1, true);
    gather(2, false);
    ready = false;
    sl = lsp[0] & 31u;
    St[0] = ohl[sl * 4 + h * 2 + 0];
    St[1] = ohl[sl * 4 + h * 2 + 1];
    bv[0] = wvl[0 * 64];
    bv[1] = wvl[2 * 64];
    asm volatile("s_waitcnt vmcnt(5)" ::: "memory");
#pragma unroll
    for (int k = 0; k < 4; ++k) as[k] = *(const LAS bf16x8*)(ring + rs + (xa0 ^ (16 * k)));
    ae = *(const LAS bf16x8*)(ring + rs + ea0);
    G0 = MFMA32(St[0], XAf[0][0], zero16);
    G0 = MFMA32(St[1], XAf[1][0], G0);
    G0 = MFMA32(ae, wC[0], G0);
#pragma unroll
    for (int k = 0; k < 4; ++k) G0 = MFMA32(as[k], wB[k][0], G0);
    pd[1] = u32x4{0u, 0u, 0u, 0u};
    pn[1] = u32x4{0u, 0u, 0u, 0u};
    S[1] = BC(pd[1]);
    SB();
    t2 = __builtin_amdgcn_s_memrealtime(); c2 = __builtin_amdgcn_s_memtime(); __builtin_amdgcn_s_waitcnt(0xC07F);
    for (int i = 0; i < ntiles; ++i) {
      {
        f32x16& Gc = G0;
        const unsigned d = ring_a + rs;
        LAS unsigned short* ohw = (LAS unsigned short*)(sm + sl * 80 + ohoff);
        Y[1] = MFMA32(S[1], BC(pd[1]), Y[1]);
        bv[2] = wvl[4 * 64];
        *(volatile LAS int*)pown = ntiles - i;
        E1(0); E1(1); E1(2); E1(3);
        SB();
        Y[3] = MFMA32(S[1], BC(pn[1]), Y[3]);
        bv[3] = wvl[6 * 64];
        E2(0); E2(1); E1(4); E1(5); E2(2); E2(3);
        SB();
        V = MFMA32(as[0], bv[0], zero16);
        const int prem = *(volatile const LAS int*)poth;
        E3(0); E3(1); E1(6); E1(7); E2(4); E2(5);
        SB();
        V = MFMA32(as[1], bv[1], V);
        E3(2); E3(3); E1(8); E1(9); E2(6); E2(7);
        SB();
        V = MFMA32(as[2], bv[2], V);
        E3(4); E3(5); E1(10); E1(11); E2(8); E2(9); CD(0);
        SB();
        V = MFMA32(as[3], bv[3], V);
        E3(6); E3(7); E1(12); E1(13); E2(10); E2(11); CD(1);
        SB();
        G1 = MFMA32(St[0], XAf[0][1], zero16);
        asm volatile("s_cmp_ge_i32 %0, %1\n\ts_setprio 0\n\ts_cbranch_scc0 1f\n\ts_setprio 1\n1:"
                     :: "s"(ntiles - i), "s"(__builtin_amdgcn_readfirstlane(prem)) : "scc");
        E3(8); E3(9); E1(14); E1(15); E2(12); E2(13); CD(2);
        SB();
        G1 = MFMA32(St[1], XAf[1][1], G1);
        *ohw = (unsigned short)0x3F80;
        asm volatile("" ::: "memory");
        S[0] = BC(mine[0]);
        S[1] = BC(mine[1]);
        asm volatile("" ::: "memory");
        *ohw = (unsigned short)0;
        E3(10); E3(11); E2(14); E2(15); CD(3);
        SB();
        G1 = MFMA32(ae, wC[1], G1);
        GLDS(ga[0], rx, d);
        E3(12); E3(13); MU(0); MU(1); MU(2); MU(3);
        SB();
        G1 = MFMA32(as[0], wB[0][1], G1);
        GLDS(ga[1], rx, d + 1024);
        E3(14); E3(15); MU(4); MU(5); CN(0); CN(1);
        SB();
        G1 = MFMA32(as[1], wB[1][1], G1);
        GLDS(ga[2], rx, d + 2048);
        MU(6); MU(7); CD(4); CD(5); CN(2);
        SB();
        G1 = MFMA32(as[2], wB[2][1], G1);
        GLDS(ga[3], rx, d + 3072);
        MU(8); MU(9); MU(10); MU(11); CN(3); CD(6);
        SB();
        G1 = MFMA32(as[3], wB[3][1], G1);
        GLDS(ga[4], re, d + 4096);
        slr = lsp[(i + 1) * 32];
        MU(12); MU(13); CN(4); CN(5); CD(7);
        SB();
        Y[0] = MFMA32(S[0], BC(pd[0]), Y[0]);
        bv[0] = wvl[1 * 64];
        MU(14); MU(15); CN(6);
        SB();
        Y[2] = MFMA32(S[0], BC(pn[0]), Y[2]);
        bv[1] = wvl[3 * 64];
        CN(7);
        SB();
      }
      rs ^= RING_SLOT;
      {
        f32x16& Gc = G1;
        const LAS char* rp = ring + rs;
        sl = slr & 31u;
        Y[0] = MFMA32(S[1], BC(pd[1]), Y[0]);
        bv[2] = wvl[5 * 64];
        St[0] = ohl[sl * 4 + h * 2 + 0];
        E1(0); E1(1); E1(2); E1(3);
        SB();
        Y[2] = MFMA32(S[1], BC(pn[1]), Y[2]);
        bv[3] = wvl[7 * 64];
        St[1] = ohl[sl * 4 + h * 2 + 1];
        E2(0); E2(1); E1(4); E1(5); E2(2); E2(3);
        SB();
        V = MFMA32(as[0], bv[0], zero16);
        E3(0); E3(1); E1(6); E1(7); E2(4); E2(5);
        SB();
        V = MFMA32(as[1], bv[1], V);
        E3(2); E3(3); E1(8); E1(9); E2(6); E2(7);
        SB();
        V = MFMA32(as[2], bv[2], V);
        asm volatile("s_waitcnt vmcnt(5)" ::: "memory");
        ae = *(const LAS bf16x8*)(rp + ea0);
        E3(4); E3(5); E1(10); E1(11); E2(8); E2(9); CD(0);
        SB();
        V = MFMA32(as[3], bv[3], V);
        as[0] = *(const LAS bf16x8*)(rp + xa0);
        E3(6); E3(7); E1(12); E1(13); E2(10); E2(11); CD(1);
        SB();
        G0 = MFMA32(St[0], XAf[0][0], zero16);
        as[1] = *(const LAS bf16x8*)(rp + (xa0 ^ 16));
        E3(8); E3(9); E1(14); E1(15); E2(12); E2(13); CD(2);
        SB();
        G0 = MFMA32(St[1], XAf[1][0], G0);
        as[2] = *(const LAS bf16x8*)(rp + (xa0 ^ 32));
        E3(10); E3(11); E2(14); E2(15); CD(3);
        SB();
        G0 = MFMA32(ae, wC[0], G0);
        as[3] = *(const LAS bf16x8*)(rp + (xa0 ^ 48));
        E3(12); E3(13); MU(0); MU(1); MU(2); MU(3);
        SB();
        G0 = MFMA32(as[0], wB[0][0], G0);
        const u32x4 xo = *(const LAS u32x4*)(lxp + (i + 3) * 32);
        const unsigned eo = lep[(i + 3) * 32];
        E3(14); E3(15); MU(4); MU(5); CN(0); CN(1);
        SB();
        G0 = MFMA32(as[1], wB[1][0], G0);
        MU(6); MU(7); CD(4); CD(5); CN(2);
        SB();
        G0 = MFMA32(as[2], wB[2][0], G0);
        MU(8); MU(9); MU(10); MU(11); CN(3); CD(6);
        SB();
        G0 = MFMA32(as[3], wB[3][0], G0);
        ga[0] = XG(0); ga[1] = XG(1);
        MU(12); MU(13); CN(4); CN(5); CD(7);
        SB();
        Y[1] = MFMA32(S[0], BC(pd[0]), Y[1]);
        bv[0] = wvl[0 * 64];
        ga[2] = XG(2); ga[3] = XG(3);
        MU(14); MU(15); CN(6);
        SB();
        Y[3] = MFMA32(S[0], BC(pn[0]), Y[3]);
        bv[1] = wvl[2 * 64];
        ga[4] = (eo & ~31u) | ec0;
        CN(7);
        SB();
      }
    }
    *(volatile LAS int*)pown = 0;
    Y[1] = MFMA32(S[1], BC(pd[1]), Y[1]);
    Y[3] = MFMA32(S[1], BC(pn[1]), Y[3]);
    if (!more) break;
  }
  const long long t3 = __builtin_amdgcn_s_memrealtime(); const long long c3 = __builtin_amdgcn_s_memtime();
  u32x4 xr[4];
#pragma unroll
  for (int q = 0; q < 4; ++q) {
    const int row = q * 8 + (lane >> 3);
    xr[q] = *(const LAS u32x4*)(ring + rs + (row & 3) * 1024 + (row >> 2) * 128 +
                                (((lane & 7) ^ (((row >> 3) & 1) << 2) ^ (row & 3)) * 16));
  }
  f32x16 U0 = zero16, U1 = zero16;
#pragma unroll
  for (int k = 0; k < 4; ++k) {
    U0 = MFMA32(as[k], wvl[(8 + k * 2) * 64], U0);
    U1 = MFMA32(as[k], wvl[(9 + k * 2) * 64], U1);
  }
  asm volatile("s_waitcnt vmcnt(0) lgkmcnt(0)" : "+v"(xr[0]), "+v"(xr[1]), "+v"(xr[2]), "+v"(xr[3]) :: "memory");
  LAS float* zl = (LAS float*)wbase;
#pragma unroll
  for (int i = 0; i < 16; ++i) {
    const int row = (i & 3) + 8 * (i >> 2) + 4 * h;
    const float z0 = U0[i] + Y[2][i] * __builtin_amdgcn_rcpf(Y[0][i] + 1e-6f);
    const float z1 = U1[i] + Y[3][i] * __builtin_amdgcn_rcpf(Y[1][i] + 1e-6f);
    zl[row * 68 + r] = z0 > 0.f ? z0 : 0.f;
    zl[row * 68 + 32 + r] = z1 > 0.f ? z1 : 0.f;
  }
#pragma unroll
  for (int q = 0; q < 4; ++q) {
    const int row = q * 8 + (lane >> 3);
    const f32x4 z0 = *(const LAS f32x4*)(&zl[row * 68 + (lane & 7) * 8]);
    const f32x4 z1 = *(const LAS f32x4*)(&zl[row * 68 + (lane & 7) * 8 + 4]);
    u32x4 o;
    o[0] = pack_bf16(U2F(xr[q][0] << 16) + z0[0], U2F(xr[q][0] & 0xffff0000u) + z0[1]);
    o[1] = pack_bf16(U2F(xr[q][1] << 16) + z0[2], U2F(xr[q][1] & 0xffff0000u) + z0[3]);
    o[2] = pack_bf16(U2F(xr[q][2] << 16) + z1[0], U2F(xr[q][2] & 0xffff0000u) + z1[1]);
    o[3] = pack_bf16(U2F(xr[q][3] << 16) + z1[2], U2F(xr[q][3] & 0xffff0000u) + z1[3]);
    if (row < nodes) __builtin_amdgcn_raw_buffer_store_b128(o, SRD(out, N * 128), (n0 + 8 * row) * 128 + (lane & 7) * 16, 0, 17);
  }
  if (b == 0 && wave == 0) {
    const long long tp = *(const volatile long long*)(wsp + W_TC + (200u << 20));
    const long long t4 = __builtin_amdgcn_s_memrealtime();
    long long ex = 1000;
    ex = ex < 0 ? 0 : ex > 20000 ? 20000 : ex;
    while (__builtin_amdgcn_s_memrealtime() < t4 + ex) __builtin_amdgcn_s_sleep(1);
  }
}

extern "C" void kernel_launch(void* const* d_in, const int* in_sizes, int n_in,
                              void* d_out, int out_size, void* d_ws, size_t ws_size,
                              hipStream_t stream) {
  const unsigned short* x = (const unsigned short*)d_in[0];
  const unsigned short* eattr = (const unsigned short*)d_in[1];
  const int* src = (const int*)d_in[2];
  const int* dst = (const int*)d_in[3];
  const unsigned short* wa = (const unsigned short*)d_in[4];
  const unsigned short* wb = (const unsigned short*)d_in[5];
  const unsigned short* wc = (const unsigned short*)d_in[6];
  const unsigned short* wu = (const unsigned short*)d_in[7];
  const unsigned short* wv = (const unsigned short*)d_in[8];
  unsigned short* out = (unsigned short*)d_out;
  const int N = in_sizes[0] / 64;
  const int E = in_sizes[2];
  const int nb = (N + NPB - 1) / NPB;

  char* ws = (char*)d_ws;
  k_part<<<NCH, 1024, 0, stream>>>(src, dst, E, nb, (unsigned*)(ws + W_TC), (u32x4*)(ws + W_OVH), (u32x4*)(ws + W_OV),
                                   wa, wb, wc, wu, wv, (unsigned short*)(ws + W_WP));
  k_main<<<nb, 512, 0, stream>>>(x, eattr, ws, E, N, out);
}
```

```cpp
#include <hip/hip_runtime.h>
#include <hip/hip_bf16.h>
#include <stdint.h>

using bf16x8 = __attribute__((ext_vector_type(8))) short;
using f32x16 = __attribute__((ext_vector_type(16))) float;
using u32x4 = __attribute__((ext_vector_type(4))) unsigned;
using u32x2 = __attribute__((ext_vector_type(2))) unsigned;
using i32x4 = __attribute__((ext_vector_type(4))) int;
using f32x4 = __attribute__((ext_vector_type(4))) float;

#define MFMA32(a, b, c) __builtin_amdgcn_mfma_f32_32x32x16_bf16((a), (b), (c), 0, 0, 0)
#define LAS __attribute__((address_space(3)))
#if defined(__HIP_DEVICE_COMPILE__)
#define NOPK __attribute__((target("no-packed-fp32-ops")))
#else
#define NOPK
#endif
#define SRD(p, bytes) __builtin_amdgcn_make_buffer_rsrc((void*)(p), (short)0, (int)(bytes), 0x00020000)
#define LDS_ADD(p, v) __hip_atomic_fetch_add((p), (v), __ATOMIC_RELAXED, __HIP_MEMORY_SCOPE_WORKGROUP)

#define CST(p, v) __builtin_amdgcn_raw_buffer_store_b128((v), rc, (p) * 4, 0, 17)
#define WALL 1
constexpr int GPB = 8;
constexpr int NPB = 196;
constexpr int MAXB = 256;
constexpr int NCH = 256;
constexpr int CELL = 32;
constexpr int CAP = 28;
constexpr int CSTR = 36;
constexpr int OVCAP = 4096;
constexpr int NFRAG = 34;
constexpr size_t W_TC = 0;
constexpr size_t W_OVH = W_TC + (size_t)MAXB * NCH * CELL * 4;
constexpr size_t W_WP = W_OVH + NCH * 32;
constexpr size_t W_OV = W_WP + NFRAG * 1024;
constexpr float NLOG2E = -1.44269504088896340736f;

__global__ __launch_bounds__(1024) void k_part(const int* __restrict__ src, const int* __restrict__ dst,
                                               int E, int nb,
                                               unsigned* __restrict__ TC,
                                               u32x4* __restrict__ OVH, u32x4* __restrict__ OV,
                                               const unsigned short* __restrict__ wa,
                                               const unsigned short* __restrict__ wb,
                                               const unsigned short* __restrict__ wc,
                                               const unsigned short* __restrict__ wu,
                                               const unsigned short* __restrict__ wv,
                                               unsigned short* __restrict__ wpack) {
  __shared__ __attribute__((aligned(16))) unsigned cT[(MAXB + 1) * CSTR];
  __shared__ __attribute__((aligned(16))) unsigned ovl[8];
  __shared__ int cnt[MAXB + 4];
  const long long tp0 = __builtin_amdgcn_s_memrealtime();
  const int tid = threadIdx.x, lane = tid & 63;
  const int rnd = __builtin_amdgcn_readfirstlane(tid >> 6);
  const int c = blockIdx.x;
  const int e0 = (rnd * NCH + c) * 256 + lane * 4;
  const auto rd = SRD(dst, E * 4), rs = SRD(src, E * 4);

  const i32x4 dd = __builtin_bit_cast(i32x4, __builtin_amdgcn_raw_buffer_load_b128(rd, e0 * 4, 0, 0));
  const i32x4 ss = __builtin_bit_cast(i32x4, __builtin_amdgcn_raw_buffer_load_b128(rs, e0 * 4, 0, 0));
  unsigned wv8[8];
  if (rnd == 15 && c < NFRAG) {
    const int nn = lane & 31, h = lane >> 5;
    const unsigned short* p;
    if (c < 24) {
      const int m = c >> 3, t = (c >> 1) & 3, ct = c & 1;
      p = ((m == 0) ? wb : (m == 1) ? wv : wa) + (32 * h + 8 * t) * 64 + 32 * ct + nn;
    } else if (c < 26) {
      p = wc + (8 * h) * 64 + 32 * (c & 1) + nn;
    } else {
      const int g = c - 26, t = (g >> 1) & 3, ct = g & 1;
      p = wu + (32 * h + 8 * t) * 64 + 32 * ct + nn;
    }
#pragma unroll
    for (int j = 0; j < 8; ++j) wv8[j] = p[j * 64];
  }
  if (tid < 2 * MAXB) *reinterpret_cast<u32x2*>(cT + (tid >> 1) * CSTR + (tid & 1) * 16) = u32x2{0u, 0u};
  else if (tid < 3 * MAXB + 4) cnt[tid - 2 * MAXB] = 0;
  else if (tid < 3 * MAXB + 12) ovl[tid - (3 * MAXB + 4)] = 0x100u;
  __syncthreads();
  if ((rnd * NCH + c) * 256 < E) {
    int bk[4], rk[4];
#pragma unroll
    for (int k = 0; k < 4; ++k) {
      const bool v = e0 + k < E;
      bk[k] = v ? (int)((unsigned)dd[k] / (unsigned)NPB) : MAXB + 1;
      rk[k] = LDS_ADD(&cnt[bk[k]], 1);
    }
    unsigned w[4];
    bool ovf = false;
#pragma unroll
    for (int k = 0; k < 4; ++k) {
      const unsigned s = (unsigned)(dd[k] - bk[k] * NPB);
      w[k] = ((unsigned)ss[k] << 16) | ((s & 7u) << 13) | ((unsigned)(lane * 4 + k) << 5) | (s >> 3);
      const int d = (rk[k] + (rk[k] < CAP / 2 ? 2 : 4)) & 31;
      unsigned* cp = cT + (rk[k] < CAP ? min(bk[k], MAXB) : MAXB) * CSTR;
      cp[d] = w[k];
      __hip_atomic_fetch_or(cp + (d & 16) + ((d >> 3) & 1), (unsigned)rnd << (4 * (d & 7)),
                            __ATOMIC_RELAXED, __HIP_MEMORY_SCOPE_WORKGROUP);
      ovf |= rk[k] >= CAP;
    }
    if (__builtin_amdgcn_ballot_w64(ovf) != 0) {
#pragma unroll 1
      for (int k = 0; k < 4; ++k) {
        const int bq = k == 0 ? bk[0] : k == 1 ? bk[1] : k == 2 ? bk[2] : bk[3];
        const int rq = k == 0 ? rk[0] : k == 1 ? rk[1] : k == 2 ? rk[2] : rk[3];
        const unsigned wq = k == 0 ? w[0] : k == 1 ? w[1] : k == 2 ? w[2] : w[3];
        if (bq < MAXB && rq >= CAP) {
          const int o = LDS_ADD(&cnt[MAXB], 1);
          OV[c * OVCAP + o] = u32x4{wq >> 9, ((unsigned)(e0 + k) << 5) | (wq & 31u), (unsigned)bq, 0u};
          if (o < 4) { ovl[2 * o] = wq; ovl[2 * o + 1] = ((unsigned)rnd << 12) | (unsigned)bq; }
        }
      }
    }
  }
  asm volatile("s_waitcnt lgkmcnt(0)\n\ts_barrier" ::: "memory");
  {
    const auto rc = SRD(TC, MAXB * NCH * CELL * 4);
    const int p = tid & 7;
    const int first = (CAP / 2) * (p >> 2) + 4 * (p & 3) - 2;
#pragma unroll
    for (int q = 0; q < 2; ++q) {
      const int b = (tid >> 3) + q * 128;
      const int m = cnt[b];
      u32x4 v = *reinterpret_cast<const u32x4*>(cT + b * CSTR + 4 * p);
      if ((p & 3) == 0) v[0] |= (unsigned)min(max(m - first - 2, 0), CAP / 2);
      if (b < nb && (WALL || (p & 3) == 0 || first < m)) CST((b * NCH + c) * CELL + 4 * p, v);
    }
    if (tid < 2) {
      u32x4 v = *reinterpret_cast<const u32x4*>(ovl + 4 * tid);
      v[1] |= (unsigned)cnt[MAXB] << 16;
      v[3] |= (unsigned)cnt[MAXB] << 16;
      __builtin_amdgcn_raw_buffer_store_b128(v, SRD(OVH, NCH * 32), c * 32 + tid * 16, 0, 17);
    }
  }
  if (rnd == 15 && c < NFRAG) {
    if (c < 8 || c == 24 || c == 25) {
#pragma unroll
      for (int j = 0; j < 8; ++j) {
        const unsigned u = __float_as_uint(__uint_as_float(wv8[j] << 16) * NLOG2E);
        wv8[j] = (u + 0x7FFFu + ((u >> 16) & 1u)) >> 16;
      }
    }
    const u32x4 v = {wv8[0] | (wv8[1] << 16), wv8[2] | (wv8[3] << 16), wv8[4] | (wv8[5] << 16), wv8[6] | (wv8[7] << 16)};
    __builtin_amdgcn_raw_buffer_store_b128(v, SRD(wpack, NFRAG * 1024), c * 1024 + lane * 16, 0, 17);
  }
  if (tid == 0) ((long long*)((char*)TC + (200u << 20)))[c] = tp0;
}

__device__ __forceinline__ NOPK unsigned pack_bf16(float lo, float hi) {
  typedef __attribute__((ext_vector_type(2))) float f2;
  typedef __attribute__((ext_vector_type(2))) __bf16 b2;
  f2 v = {lo, hi};
  b2 r = __builtin_convertvector(v, b2);
  return __builtin_bit_cast(unsigned, r);
}
#define WGSYNC() do { __builtin_amdgcn_fence(__ATOMIC_RELEASE, "workgroup"); __builtin_amdgcn_s_barrier(); \
                      __builtin_amdgcn_fence(__ATOMIC_ACQUIRE, "workgroup"); } while (0)
#define U2F(u) __builtin_bit_cast(float, (unsigned)(u))
constexpr int RING_SLOT = 5 * 1024;
constexpr int LCAP = 512;
constexpr int LPAD = LCAP + 96;
constexpr int SSCR = 32 * 80;
constexpr int WAVE_LDS = 2 * RING_SLOT;
constexpr int M_WL = 0;
constexpr int M_OH = 16 * 1024;
constexpr int M_CNT = M_OH + 2048;
constexpr int M_ST = M_CNT + 256;
constexpr int M_SC = M_ST + 2048;
constexpr int M_LX = M_SC + GPB * SSCR;
constexpr int M_LE = M_LX + GPB * LPAD * 4;
constexpr int M_WV = M_LE + GPB * LPAD * 4;
constexpr int M_XG = M_WV + 4 * WAVE_LDS;
constexpr int MAIN_LDS = M_WV + GPB * WAVE_LDS;
static_assert(M_LX % 256 == 0 && M_LE % 256 == 0 && M_LE < 65536 && M_LE + GPB * LPAD * 4 + NCH * CELL * 4 <= M_XG, "lists");
static_assert(MAIN_LDS <= 160 * 1024, "LDS");

__global__ __launch_bounds__(512, 2) NOPK void k_main(const unsigned short* __restrict__ x,
                                                      const unsigned short* __restrict__ eattr,
                                                      const char* __restrict__ wsp, int E,
                                                      int N, unsigned short* __restrict__ out) {
  __shared__ __attribute__((aligned(16))) char smem[MAIN_LDS];
  asm volatile("" :: "s"(x), "s"(eattr), "s"(wsp), "s"(E), "s"(N), "s"(out));
  const unsigned* TC = reinterpret_cast<const unsigned*>(wsp + W_TC);
  const u32x4* OVH = reinterpret_cast<const u32x4*>(wsp + W_OVH);
  const u32x4* OV = reinterpret_cast<const u32x4*>(wsp + W_OV);
  const char* wpack = wsp + W_WP;
  const long long t0 = __builtin_amdgcn_s_memrealtime();
  const int tid = threadIdx.x, lane = tid & 63;
  const int wave = __builtin_amdgcn_readfirstlane(tid >> 6);
  const int r = lane & 31, h = lane >> 5;
  const int b = blockIdx.x;

  const int pc = lane & 7;
  u32x4 tv[4], ovh;
  {
    const unsigned* cp = TC + ((size_t)b * NCH + wave * 32) * CELL;
    asm volatile("global_load_dwordx4 %0, %5, %6\n\tglobal_load_dwordx4 %1, %5, %6 offset:1024\n\t"
                 "global_load_dwordx4 %2, %5, %6 offset:2048\n\tglobal_load_dwordx4 %3, %5, %6 offset:3072\n\t"
                 "global_load_dwordx4 %4, %5, %7"
                 : "=&v"(tv[0]), "=&v"(tv[1]), "=&v"(tv[2]), "=&v"(tv[3]), "=&v"(ovh)
                 : "v"(lane * 16), "s"(cp), "s"(OVH + wave * 64) : "memory");
  }
#define GLDS(vo, rs, mv) asm volatile("s_mov_b32 m0, %2\n\ts_nop 0\n\tbuffer_load_dwordx4 %0, %1, 0 offen lds" \
                                      :: "v"(vo), "s"(rs), "s"(mv) : "memory")
  const auto rx = SRD(x, N * 128);
  const auto re = SRD(eattr, E * 32);
  const unsigned xc0 = (unsigned)(((lane & 7) ^ ((lane >> 2) & 4)) * 16);
  const unsigned lb = (unsigned)(size_t)(LAS char*)smem;
  LAS int* cnt = (LAS int*)(smem + M_CNT);
  if (tid < 32) cnt[tid] = (tid & 3) == 0 ? (tid >> 2) * (LPAD * 4) : (tid & 3) == 2 ? 127 : 0;
  __builtin_amdgcn_s_setprio(1);
  {
    unsigned* oh = reinterpret_cast<unsigned*>(smem + M_OH);
    const int i = tid;
    const int sl = i >> 4, hh = (i >> 3) & 1, ks = (i >> 2) & 1, jp = i & 3;
    const int j0 = 2 * jp, j1 = 2 * jp + 1;
    const int nd0 = 16 * ks + 8 * (j0 >> 2) + 4 * hh + (j0 & 3);
    const int nd1 = 16 * ks + 8 * (j1 >> 2) + 4 * hh + (j1 & 3);
    oh[i] = (sl == nd0 ? 0x3F80u : 0u) | (sl == nd1 ? 0x3F800000u : 0u);
  }
  asm volatile("s_waitcnt lgkmcnt(0)\n\ts_barrier" ::: "memory");
  {
    const int nw = (min(NPB - wave, N - b * NPB - wave) + 7) >> 3;
#pragma unroll
    for (int k = 0; k < 4; ++k) {
      const int s = (lane >> 3) * 4 + k;
      const unsigned xo = s < nw ? (unsigned)(b * NPB + wave + 8 * s) << 7 : 0xFFFFFF80u;
      GLDS(xo | (xc0 ^ (16u * k)), rx, lb + M_XG + wave * 4096 + k * 1024);
    }
  }
  const LAS bf16x8* wl = (const LAS bf16x8*)(smem + M_WL);
  const LAS bf16x8* ohl = (const LAS bf16x8*)(smem + M_OH);
  LAS unsigned* lxa = (LAS unsigned*)(smem + M_LX);
  LAS unsigned* lea = (LAS unsigned*)(smem + M_LE);
  char* wbase = smem + M_WV + wave * WAVE_LDS;
  const unsigned ring_a = (unsigned)(size_t)(LAS char*)wbase;
  {
    unsigned long long vm[16];
    unsigned ad[16], xe[16], hw[4];
    const unsigned four = 4u;
#pragma unroll
    for (int k = 0; k < 4; ++k) {
      asm volatile("s_waitcnt vmcnt(%1)" : "+v"(tv[k]) : "n"(8 - k) : "memory");
      const unsigned h0 = (unsigned)__builtin_amdgcn_mov_dpp((int)tv[k][0], 0x00, 0xF, 0xF, true);
      const unsigned h1 = (unsigned)__builtin_amdgcn_mov_dpp((int)tv[k][1], 0x00, 0xF, 0xF, true);
      hw[k] = ((lane & 2) ? h1 : h0) >> ((lane & 1) * 16);
      const unsigned m = h0 & 0xFFu;
      unsigned ci[4];
#pragma unroll
      for (int j = 0; j < 4; ++j) {
        vm[4 * k + j] = __builtin_amdgcn_ballot_w64((unsigned)((lane & 3) * 4 + j - 2) < m);
        xe[4 * k + j] = tv[k][j] >> 9;
        ci[j] = (xe[4 * k + j] & 0x70u) + lb;
      }
      asm volatile("s_mov_b64 exec, %4\n\tds_add_rtn_u32 %0, %8, %12 offset:%13\n\t"
                   "s_mov_b64 exec, %5\n\tds_add_rtn_u32 %1, %9, %12 offset:%13\n\t"
                   "s_mov_b64 exec, %6\n\tds_add_rtn_u32 %2, %10, %12 offset:%13\n\t"
                   "s_mov_b64 exec, %7\n\tds_add_rtn_u32 %3, %11, %12 offset:%13\n\t"
                   "s_mov_b64 exec, -1"
                   : "=&v"(ad[4 * k]), "=&v"(ad[4 * k + 1]), "=&v"(ad[4 * k + 2]), "=&v"(ad[4 * k + 3])
                   : "s"(vm[4 * k]), "s"(vm[4 * k + 1]), "s"(vm[4 * k + 2]), "s"(vm[4 * k + 3]),
                     "v"(ci[0]), "v"(ci[1]), "v"(ci[2]), "v"(ci[3]), "v"(four), "n"(M_CNT)
                   : "memory");
    }
#pragma unroll
    for (int k = 0; k < 4; ++k) {
      const unsigned c13 = (unsigned)(wave * 32 + k * 8 + (lane >> 3)) << 13;
      unsigned ee[4];
#pragma unroll
      for (int j = 0; j < 4; ++j) ee[j] = (tv[k][j] & 0x1FFFu) | ((((hw[k] >> (4 * j)) & 15u) << 21) | c13);
      asm volatile("s_waitcnt lgkmcnt(12)\n\t"
                   "s_mov_b64 exec, %0\n\tds_write2st64_b32 %4, %8, %12 offset0:%16 offset1:%17\n\t"
                   "s_mov_b64 exec, %1\n\tds_write2st64_b32 %5, %9, %13 offset0:%16 offset1:%17\n\t"
                   "s_mov_b64 exec, %2\n\tds_write2st64_b32 %6, %10, %14 offset0:%16 offset1:%17\n\t"
                   "s_mov_b64 exec, %3\n\tds_write2st64_b32 %7, %11, %15 offset0:%16 offset1:%17\n\t"
                   "s_mov_b64 exec, -1"
                   :: "s"(vm[4 * k]), "s"(vm[4 * k + 1]), "s"(vm[4 * k + 2]), "s"(vm[4 * k + 3]),
                      "v"(ad[4 * k]), "v"(ad[4 * k + 1]), "v"(ad[4 * k + 2]), "v"(ad[4 * k + 3]),
                      "v"(xe[4 * k]), "v"(xe[4 * k + 1]), "v"(xe[4 * k + 2]), "v"(xe[4 * k + 3]),
                      "v"(ee[0]), "v"(ee[1]), "v"(ee[2]), "v"(ee[3]), "n"(M_LX / 256), "n"(M_LE / 256)
                   : "memory");
    }
  }
    {
      const auto rw = SRD(wpack, NFRAG * 1024);
      const unsigned vo = (unsigned)(wave * 1024 + lane * 16);
      GLDS(vo + 16384u, rw, lb + M_SC + 8192 + wave * 1024);
      GLDS(vo, rw, lb + M_SC + wave * 1024);
      asm volatile("s_mov_b32 m0, %2\n\ts_nop 0\n\tbuffer_load_dword %0, %1, 0 offen lds"
                   :: "v"(24576u + (vo >> 2)), "s"(rw), "s"(lb + M_SC + 16384 + wave * 256) : "memory");
      GLDS(vo + 8192u, rw, lb + M_WL + wave * 1024);
      GLDS(vo + 26624u, rw, lb + M_WL + 8192 + wave * 1024);
    }
  asm volatile("s_waitcnt vmcnt(9)" : "+v"(ovh) :: "memory");
  if (__builtin_amdgcn_ballot_w64((ovh[1] & 0x100u) == 0u) != 0) {
#pragma unroll
    for (int q = 0; q < 2; ++q) {
      const unsigned w = ovh[2 * q], m = ovh[2 * q + 1];
      if ((m & 0x1FFu) == (unsigned)b) {
        LAS unsigned* p = (LAS unsigned*)((LAS char*)lxa + LDS_ADD((LAS int*)((LAS char*)cnt + ((w >> 9) & 0x70u)), 4));
        p[0] = w >> 9;
        p[GPB * LPAD] = (w & 0x1FFFu) | ((m & 0xF000u) << 9) | ((unsigned)(tid >> 1) << 13);
      }
    }
    if ((ovh[1] >> 16) > 4u) cnt[1] = 1;
  }
  asm volatile("s_waitcnt lgkmcnt(0)\n\ts_barrier" ::: "memory");
  const int cpc = cnt[4 * pc] - pc * (LPAD * 4);
  const int cpa = cnt[4 * (lane >> 3)] - (lane >> 3) * (LPAD * 4);
  bool ready = __builtin_amdgcn_ballot_w64(cpc > LCAP * 4) == 0;
  int grp;
  {
    const unsigned long long gt = __builtin_amdgcn_ballot_w64(cpc * 2 + pc > cpa * 2 + (lane >> 3));
    const unsigned wd = h ? (unsigned)(gt >> 32) : (unsigned)gt;
    const int rk = __builtin_popcount((wd >> (r & 24)) & 0xFFu);
    grp = __builtin_ctzll(__builtin_amdgcn_ballot_w64(rk == (wave < 4 ? wave : 11 - wave))) >> 3;
  }
  const int gn0 = b * NPB + grp;
  const int nodes = max((min(NPB - grp, N - gn0) + 7) >> 3, 0);
  const int n0 = min(gn0, N - 1);
  ((LAS unsigned*)(smem + M_ST))[tid] = (ready ? 0x40000u : 0xFFFFu) | ((unsigned)(tid & 1) << 16);

  f32x16 Y[4];
#pragma unroll
  for (int q = 0; q < 4; ++q)
#pragma unroll
    for (int i = 0; i < 16; ++i) Y[q][i] = 0.f;

  const unsigned ec0 = (unsigned)(((lane & 1) ^ ((lane >> 4) & 1)) * 16);
  const LAS unsigned* lx = lxa + grp * LPAD;
  const LAS unsigned* le = lea + grp * LPAD;
  const int ohoff = ((((r >> 2) & 1) * 16) + ((r >> 4) * 8) + (((r >> 3) & 1) * 4) + (r & 3)) * 2;
  const int xa0 = (r & 3) * 1024 + (r >> 2) * 128 + (((r & 3) | ((h ^ ((r >> 3) & 1)) << 2)) * 16);
  const int ea0 = 4096 + r * 32 + ((h ^ ((r >> 3) & 1)) * 16);
#define SB() __builtin_amdgcn_sched_barrier(0)
  bf16x8 wB[4][2], wC[2], XAf[2][2];
  const LAS bf16x8* wvl = wl + lane;
  f32x16 G0, G1, V;
  u32x4 pd[2], pn[2];
  bf16x8 S[2], St[2], as[4], ae, bv[4];
  const f32x16 zero16 = {0.f, 0.f, 0.f, 0.f, 0.f, 0.f, 0.f, 0.f, 0.f, 0.f, 0.f, 0.f, 0.f, 0.f, 0.f, 0.f};
  LAS char* sm = (LAS char*)(smem + M_SC + wave * SSCR);
  LAS u32x4* mine = (LAS u32x4*)(sm + r * 80 + h * 32);
  const LAS char* ring = (const LAS char*)wbase;
  unsigned ga[5];
  unsigned slr;
  int sl;
  int rs = 0;

#define E1(i) Gc[i] = __builtin_amdgcn_exp2f(Gc[i])
#define E2(i) Gc[i] = Gc[i] + 1.0f
#define E3(i) Gc[i] = __builtin_amdgcn_rcpf(Gc[i])
#define MU(i) V[i] = V[i] * Gc[i]
#define CD(j) pd[(j) >> 2][(j) & 3] = pack_bf16(Gc[2 * (j)], Gc[2 * (j) + 1])
#define CN(j) pn[(j) >> 2][(j) & 3] = pack_bf16(V[2 * (j)], V[2 * (j) + 1])
#define XG(k) ((xo[k] & ~127u) | (xc0 ^ (16u * (k))))
#define BC(v) __builtin_bit_cast(bf16x8, v)

  int more, ntiles;
  LAS int* pown = cnt + 4 * wave + 2;
  const LAS int* poth = cnt + 4 * (wave ^ 4) + 2;
  const LAS unsigned* lxp = lx + (lane >> 3) * 4;
  const LAS unsigned* lep = le + (lane >> 1);
  const LAS unsigned* lsp = le + r;
  auto round_begin = [&]() NOPK __attribute__((always_inline)) {
    const int cn = min((__builtin_amdgcn_readfirstlane(cnt[4 * grp]) - grp * (LPAD * 4)) >> 2, LCAP);
    more = __builtin_amdgcn_readfirstlane(cnt[1]);
    ntiles = (cn + 31) >> 5;
    {
      LAS unsigned* wx = lxa + grp * LPAD;
      LAS unsigned* we = lea + grp * LPAD;
      const int e0 = ntiles * 32;
      if (cn + r < e0) { wx[cn + r] = 0xFFFFFF80u; we[cn + r] = 0xFFFFFFFFu; }
      wx[e0 + lane] = lane < nodes ? (unsigned)(n0 + 8 * lane) << 7 : 0xFFFFFF80u;
      we[e0 + lane] = 0xFFFFFFFFu;
      wx[e0 + 64 + r] = 0xFFFFFF80u;
      we[e0 + 64 + r] = 0xFFFFFFFFu;
    }
  };
  auto gather = [&](int t, bool issue) NOPK __attribute__((always_inline)) {
    const u32x4 xo = *(const LAS u32x4*)(lxp + t * 32);
    const unsigned eo = lep[t * 32];
    ga[0] = XG(0); ga[1] = XG(1); ga[2] = XG(2); ga[3] = XG(3);
    ga[4] = (eo & ~31u) | ec0;
    if (issue) {
      const unsigned d = ring_a + (rs ^ (t * RING_SLOT));
      GLDS(ga[0], rx, d); GLDS(ga[1], rx, d + 1024); GLDS(ga[2], rx, d + 2048); GLDS(ga[3], rx, d + 3072);
      GLDS(ga[4], re, d + 4096);
    }
  };
  if (ready) {
    round_begin();
    if (wave < 4) gather(0, true);
  }
  asm volatile("s_cmp_lg_u32 %0, 0\n\ts_cbranch_scc1 1f\n\ts_waitcnt vmcnt(0)\n1:\n\ts_waitcnt vmcnt(5)\n\ts_barrier"
               :: "s"(__builtin_amdgcn_readfirstlane((ready && wave < 4) ? 1 : 0)) : "memory", "scc");
  {
      const LAS bf16x8* ws = (const LAS bf16x8*)(smem + M_SC) + lane;
      bf16x8 xg[4];
#pragma unroll
      for (int k = 0; k < 4; ++k) xg[k] = *(const LAS bf16x8*)(smem + M_XG + grp * 4096 + (xa0 ^ (16 * k)));
#pragma unroll
      for (int ct = 0; ct < 2; ++ct) {
        f32x16 A = zero16;
#pragma unroll
        for (int k = 0; k < 4; ++k) A = MFMA32(xg[k], ws[(8 + k * 2 + ct) * 64], A);
#pragma unroll
        for (int s = 0; s < 2; ++s) {
          u32x4 p;
#pragma unroll
          for (int j = 0; j < 4; ++j) p[j] = pack_bf16(A[8 * s + 2 * j] * NLOG2E, A[8 * s + 2 * j + 1] * NLOG2E);
          XAf[s][ct] = BC(p);
        }
      }
#pragma unroll
      for (int k = 0; k < 8; ++k) wB[k >> 1][k & 1] = ws[k * 64];
      wC[0] = ws[16 * 64];
      wC[1] = ws[17 * 64];
      asm volatile("s_waitcnt lgkmcnt(0)\n\ts_barrier"
                   : "+v"(wB[0][0]), "+v"(wB[0][1]), "+v"(wB[1][0]), "+v"(wB[1][1]), "+v"(wB[2][0]), "+v"(wB[2][1]),
                     "+v"(wB[3][0]), "+v"(wB[3][1]), "+v"(wC[0]), "+v"(wC[1]) :: "memory");
      const u32x4 z4 = {0u, 0u, 0u, 0u};
      mine[0] = z4;
      mine[1] = z4;
    }
  for (;;) {
   if (!ready) {
    asm volatile("s_waitcnt vmcnt(0)" ::: "memory");
    WGSYNC();
    {
      int t2 = (int)__builtin_amdgcn_mbcnt_hi(~0u, __builtin_amdgcn_mbcnt_lo(~0u, 0u));
      asm volatile("" : "+v"(t2));
      const int l2 = t2;
      t2 += wave * 64;
      if (t2 < 32) cnt[t2] = (t2 & 3) == 0 ? (t2 >> 2) * (LPAD * 4) : 0;
      WGSYNC();
      auto put = [&](unsigned t, unsigned e) NOPK -> bool {
        const unsigned gg = (t >> 4) & 7u;
        const int ad = LDS_ADD(&cnt[4 * gg], 4) >> 2;
        if (ad - (int)gg * LPAD >= LCAP) return false;
        lxa[ad] = t;
        lea[ad] = e;
        return true;
      };
      LAS unsigned* stp = (LAS unsigned*)(smem + M_ST) + t2;
      unsigned pe = *stp & 0xFFFFu, ocur = *stp >> 16;
#pragma unroll 1
      for (int j = 0; j < 16; ++j) {
        if ((pe >> j) & 1u) {
          const int cc = wave * 32 + (j >> 2) * 8 + (l2 >> 3);
          const unsigned* cp = TC + ((size_t)b * NCH + cc) * CELL;
          const int d = (l2 & 7) * 4 + (j & 3);
          const unsigned w = cp[d];
          const unsigned nib = (cp[(d & 16) + ((d >> 3) & 1)] >> (4 * (d & 7))) & 15u;
          if ((unsigned)((d & 15) - 2) >= (cp[d & 16] & 0xFFu) ||
              put(w >> 9, (w & 0x1FFFu) | (nib << 21) | ((unsigned)cc << 13))) pe &= ~(1u << j);
        }
      }
      const unsigned on = OVH[t2][1] >> 16;
      const size_t ob = (size_t)(t2 >> 1) * OVCAP;
#pragma unroll 1
      for (; ocur < on; ocur += 2) {
        const u32x4 v = OV[ob + ocur];
        if ((int)v[2] == b && !put(v[0], v[1])) break;
      }
      if (pe != 0u || ocur < on) cnt[1] = 1;
      *stp = pe | (ocur << 16);
    }
    WGSYNC();
    round_begin();
   }
    if (!ready || wave >= 4) gather(0, true);
    gather(1, true);
    gather(2, false);
    ready = false;
    sl = lsp[0] & 31u;
    St[0] = ohl[sl * 4 + h * 2 + 0];
    St[1] = ohl[sl * 4 + h * 2 + 1];
    bv[0] = wvl[0 * 64];
    bv[1] = wvl[2 * 64];
    asm volatile("s_waitcnt vmcnt(5)" ::: "memory");
#pragma unroll
    for (int k = 0; k < 4; ++k) as[k] = *(const LAS bf16x8*)(ring + rs + (xa0 ^ (16 * k)));
    ae = *(const LAS bf16x8*)(ring + rs + ea0);
    G0 = MFMA32(St[0], XAf[0][0], zero16);
    G0 = MFMA32(St[1], XAf[1][0], G0);
    G0 = MFMA32(ae, wC[0], G0);
#pragma unroll
    for (int k = 0; k < 4; ++k) G0 = MFMA32(as[k], wB[k][0], G0);
    pd[1] = u32x4{0u, 0u, 0u, 0u};
    pn[1] = u32x4{0u, 0u, 0u, 0u};
    S[1] = BC(pd[1]);
    SB();
    for (int i = 0; i < ntiles; ++i) {
      {
        f32x16& Gc = G0;
        const unsigned d = ring_a + rs;
        LAS unsigned short* ohw = (LAS unsigned short*)(sm + sl * 80 + ohoff);
        Y[1] = MFMA32(S[1], BC(pd[1]), Y[1]);
        bv[2] = wvl[4 * 64];
        *(volatile LAS int*)pown = ntiles - i;
        E1(0); E1(1); E1(2); E1(3);
        SB();
        Y[3] = MFMA32(S[1], BC(pn[1]), Y[3]);
        bv[3] = wvl[6 * 64];
        E2(0); E2(1); E1(4); E1(5); E2(2); E2(3);
        SB();
        V = MFMA32(as[0], bv[0], zero16);
        const int prem = *(volatile const LAS int*)poth;
        E3(0); E3(1); E1(6); E1(7); E2(4); E2(5);
        SB();
        V = MFMA32(as[1], bv[1], V);
        E3(2); E3(3); E1(8); E1(9); E2(6); E2(7);
        SB();
        V = MFMA32(as[2], bv[2], V);
        E3(4); E3(5); E1(10); E1(11); E2(8); E2(9); CD(0);
        SB();
        V = MFMA32(as[3], bv[3], V);
        E3(6); E3(7); E1(12); E1(13); E2(10); E2(11); CD(1);
        SB();
        G1 = MFMA32(St[0], XAf[0][1], zero16);
        asm volatile("s_cmp_ge_i32 %0, %1\n\ts_setprio 0\n\ts_cbranch_scc0 1f\n\ts_setprio 1\n1:"
                     :: "s"(ntiles - i), "s"(__builtin_amdgcn_readfirstlane(prem)) : "scc");
        E3(8); E3(9); E1(14); E1(15); E2(12); E2(13); CD(2);
        SB();
        G1 = MFMA32(St[1], XAf[1][1], G1);
        *ohw = (unsigned short)0x3F80;
        asm volatile("" ::: "memory");
        S[0] = BC(mine[0]);
        S[1] = BC(mine[1]);
        asm volatile("" ::: "memory");
        *ohw = (unsigned short)0;
        E3(10); E3(11); E2(14); E2(15); CD(3);
        SB();
        G1 = MFMA32(ae, wC[1], G1);
        GLDS(ga[0], rx, d);
        E3(12); E3(13); MU(0); MU(1); MU(2); MU(3);
        SB();
        G1 = MFMA32(as[0], wB[0][1], G1);
        GLDS(ga[1], rx, d + 1024);
        E3(14); E3(15); MU(4); MU(5); CN(0); CN(1);
        SB();
        G1 = MFMA32(as[1], wB[1][1], G1);
        GLDS(ga[2], rx, d + 2048);
        MU(6); MU(7); CD(4); CD(5); CN(2);
        SB();
        G1 = MFMA32(as[2], wB[2][1], G1);
        GLDS(ga[3], rx, d + 3072);
        MU(8); MU(9); MU(10); MU(11); CN(3); CD(6);
        SB();
        G1 = MFMA32(as[3], wB[3][1], G1);
        GLDS(ga[4], re, d + 4096);
        slr = lsp[(i + 1) * 32];
        MU(12); MU(13); CN(4); CN(5); CD(7);
        SB();
        Y[0] = MFMA32(S[0], BC(pd[0]), Y[0]);
        bv[0] = wvl[1 * 64];
        MU(14); MU(15); CN(6);
        SB();
        Y[2] = MFMA32(S[0], BC(pn[0]), Y[2]);
        bv[1] = wvl[3 * 64];
        CN(7);
        SB();
      }
      rs ^= RING_SLOT;
      {
        f32x16& Gc = G1;
        const LAS char* rp = ring + rs;
        sl = slr & 31u;
        Y[0] = MFMA32(S[1], BC(pd[1]), Y[0]);
        bv[2] = wvl[5 * 64];
        St[0] = ohl[sl * 4 + h * 2 + 0];
        E1(0); E1(1); E1(2); E1(3);
        SB();
        Y[2] = MFMA32(S[1], BC(pn[1]), Y[2]);
        bv[3] = wvl[7 * 64];
        St[1] = ohl[sl * 4 + h * 2 + 1];
        E2(0); E2(1); E1(4); E1(5); E2(2); E2(3);
        SB();
        V = MFMA32(as[0], bv[0], zero16);
        E3(0); E3(1); E1(6); E1(7); E2(4); E2(5);
        SB();
        V = MFMA32(as[1], bv[1], V);
        E3(2); E3(3); E1(8); E1(9); E2(6); E2(7);
        SB();
        V = MFMA32(as[2], bv[2], V);
        asm volatile("s_waitcnt vmcnt(5)" ::: "memory");
        ae = *(const LAS bf16x8*)(rp + ea0);
        E3(4); E3(5); E1(10); E1(11); E2(8); E2(9); CD(0);
        SB();
        V = MFMA32(as[3], bv[3], V);
        as[0] = *(const LAS bf16x8*)(rp + xa0);
        E3(6); E3(7); E1(12); E1(13); E2(10); E2(11); CD(1);
        SB();
        G0 = MFMA32(St[0], XAf[0][0], zero16);
        as[1] = *(const LAS bf16x8*)(rp + (xa0 ^ 16));
        E3(8); E3(9); E1(14); E1(15); E2(12); E2(13); CD(2);
        SB();
        G0 = MFMA32(St[1], XAf[1][0], G0);
        as[2] = *(const LAS bf16x8*)(rp + (xa0 ^ 32));
        E3(10); E3(11); E2(14); E2(15); CD(3);
        SB();
        G0 = MFMA32(ae, wC[0], G0);
        as[3] = *(const LAS bf16x8*)(rp + (xa0 ^ 48));
        E3(12); E3(13); MU(0); MU(1); MU(2); MU(3);
        SB();
        G0 = MFMA32(as[0], wB[0][0], G0);
        const u32x4 xo = *(const LAS u32x4*)(lxp + (i + 3) * 32);
        const unsigned eo = lep[(i + 3) * 32];
        E3(14); E3(15); MU(4); MU(5); CN(0); CN(1);
        SB();
        G0 = MFMA32(as[1], wB[1][0], G0);
        MU(6); MU(7); CD(4); CD(5); CN(2);
        SB();
        G0 = MFMA32(as[2], wB[2][0], G0);
        MU(8); MU(9); MU(10); MU(11); CN(3); CD(6);
        SB();
        G0 = MFMA32(as[3], wB[3][0], G0);
        ga[0] = XG(0); ga[1] = XG(1);
        MU(12); MU(13); CN(4); CN(5); CD(7);
        SB();
        Y[1] = MFMA32(S[0], BC(pd[0]), Y[1]);
        bv[0] = wvl[0 * 64];
        ga[2] = XG(2); ga[3] = XG(3);
        MU(14); MU(15); CN(6);
        SB();
        Y[3] = MFMA32(S[0], BC(pn[0]), Y[3]);
        bv[1] = wvl[2 * 64];
        ga[4] = (eo & ~31u) | ec0;
        CN(7);
        SB();
      }
    }
    *(volatile LAS int*)pown = 0;
    Y[1] = MFMA32(S[1], BC(pd[1]), Y[1]);
    Y[3] = MFMA32(S[1], BC(pn[1]), Y[3]);
    if (!more) break;
  }
  u32x4 xr[4];
#pragma unroll
  for (int q = 0; q < 4; ++q) {
    const int row = q * 8 + (lane >> 3);
    xr[q] = *(const LAS u32x4*)(ring + rs + (row & 3) * 1024 + (row >> 2) * 128 +
                                (((lane & 7) ^ (((row >> 3) & 1) << 2) ^ (row & 3)) * 16));
  }
  f32x16 U0 = zero16, U1 = zero16;
#pragma unroll
  for (int k = 0; k < 4; ++k) {
    U0 = MFMA32(as[k], wvl[(8 + k * 2) * 64], U0);
    U1 = MFMA32(as[k], wvl[(9 + k * 2) * 64], U1);
  }
  asm volatile("s_waitcnt vmcnt(0) lgkmcnt(0)" : "+v"(xr[0]), "+v"(xr[1]), "+v"(xr[2]), "+v"(xr[3]) :: "memory");
  LAS float* zl = (LAS float*)wbase;
#pragma unroll
  for (int i = 0; i < 16; ++i) {
    const int row = (i & 3) + 8 * (i >> 2) + 4 * h;
    const float z0 = U0[i] + Y[2][i] * __builtin_amdgcn_rcpf(Y[0][i] + 1e-6f);
    const float z1 = U1[i] + Y[3][i] * __builtin_amdgcn_rcpf(Y[1][i] + 1e-6f);
    zl[row * 68 + r] = z0 > 0.f ? z0 : 0.f;
    zl[row * 68 + 32 + r] = z1 > 0.f ? z1 : 0.f;
  }
#pragma unroll
  for (int q = 0; q < 4; ++q) {
    const int row = q * 8 + (lane >> 3);
    const f32x4 z0 = *(const LAS f32x4*)(&zl[row * 68 + (lane & 7) * 8]);
    const f32x4 z1 = *(const LAS f32x4*)(&zl[row * 68 + (lane & 7) * 8 + 4]);
    u32x4 o;
    o[0] = pack_bf16(U2F(xr[q][0] << 16) + z0[0], U2F(xr[q][0] & 0xffff0000u) + z0[1]);
    o[1] = pack_bf16(U2F(xr[q][1] << 16) + z0[2], U2F(xr[q][1] & 0xffff0000u) + z0[3]);
    o[2] = pack_bf16(U2F(xr[q][2] << 16) + z1[0], U2F(xr[q][2] & 0xffff0000u) + z1[1]);
    o[3] = pack_bf16(U2F(xr[q][3] << 16) + z1[2], U2F(xr[q][3] & 0xffff0000u) + z1[3]);
    if (row < nodes) __builtin_amdgcn_raw_buffer_store_b128(o, SRD(out, N * 128), (n0 + 8 * row) * 128 + (lane & 7) * 16, 0, 17);
  }
  if (b == 0 && wave == 0) {
    const volatile long long* tpp = (const volatile long long*)(wsp + W_TC + (200u << 20));
    const long long tp = tpp[0], tn = tpp[256];
    long long mn = tp, mx = tp;
    for (int i = 1; i < 256; ++i) { const long long v = tpp[i]; mn = v < mn ? v : mn; mx = v > mx ? v : mx; }
    const long long t4 = t0 + 6000;
    long long ex = 0;
    ex = ex < 0 ? 0 : ex > 20000 ? 20000 : ex;
    while (__builtin_amdgcn_s_memrealtime() < t4 + ex) __builtin_amdgcn_s_sleep(1);
  }
}

extern "C" void kernel_launch(void* const* d_in, const int* in_sizes, int n_in,
                              void* d_out, int out_size, void* d_ws, size_t ws_size,
                              hipStream_t stream) {
  const unsigned short* x = (const unsigned short*)d_in[0];
  const unsigned short* eattr = (const unsigned short*)d_in[1];
  const int* src = (const int*)d_in[2];
  const int* dst = (const int*)d_in[3];
  const unsigned short* wa = (const unsigned short*)d_in[4];
  const unsigned short* wb = (const unsigned short*)d_in[5];
  const unsigned short* wc = (const unsigned short*)d_in[6];
  const unsigned short* wu = (const unsigned short*)d_in[7];
  const unsigned short* wv = (const unsigned short*)d_in[8];
  unsigned short* out = (unsigned short*)d_out;
  const int N = in_sizes[0] / 64;
  const int E = in_sizes[2];
  const int nb = (N + NPB - 1) / NPB;

  char* ws = (char*)d_ws;
  k_part<<<NCH, 1024, 0, stream>>>(src, dst, E, nb, (unsigned*)(ws + W_TC), (u32x4*)(ws + W_OVH), (u32x4*)(ws + W_OV),
                                   wa, wb, wc, wu, wv, (unsigned short*)(ws + W_WP));
  k_main<<<nb, 512, 0, stream>>>(x, eattr, ws, E, N, out);
}
```

```cpp
#include <hip/hip_runtime.h>
#include <hip/hip_bf16.h>
#include <stdint.h>

using bf16x8 = __attribute__((ext_vector_type(8))) short;
using f32x16 = __attribute__((ext_vector_type(16))) float;
using u32x4 = __attribute__((ext_vector_type(4))) unsigned;
using u32x2 = __attribute__((ext_vector_type(2))) unsigned;
using i32x4 = __attribute__((ext_vector_type(4))) int;
using f32x4 = __attribute__((ext_vector_type(4))) float;

#define MFMA32(a, b, c) __builtin_amdgcn_mfma_f32_32x32x16_bf16((a), (b), (c), 0, 0, 0)
#define LAS __attribute__((address_space(3)))
#if defined(__HIP_DEVICE_COMPILE__)
#define NOPK __attribute__((target("no-packed-fp32-ops")))
#else
#define NOPK
#endif
#define SRD(p, bytes) __builtin_amdgcn_make_buffer_rsrc((void*)(p), (short)0, (int)(bytes), 0x00020000)
#define LDS_ADD(p, v) __hip_atomic_fetch_add((p), (v), __ATOMIC_RELAXED, __HIP_MEMORY_SCOPE_WORKGROUP)

#define CST(p, v) __builtin_amdgcn_raw_buffer_store_b128((v), rc, (p) * 4, 0, 17)
#define WALL 1
constexpr int GPB = 8;
constexpr int NPB = 196;
constexpr int MAXB = 256;
constexpr int NCH = 256;
constexpr int CELL = 32;
constexpr int CAP = 28;
constexpr int CSTR = 36;
constexpr int OVCAP = 4096;
constexpr int NFRAG = 34;
constexpr size_t W_TC = 0;
constexpr size_t W_OVH = W_TC + (size_t)MAXB * NCH * CELL * 4;
constexpr size_t W_WP = W_OVH + NCH * 32;
constexpr size_t W_OV = W_WP + NFRAG * 1024;
constexpr float NLOG2E = -1.44269504088896340736f;

__global__ __launch_bounds__(1024) void k_part(const int* __restrict__ src, const int* __restrict__ dst,
                                               int E, int nb,
                                               unsigned* __restrict__ TC,
                                               u32x4* __restrict__ OVH, u32x4* __restrict__ OV,
                                               const unsigned short* __restrict__ wa,
                                               const unsigned short* __restrict__ wb,
                                               const unsigned short* __restrict__ wc,
                                               const unsigned short* __restrict__ wu,
                                               const unsigned short* __restrict__ wv,
                                               unsigned short* __restrict__ wpack) {
  __shared__ __attribute__((aligned(16))) unsigned cT[(MAXB + 1) * CSTR];
  __shared__ __attribute__((aligned(16))) unsigned ovl[8];
  __shared__ int cnt[MAXB + 4];
  const int tid = threadIdx.x, lane = tid & 63;
  const int rnd = __builtin_amdgcn_readfirstlane(tid >> 6);
  const int c = blockIdx.x;
  const int e0 = (rnd * NCH + c) * 256 + lane * 4;
  const auto rd = SRD(dst, E * 4), rs = SRD(src, E * 4);

  const i32x4 dd = __builtin_bit_cast(i32x4, __builtin_amdgcn_raw_buffer_load_b128(rd, e0 * 4, 0, 0));
  const i32x4 ss = __builtin_bit_cast(i32x4, __builtin_amdgcn_raw_buffer_load_b128(rs, e0 * 4, 0, 0));
  unsigned wv8[8];
  if (rnd == 15 && c < NFRAG) {
    const int nn = lane & 31, h = lane >> 5;
    const unsigned short* p;
    if (c < 24) {
      const int m = c >> 3, t = (c >> 1) & 3, ct = c & 1;
      p = ((m == 0) ? wb : (m == 1) ? wv : wa) + (32 * h + 8 * t) * 64 + 32 * ct + nn;
    } else if (c < 26) {
      p = wc + (8 * h) * 64 + 32 * (c & 1) + nn;
    } else {
      const int g = c - 26, t = (g >> 1) & 3, ct = g & 1;
      p = wu + (32 * h + 8 * t) * 64 + 32 * ct + nn;
    }
#pragma unroll
    for (int j = 0; j < 8; ++j) wv8[j] = p[j * 64];
  }
#define CPN 80
  u32x4 t;
  if (rnd == 14 && c < CPN) {
    unsigned long long pc;
    int rel;
    asm volatile("s_getpc_b64 %0\n\ts_mov_b32 %1, _Z6k_mainPKtS0_PKciiPt@rel32@lo+4" : "=s"(pc), "=s"(rel));
    pc += (long long)rel;
    asm volatile("global_load_dwordx4 %0, %1, %2" : "=v"(t) : "v"(((c >> 3) % 10) * 1024 + lane * 16), "s"(pc) : "memory");
  }
  if (tid < 2 * MAXB) *reinterpret_cast<u32x2*>(cT + (tid >> 1) * CSTR + (tid & 1) * 16) = u32x2{0u, 0u};
  else if (tid < 3 * MAXB + 4) cnt[tid - 2 * MAXB] = 0;
  else if (tid < 3 * MAXB + 12) ovl[tid - (3 * MAXB + 4)] = 0x100u;
  __syncthreads();
  if ((rnd * NCH + c) * 256 < E) {
    int bk[4], rk[4];
#pragma unroll
    for (int k = 0; k < 4; ++k) {
      const bool v = e0 + k < E;
      bk[k] = v ? (int)((unsigned)dd[k] / (unsigned)NPB) : MAXB + 1;
      rk[k] = LDS_ADD(&cnt[bk[k]], 1);
    }
    unsigned w[4];
    bool ovf = false;
#pragma unroll
    for (int k = 0; k < 4; ++k) {
      const unsigned s = (unsigned)(dd[k] - bk[k] * NPB);
      w[k] = ((unsigned)ss[k] << 16) | ((s & 7u) << 13) | ((unsigned)(lane * 4 + k) << 5) | (s >> 3);
      const int d = (rk[k] + (rk[k] < CAP / 2 ? 2 : 4)) & 31;
      unsigned* cp = cT + (rk[k] < CAP ? min(bk[k], MAXB) : MAXB) * CSTR;
      cp[d] = w[k];
      __hip_atomic_fetch_or(cp + (d & 16) + ((d >> 3) & 1), (unsigned)rnd << (4 * (d & 7)),
                            __ATOMIC_RELAXED, __HIP_MEMORY_SCOPE_WORKGROUP);
      ovf |= rk[k] >= CAP;
    }
    if (__builtin_amdgcn_ballot_w64(ovf) != 0) {
#pragma unroll 1
      for (int k = 0; k < 4; ++k) {
        const int bq = k == 0 ? bk[0] : k == 1 ? bk[1] : k == 2 ? bk[2] : bk[3];
        const int rq = k == 0 ? rk[0] : k == 1 ? rk[1] : k == 2 ? rk[2] : rk[3];
        const unsigned wq = k == 0 ? w[0] : k == 1 ? w[1] : k == 2 ? w[2] : w[3];
        if (bq < MAXB && rq >= CAP) {
          const int o = LDS_ADD(&cnt[MAXB], 1);
          OV[c * OVCAP + o] = u32x4{wq >> 9, ((unsigned)(e0 + k) << 5) | (wq & 31u), (unsigned)bq, 0u};
          if (o < 4) { ovl[2 * o] = wq; ovl[2 * o + 1] = ((unsigned)rnd << 12) | (unsigned)bq; }
        }
      }
    }
  }
  asm volatile("s_waitcnt lgkmcnt(0)\n\ts_barrier" ::: "memory");
  {
    const auto rc = SRD(TC, MAXB * NCH * CELL * 4);
    const int p = tid & 7;
    const int first = (CAP / 2) * (p >> 2) + 4 * (p & 3) - 2;
#pragma unroll
    for (int q = 0; q < 2; ++q) {
      const int b = (tid >> 3) + q * 128;
      const int m = cnt[b];
      u32x4 v = *reinterpret_cast<const u32x4*>(cT + b * CSTR + 4 * p);
      if ((p & 3) == 0) v[0] |= (unsigned)min(max(m - first - 2, 0), CAP / 2);
      if (b < nb && (WALL || (p & 3) == 0 || first < m)) CST((b * NCH + c) * CELL + 4 * p, v);
    }
    if (tid < 2) {
      u32x4 v = *reinterpret_cast<const u32x4*>(ovl + 4 * tid);
      v[1] |= (unsigned)cnt[MAXB] << 16;
      v[3] |= (unsigned)cnt[MAXB] << 16;
      __builtin_amdgcn_raw_buffer_store_b128(v, SRD(OVH, NCH * 32), c * 32 + tid * 16, 0, 17);
    }
  }
  if (rnd == 15 && c < NFRAG) {
    if (c < 8 || c == 24 || c == 25) {
#pragma unroll
      for (int j = 0; j < 8; ++j) {
        const unsigned u = __float_as_uint(__uint_as_float(wv8[j] << 16) * NLOG2E);
        wv8[j] = (u + 0x7FFFu + ((u >> 16) & 1u)) >> 16;
      }
    }
    const u32x4 v = {wv8[0] | (wv8[1] << 16), wv8[2] | (wv8[3] << 16), wv8[4] | (wv8[5] << 16), wv8[6] | (wv8[7] << 16)};
    __builtin_amdgcn_raw_buffer_store_b128(v, SRD(wpack, NFRAG * 1024), c * 1024 + lane * 16, 0, 17);
  }
  asm volatile("s_waitcnt vmcnt(0)" :: "v"(t) : "memory");
}

__device__ __forceinline__ NOPK unsigned pack_bf16(float lo, float hi) {
  typedef __attribute__((ext_vector_type(2))) float f2;
  typedef __attribute__((ext_vector_type(2))) __bf16 b2;
  f2 v = {lo, hi};
  b2 r = __builtin_convertvector(v, b2);
  return __builtin_bit_cast(unsigned, r);
}
#define WGSYNC() do { __builtin_amdgcn_fence(__ATOMIC_RELEASE, "workgroup"); __builtin_amdgcn_s_barrier(); \
                      __builtin_amdgcn_fence(__ATOMIC_ACQUIRE, "workgroup"); } while (0)
#define U2F(u) __builtin_bit_cast(float, (unsigned)(u))
constexpr int RING_SLOT = 5 * 1024;
constexpr int LCAP = 512;
constexpr int LPAD = LCAP + 96;
constexpr int SSCR = 32 * 80;
constexpr int WAVE_LDS = 2 * RING_SLOT;
constexpr int M_WL = 0;
constexpr int M_OH = 16 * 1024;
constexpr int M_CNT = M_OH + 2048;
constexpr int M_ST = M_CNT + 256;
constexpr int M_SC = M_ST + 2048;
constexpr int M_LX = M_SC + GPB * SSCR;
constexpr int M_LE = M_LX + GPB * LPAD * 4;
constexpr int M_WV = M_LE + GPB * LPAD * 4;
constexpr int M_XG = M_WV + 4 * WAVE_LDS;
constexpr int MAIN_LDS = M_WV + GPB * WAVE_LDS;
static_assert(M_LX % 256 == 0 && M_LE % 256 == 0 && M_LE < 65536 && M_LE + GPB * LPAD * 4 + NCH * CELL * 4 <= M_XG, "lists");
static_assert(MAIN_LDS <= 160 * 1024, "LDS");

__global__ __launch_bounds__(512, 2) NOPK void k_main(const unsigned short* __restrict__ x,
                                                      const unsigned short* __restrict__ eattr,
                                                      const char* __restrict__ wsp, int E,
                                                      int N, unsigned short* __restrict__ out) {
  __shared__ __attribute__((aligned(16))) char smem[MAIN_LDS];
  asm volatile("" :: "s"(x), "s"(eattr), "s"(wsp), "s"(E), "s"(N), "s"(out));
  const unsigned* TC = reinterpret_cast<const unsigned*>(wsp + W_TC);
  const u32x4* OVH = reinterpret_cast<const u32x4*>(wsp + W_OVH);
  const u32x4* OV = reinterpret_cast<const u32x4*>(wsp + W_OV);
  const char* wpack = wsp + W_WP;
  const int tid = threadIdx.x, lane = tid & 63;
  const int wave = __builtin_amdgcn_readfirstlane(tid >> 6);
  const int r = lane & 31, h = lane >> 5;
  const int b = blockIdx.x;

  const int pc = lane & 7;
  u32x4 tv[4], ovh;
  {
    const unsigned* cp = TC + ((size_t)b * NCH + wave * 32) * CELL;
    asm volatile("global_load_dwordx4 %0, %5, %6\n\tglobal_load_dwordx4 %1, %5, %6 offset:1024\n\t"
                 "global_load_dwordx4 %2, %5, %6 offset:2048\n\tglobal_load_dwordx4 %3, %5, %6 offset:3072\n\t"
                 "global_load_dwordx4 %4, %5, %7"
                 : "=&v"(tv[0]), "=&v"(tv[1]), "=&v"(tv[2]), "=&v"(tv[3]), "=&v"(ovh)
                 : "v"(lane * 16), "s"(cp), "s"(OVH + wave * 64) : "memory");
  }
#define GLDS(vo, rs, mv) asm volatile("s_mov_b32 m0, %2\n\ts_nop 0\n\tbuffer_load_dwordx4 %0, %1, 0 offen lds" \
                                      :: "v"(vo), "s"(rs), "s"(mv) : "memory")
  const auto rx = SRD(x, N * 128);
  const auto re = SRD(eattr, E * 32);
  const unsigned xc0 = (unsigned)(((lane & 7) ^ ((lane >> 2) & 4)) * 16);
  const unsigned lb = (unsigned)(size_t)(LAS char*)smem;
  LAS int* cnt = (LAS int*)(smem + M_CNT);
  if (tid < 32) cnt[tid] = (tid & 3) == 0 ? (tid >> 2) * (LPAD * 4) : (tid & 3) == 2 ? 127 : 0;
  __builtin_amdgcn_s_setprio(1);
  {
    unsigned* oh = reinterpret_cast<unsigned*>(smem + M_OH);
    const int i = tid;
    const int sl = i >> 4, hh = (i >> 3) & 1, ks = (i >> 2) & 1, jp = i & 3;
    const int j0 = 2 * jp, j1 = 2 * jp + 1;
    const int nd0 = 16 * ks + 8 * (j0 >> 2) + 4 * hh + (j0 & 3);
    const int nd1 = 16 * ks + 8 * (j1 >> 2) + 4 * hh + (j1 & 3);
    oh[i] = (sl == nd0 ? 0x3F80u : 0u) | (sl == nd1 ? 0x3F800000u : 0u);
  }
  asm volatile("s_waitcnt lgkmcnt(0)\n\ts_barrier" ::: "memory");
  {
    const int nw = (min(NPB - wave, N - b * NPB - wave) + 7) >> 3;
#pragma unroll
    for (int k = 0; k < 4; ++k) {
      const int s = (lane >> 3) * 4 + k;
      const unsigned xo = s < nw ? (unsigned)(b * NPB + wave + 8 * s) << 7 : 0xFFFFFF80u;
      GLDS(xo | (xc0 ^ (16u * k)), rx, lb + M_XG + wave * 4096 + k * 1024);
    }
  }
  const LAS bf16x8* wl = (const LAS bf16x8*)(smem + M_WL);
  const LAS bf16x8* ohl = (const LAS bf16x8*)(smem + M_OH);
  LAS unsigned* lxa = (LAS unsigned*)(smem + M_LX);
  LAS unsigned* lea = (LAS unsigned*)(smem + M_LE);
  char* wbase = smem + M_WV + wave * WAVE_LDS;
  const unsigned ring_a = (unsigned)(size_t)(LAS char*)wbase;
  {
    unsigned long long vm[16];
    unsigned ad[16], xe[16], hw[4];
    const unsigned four = 4u;
#pragma unroll
    for (int k = 0; k < 4; ++k) {
      asm volatile("s_waitcnt vmcnt(%1)" : "+v"(tv[k]) : "n"(8 - k) : "memory");
      const unsigned h0 = (unsigned)__builtin_amdgcn_mov_dpp((int)tv[k][0], 0x00, 0xF, 0xF, true);
      const unsigned h1 = (unsigned)__builtin_amdgcn_mov_dpp((int)tv[k][1], 0x00, 0xF, 0xF, true);
      hw[k] = ((lane & 2) ? h1 : h0) >> ((lane & 1) * 16);
      const unsigned m = h0 & 0xFFu;
      unsigned ci[4];
#pragma unroll
      for (int j = 0; j < 4; ++j) {
        vm[4 * k + j] = __builtin_amdgcn_ballot_w64((unsigned)((lane & 3) * 4 + j - 2) < m);
        xe[4 * k + j] = tv[k][j] >> 9;
        ci[j] = (xe[4 * k + j] & 0x70u) + lb;
      }
      asm volatile("s_mov_b64 exec, %4\n\tds_add_rtn_u32 %0, %8, %12 offset:%13\n\t"
                   "s_mov_b64 exec, %5\n\tds_add_rtn_u32 %1, %9, %12 offset:%13\n\t"
                   "s_mov_b64 exec, %6\n\tds_add_rtn_u32 %2, %10, %12 offset:%13\n\t"
                   "s_mov_b64 exec, %7\n\tds_add_rtn_u32 %3, %11, %12 offset:%13\n\t"
                   "s_mov_b64 exec, -1"
                   : "=&v"(ad[4 * k]), "=&v"(ad[4 * k + 1]), "=&v"(ad[4 * k + 2]), "=&v"(ad[4 * k + 3])
                   : "s"(vm[4 * k]), "s"(vm[4 * k + 1]), "s"(vm[4 * k + 2]), "s"(vm[4 * k + 3]),
                     "v"(ci[0]), "v"(ci[1]), "v"(ci[2]), "v"(ci[3]), "v"(four), "n"(M_CNT)
                   : "memory");
    }
#pragma unroll
    for (int k = 0; k < 4; ++k) {
      const unsigned c13 = (unsigned)(wave * 32 + k * 8 + (lane >> 3)) << 13;
      unsigned ee[4];
#pragma unroll
      for (int j = 0; j < 4; ++j) ee[j] = (tv[k][j] & 0x1FFFu) | ((((hw[k] >> (4 * j)) & 15u) << 21) | c13);
      asm volatile("s_waitcnt lgkmcnt(12)\n\t"
                   "s_mov_b64 exec, %0\n\tds_write2st64_b32 %4, %8, %12 offset0:%16 offset1:%17\n\t"
                   "s_mov_b64 exec, %1\n\tds_write2st64_b32 %5, %9, %13 offset0:%16 offset1:%17\n\t"
                   "s_mov_b64 exec, %2\n\tds_write2st64_b32 %6, %10, %14 offset0:%16 offset1:%17\n\t"
                   "s_mov_b64 exec, %3\n\tds_write2st64_b32 %7, %11, %15 offset0:%16 offset1:%17\n\t"
                   "s_mov_b64 exec, -1"
                   :: "s"(vm[4 * k]), "s"(vm[4 * k + 1]), "s"(vm[4 * k + 2]), "s"(vm[4 * k + 3]),
                      "v"(ad[4 * k]), "v"(ad[4 * k + 1]), "v"(ad[4 * k + 2]), "v"(ad[4 * k + 3]),
                      "v"(xe[4 * k]), "v"(xe[4 * k + 1]), "v"(xe[4 * k + 2]), "v"(xe[4 * k + 3]),
                      "v"(ee[0]), "v"(ee[1]), "v"(ee[2]), "v"(ee[3]), "n"(M_LX / 256), "n"(M_LE / 256)
                   : "memory");
    }
  }
    {
      const auto rw = SRD(wpack, NFRAG * 1024);
      const unsigned vo = (unsigned)(wave * 1024 + lane * 16);
      GLDS(vo + 16384u, rw, lb + M_SC + 8192 + wave * 1024);
      GLDS(vo, rw, lb + M_SC + wave * 1024);
      asm volatile("s_mov_b32 m0, %2\n\ts_nop 0\n\tbuffer_load_dword %0, %1, 0 offen lds"
                   :: "v"(24576u + (vo >> 2)), "s"(rw), "s"(lb + M_SC + 16384 + wave * 256) : "memory");
      GLDS(vo + 8192u, rw, lb + M_WL + wave * 1024);
      GLDS(vo + 26624u, rw, lb + M_WL + 8192 + wave * 1024);
    }
  asm volatile("s_waitcnt vmcnt(9)" : "+v"(ovh) :: "memory");
  if (__builtin_amdgcn_ballot_w64((ovh[1] & 0x100u) == 0u) != 0) {
#pragma unroll
    for (int q = 0; q < 2; ++q) {
      const unsigned w = ovh[2 * q], m = ovh[2 * q + 1];
      if ((m & 0x1FFu) == (unsigned)b) {
        LAS unsigned* p = (LAS unsigned*)((LAS char*)lxa + LDS_ADD((LAS int*)((LAS char*)cnt + ((w >> 9) & 0x70u)), 4));
        p[0] = w >> 9;
        p[GPB * LPAD] = (w & 0x1FFFu) | ((m & 0xF000u) << 9) | ((unsigned)(tid >> 1) << 13);
      }
    }
    if ((ovh[1] >> 16) > 4u) cnt[1] = 1;
  }
  asm volatile("s_waitcnt lgkmcnt(0)\n\ts_barrier" ::: "memory");
  const int cpc = cnt[4 * pc] - pc * (LPAD * 4);
  const int cpa = cnt[4 * (lane >> 3)] - (lane >> 3) * (LPAD * 4);
  bool ready = __builtin_amdgcn_ballot_w64(cpc > LCAP * 4) == 0;
  int grp;
  {
    const unsigned long long gt = __builtin_amdgcn_ballot_w64(cpc * 2 + pc > cpa * 2 + (lane >> 3));
    const unsigned wd = h ? (unsigned)(gt >> 32) : (unsigned)gt;
    const int rk = __builtin_popcount((wd >> (r & 24)) & 0xFFu);
    grp = __builtin_ctzll(__builtin_amdgcn_ballot_w64(rk == (wave < 4 ? wave : 11 - wave))) >> 3;
  }
  const int gn0 = b * NPB + grp;
  const int nodes = max((min(NPB - grp, N - gn0) + 7) >> 3, 0);
  const int n0 = min(gn0, N - 1);
  ((LAS unsigned*)(smem + M_ST))[tid] = (ready ? 0x40000u : 0xFFFFu) | ((unsigned)(tid & 1) << 16);

  f32x16 Y[4];
#pragma unroll
  for (int q = 0; q < 4; ++q)
#pragma unroll
    for (int i = 0; i < 16; ++i) Y[q][i] = 0.f;

  const unsigned ec0 = (unsigned)(((lane & 1) ^ ((lane >> 4) & 1)) * 16);
  const LAS unsigned* lx = lxa + grp * LPAD;
  const LAS unsigned* le = lea + grp * LPAD;
  const int ohoff = ((((r >> 2) & 1) * 16) + ((r >> 4) * 8) + (((r >> 3) & 1) * 4) + (r & 3)) * 2;
  const int xa0 = (r & 3) * 1024 + (r >> 2) * 128 + (((r & 3) | ((h ^ ((r >> 3) & 1)) << 2)) * 16);
  const int ea0 = 4096 + r * 32 + ((h ^ ((r >> 3) & 1)) * 16);
#define SB() __builtin_amdgcn_sched_barrier(0)
  bf16x8 wB[4][2], wC[2], XAf[2][2];
  const LAS bf16x8* wvl = wl + lane;
  f32x16 G0, G1, V;
  u32x4 pd[2], pn[2];
  bf16x8 S[2], St[2], as[4], ae, bv[4];
  const f32x16 zero16 = {0.f, 0.f, 0.f, 0.f, 0.f, 0.f, 0.f, 0.f, 0.f, 0.f, 0.f, 0.f, 0.f, 0.f, 0.f, 0.f};
  LAS char* sm = (LAS char*)(smem + M_SC + wave * SSCR);
  LAS u32x4* mine = (LAS u32x4*)(sm + r * 80 + h * 32);
  const LAS char* ring = (const LAS char*)wbase;
  unsigned ga[5];
  unsigned slr;
  int sl;
  int rs = 0;

#define E1(i) Gc[i] = __builtin_amdgcn_exp2f(Gc[i])
#define E2(i) Gc[i] = Gc[i] + 1.0f
#define E3(i) Gc[i] = __builtin_amdgcn_rcpf(Gc[i])
#define MU(i) V[i] = V[i] * Gc[i]
#define CD(j) pd[(j) >> 2][(j) & 3] = pack_bf16(Gc[2 * (j)], Gc[2 * (j) + 1])
#define CN(j) pn[(j) >> 2][(j) & 3] = pack_bf16(V[2 * (j)], V[2 * (j) + 1])
#define XG(k) ((xo[k] & ~127u) | (xc0 ^ (16u * (k))))
#define BC(v) __builtin_bit_cast(bf16x8, v)

  int more, ntiles;
  LAS int* pown = cnt + 4 * wave + 2;
  const LAS int* poth = cnt + 4 * (wave ^ 4) + 2;
  const LAS unsigned* lxp = lx + (lane >> 3) * 4;
  const LAS unsigned* lep = le + (lane >> 1);
  const LAS unsigned* lsp = le + r;
  auto round_begin = [&]() NOPK __attribute__((always_inline)) {
    const int cn = min((__builtin_amdgcn_readfirstlane(cnt[4 * grp]) - grp * (LPAD * 4)) >> 2, LCAP);
    more = __builtin_amdgcn_readfirstlane(cnt[1]);
    ntiles = (cn + 31) >> 5;
    {
      LAS unsigned* wx = lxa + grp * LPAD;
      LAS unsigned* we = lea + grp * LPAD;
      const int e0 = ntiles * 32;
      if (cn + r < e0) { wx[cn + r] = 0xFFFFFF80u; we[cn + r] = 0xFFFFFFFFu; }
      wx[e0 + lane] = lane < nodes ? (unsigned)(n0 + 8 * lane) << 7 : 0xFFFFFF80u;
      we[e0 + lane] = 0xFFFFFFFFu;
      wx[e0 + 64 + r] = 0xFFFFFF80u;
      we[e0 + 64 + r] = 0xFFFFFFFFu;
    }
  };
  auto gather = [&](int t, bool issue) NOPK __attribute__((always_inline)) {
    const u32x4 xo = *(const LAS u32x4*)(lxp + t * 32);
    const unsigned eo = lep[t * 32];
    ga[0] = XG(0); ga[1] = XG(1); ga[2] = XG(2); ga[3] = XG(3);
    ga[4] = (eo & ~31u) | ec0;
    if (issue) {
      const unsigned d = ring_a + (rs ^ (t * RING_SLOT));
      GLDS(ga[0], rx, d); GLDS(ga[1], rx, d + 1024); GLDS(ga[2], rx, d + 2048); GLDS(ga[3], rx, d + 3072);
      GLDS(ga[4], re, d + 4096);
    }
  };
  if (ready) {
    round_begin();
    if (wave < 4) gather(0, true);
  }
  asm volatile("s_cmp_lg_u32 %0, 0\n\ts_cbranch_scc1 1f\n\ts_waitcnt vmcnt(0)\n1:\n\ts_waitcnt vmcnt(5)\n\ts_barrier"
               :: "s"(__builtin_amdgcn_readfirstlane((ready && wave < 4) ? 1 : 0)) : "memory", "scc");
  {
      const LAS bf16x8* ws = (const LAS bf16x8*)(smem + M_SC) + lane;
      bf16x8 xg[4];
#pragma unroll
      for (int k = 0; k < 4; ++k) xg[k] = *(const LAS bf16x8*)(smem + M_XG + grp * 4096 + (xa0 ^ (16 * k)));
#pragma unroll
      for (int ct = 0; ct < 2; ++ct) {
        f32x16 A = zero16;
#pragma unroll
        for (int k = 0; k < 4; ++k) A = MFMA32(xg[k], ws[(8 + k * 2 + ct) * 64], A);
#pragma unroll
        for (int s = 0; s < 2; ++s) {
          u32x4 p;
#pragma unroll
          for (int j = 0; j < 4; ++j) p[j] = pack_bf16(A[8 * s + 2 * j] * NLOG2E, A[8 * s + 2 * j + 1] * NLOG2E);
          XAf[s][ct] = BC(p);
        }
      }
#pragma unroll
      for (int k = 0; k < 8; ++k) wB[k >> 1][k & 1] = ws[k * 64];
      wC[0] = ws[16 * 64];
      wC[1] = ws[17 * 64];
      asm volatile("s_waitcnt lgkmcnt(0)\n\ts_barrier"
                   : "+v"(wB[0][0]), "+v"(wB[0][1]), "+v"(wB[1][0]), "+v"(wB[1][1]), "+v"(wB[2][0]), "+v"(wB[2][1]),
                     "+v"(wB[3][0]), "+v"(wB[3][1]), "+v"(wC[0]), "+v"(wC[1]) :: "memory");
      const u32x4 z4 = {0u, 0u, 0u, 0u};
      mine[0] = z4;
      mine[1] = z4;
    }
  for (;;) {
   if (!ready) {
    asm volatile("s_waitcnt vmcnt(0)" ::: "memory");
    WGSYNC();
    {
      int t2 = (int)__builtin_amdgcn_mbcnt_hi(~0u, __builtin_amdgcn_mbcnt_lo(~0u, 0u));
      asm volatile("" : "+v"(t2));
      const int l2 = t2;
      t2 += wave * 64;
      if (t2 < 32) cnt[t2] = (t2 & 3) == 0 ? (t2 >> 2) * (LPAD * 4) : 0;
      WGSYNC();
      auto put = [&](unsigned t, unsigned e) NOPK -> bool {
        const unsigned gg = (t >> 4) & 7u;
        const int ad = LDS_ADD(&cnt[4 * gg], 4) >> 2;
        if (ad - (int)gg * LPAD >= LCAP) return false;
        lxa[ad] = t;
        lea[ad] = e;
        return true;
      };
      LAS unsigned* stp = (LAS unsigned*)(smem + M_ST) + t2;
      unsigned pe = *stp & 0xFFFFu, ocur = *stp >> 16;
#pragma unroll 1
      for (int j = 0; j < 16; ++j) {
        if ((pe >> j) & 1u) {
          const int cc = wave * 32 + (j >> 2) * 8 + (l2 >> 3);
          const unsigned* cp = TC + ((size_t)b * NCH + cc) * CELL;
          const int d = (l2 & 7) * 4 + (j & 3);
          const unsigned w = cp[d];
          const unsigned nib = (cp[(d & 16) + ((d >> 3) & 1)] >> (4 * (d & 7))) & 15u;
          if ((unsigned)((d & 15) - 2) >= (cp[d & 16] & 0xFFu) ||
              put(w >> 9, (w & 0x1FFFu) | (nib << 21) | ((unsigned)cc << 13))) pe &= ~(1u << j);
        }
      }
      const unsigned on = OVH[t2][1] >> 16;
      const size_t ob = (size_t)(t2 >> 1) * OVCAP;
#pragma unroll 1
      for (; ocur < on; ocur += 2) {
        const u32x4 v = OV[ob + ocur];
        if ((int)v[2] == b && !put(v[0], v[1])) break;
      }
      if (pe != 0u || ocur < on) cnt[1] = 1;
      *stp = pe | (ocur << 16);
    }
    WGSYNC();
    round_begin();
   }
    if (!ready || wave >= 4) gather(0, true);
    gather(1, true);
    gather(2, false);
    ready = false;
    sl = lsp[0] & 31u;
    St[0] = ohl[sl * 4 + h * 2 + 0];
    St[1] = ohl[sl * 4 + h * 2 + 1];
    bv[0] = wvl[0 * 64];
    bv[1] = wvl[2 * 64];
    asm volatile("s_waitcnt vmcnt(5)" ::: "memory");
#pragma unroll
    for (int k = 0; k < 4; ++k) as[k] = *(const LAS bf16x8*)(ring + rs + (xa0 ^ (16 * k)));
    ae = *(const LAS bf16x8*)(ring + rs + ea0);
    G0 = MFMA32(St[0], XAf[0][0], zero16);
    G0 = MFMA32(St[1], XAf[1][0], G0);
    G0 = MFMA32(ae, wC[0], G0);
#pragma unroll
    for (int k = 0; k < 4; ++k) G0 = MFMA32(as[k], wB[k][0], G0);
    pd[1] = u32x4{0u, 0u, 0u, 0u};
    pn[1] = u32x4{0u, 0u, 0u, 0u};
    S[1] = BC(pd[1]);
    SB();
    for (int i = 0; i < ntiles; ++i) {
      {
        f32x16& Gc = G0;
        const unsigned d = ring_a + rs;
        LAS unsigned short* ohw = (LAS unsigned short*)(sm + sl * 80 + ohoff);
        Y[1] = MFMA32(S[1], BC(pd[1]), Y[1]);
        bv[2] = wvl[4 * 64];
        *(volatile LAS int*)pown = ntiles - i;
        E1(0); E1(1); E1(2); E1(3);
        SB();
        Y[3] = MFMA32(S[1], BC(pn[1]), Y[3]);
        bv[3] = wvl[6 * 64];
        E2(0); E2(1); E1(4); E1(5); E2(2); E2(3);
        SB();
        V = MFMA32(as[0], bv[0], zero16);
        const int prem = *(volatile const LAS int*)poth;
        E3(0); E3(1); E1(6); E1(7); E2(4); E2(5);
        SB();
        V = MFMA32(as[1], bv[1], V);
        E3(2); E3(3); E1(8); E1(9); E2(6); E2(7);
        SB();
        V = MFMA32(as[2], bv[2], V);
        E3(4); E3(5); E1(10); E1(11); E2(8); E2(9); CD(0);
        SB();
        V = MFMA32(as[3], bv[3], V);
        E3(6); E3(7); E1(12); E1(13); E2(10); E2(11); CD(1);
        SB();
        G1 = MFMA32(St[0], XAf[0][1], zero16);
        asm volatile("s_cmp_ge_i32 %0, %1\n\ts_setprio 0\n\ts_cbranch_scc0 1f\n\ts_setprio 1\n1:"
                     :: "s"(ntiles - i), "s"(__builtin_amdgcn_readfirstlane(prem)) : "scc");
        E3(8); E3(9); E1(14); E1(15); E2(12); E2(13); CD(2);
        SB();
        G1 = MFMA32(St[1], XAf[1][1], G1);
        *ohw = (unsigned short)0x3F80;
        asm volatile("" ::: "memory");
        S[0] = BC(mine[0]);
        S[1] = BC(mine[1]);
        asm volatile("" ::: "memory");
        *ohw = (unsigned short)0;
        E3(10); E3(11); E2(14); E2(15); CD(3);
        SB();
        G1 = MFMA32(ae, wC[1], G1);
        GLDS(ga[0], rx, d);
        E3(12); E3(13); MU(0); MU(1); MU(2); MU(3);
        SB();
        G1 = MFMA32(as[0], wB[0][1], G1);
        GLDS(ga[1], rx, d + 1024);
        E3(14); E3(15); MU(4); MU(5); CN(0); CN(1);
        SB();
        G1 = MFMA32(as[1], wB[1][1], G1);
        GLDS(ga[2], rx, d + 2048);
        MU(6); MU(7); CD(4); CD(5); CN(2);
        SB();
        G1 = MFMA32(as[2], wB[2][1], G1);
        GLDS(ga[3], rx, d + 3072);
        MU(8); MU(9); MU(10); MU(11); CN(3); CD(6);
        SB();
        G1 = MFMA32(as[3], wB[3][1], G1);
        GLDS(ga[4], re, d + 4096);
        slr = lsp[(i + 1) * 32];
        MU(12); MU(13); CN(4); CN(5); CD(7);
        SB();
        Y[0] = MFMA32(S[0], BC(pd[0]), Y[0]);
        bv[0] = wvl[1 * 64];
        MU(14); MU(15); CN(6);
        SB();
        Y[2] = MFMA32(S[0], BC(pn[0]), Y[2]);
        bv[1] = wvl[3 * 64];
        CN(7);
        SB();
      }
      rs ^= RING_SLOT;
      {
        f32x16& Gc = G1;
        const LAS char* rp = ring + rs;
        sl = slr & 31u;
        Y[0] = MFMA32(S[1], BC(pd[1]), Y[0]);
        bv[2] = wvl[5 * 64];
        St[0] = ohl[sl * 4 + h * 2 + 0];
        E1(0); E1(1); E1(2); E1(3);
        SB();
        Y[2] = MFMA32(S[1], BC(pn[1]), Y[2]);
        bv[3] = wvl[7 * 64];
        St[1] = ohl[sl * 4 + h * 2 + 1];
        E2(0); E2(1); E1(4); E1(5); E2(2); E2(3);
        SB();
        V = MFMA32(as[0], bv[0], zero16);
        E3(0); E3(1); E1(6); E1(7); E2(4); E2(5);
        SB();
        V = MFMA32(as[1], bv[1], V);
        E3(2); E3(3); E1(8); E1(9); E2(6); E2(7);
        SB();
        V = MFMA32(as[2], bv[2], V);
        asm volatile("s_waitcnt vmcnt(5)" ::: "memory");
        ae = *(const LAS bf16x8*)(rp + ea0);
        E3(4); E3(5); E1(10); E1(11); E2(8); E2(9); CD(0);
        SB();
        V = MFMA32(as[3], bv[3], V);
        as[0] = *(const LAS bf16x8*)(rp + xa0);
        E3(6); E3(7); E1(12); E1(13); E2(10); E2(11); CD(1);
        SB();
        G0 = MFMA32(St[0], XAf[0][0], zero16);
        as[1] = *(const LAS bf16x8*)(rp + (xa0 ^ 16));
        E3(8); E3(9); E1(14); E1(15); E2(12); E2(13); CD(2);
        SB();
        G0 = MFMA32(St[1], XAf[1][0], G0);
        as[2] = *(const LAS bf16x8*)(rp + (xa0 ^ 32));
        E3(10); E3(11); E2(14); E2(15); CD(3);
        SB();
        G0 = MFMA32(ae, wC[0], G0);
        as[3] = *(const LAS bf16x8*)(rp + (xa0 ^ 48));
        E3(12); E3(13); MU(0); MU(1); MU(2); MU(3);
        SB();
        G0 = MFMA32(as[0], wB[0][0], G0);
        const u32x4 xo = *(const LAS u32x4*)(lxp + (i + 3) * 32);
        const unsigned eo = lep[(i + 3) * 32];
        E3(14); E3(15); MU(4); MU(5); CN(0); CN(1);
        SB();
        G0 = MFMA32(as[1], wB[1][0], G0);
        MU(6); MU(7); CD(4); CD(5); CN(2);
        SB();
        G0 = MFMA32(as[2], wB[2][0], G0);
        MU(8); MU(9); MU(10); MU(11); CN(3); CD(6);
        SB();
        G0 = MFMA32(as[3], wB[3][0], G0);
        ga[0] = XG(0); ga[1] = XG(1);
        MU(12); MU(13); CN(4); CN(5); CD(7);
        SB();
        Y[1] = MFMA32(S[0], BC(pd[0]), Y[1]);
        bv[0] = wvl[0 * 64];
        ga[2] = XG(2); ga[3] = XG(3);
        MU(14); MU(15); CN(6);
        SB();
        Y[3] = MFMA32(S[0], BC(pn[0]), Y[3]);
        bv[1] = wvl[2 * 64];
        ga[4] = (eo & ~31u) | ec0;
        CN(7);
        SB();
      }
    }
    *(volatile LAS int*)pown = 0;
    Y[1] = MFMA32(S[1], BC(pd[1]), Y[1]);
    Y[3] = MFMA32(S[1], BC(pn[1]), Y[3]);
    if (!more) break;
  }
  u32x4 xr[4];
#pragma unroll
  for (int q = 0; q < 4; ++q) {
    const int row = q * 8 + (lane >> 3);
    xr[q] = *(const LAS u32x4*)(ring + rs + (row & 3) * 1024 + (row >> 2) * 128 +
                                (((lane & 7) ^ (((row >> 3) & 1) << 2) ^ (row & 3)) * 16));
  }
  f32x16 U0 = zero16, U1 = zero16;
#pragma unroll
  for (int k = 0; k < 4; ++k) {
    U0 = MFMA32(as[k], wvl[(8 + k * 2) * 64], U0);
    U1 = MFMA32(as[k], wvl[(9 + k * 2) * 64], U1);
  }
  asm volatile("s_waitcnt vmcnt(0) lgkmcnt(0)" : "+v"(xr[0]), "+v"(xr[1]), "+v"(xr[2]), "+v"(xr[3]) :: "memory");
  LAS float* zl = (LAS float*)wbase;
#pragma unroll
  for (int i = 0; i < 16; ++i) {
    const int row = (i & 3) + 8 * (i >> 2) + 4 * h;
    const float z0 = U0[i] + Y[2][i] * __builtin_amdgcn_rcpf(Y[0][i] + 1e-6f);
    const float z1 = U1[i] + Y[3][i] * __builtin_amdgcn_rcpf(Y[1][i] + 1e-6f);
    zl[row * 68 + r] = z0 > 0.f ? z0 : 0.f;
    zl[row * 68 + 32 + r] = z1 > 0.f ? z1 : 0.f;
  }
#pragma unroll
  for (int q = 0; q < 4; ++q) {
    const int row = q * 8 + (lane >> 3);
    const f32x4 z0 = *(const LAS f32x4*)(&zl[row * 68 + (lane & 7) * 8]);
    const f32x4 z1 = *(const LAS f32x4*)(&zl[row * 68 + (lane & 7) * 8 + 4]);
    u32x4 o;
    o[0] = pack_bf16(U2F(xr[q][0] << 16) + z0[0], U2F(xr[q][0] & 0xffff0000u) + z0[1]);
    o[1] = pack_bf16(U2F(xr[q][1] << 16) + z0[2], U2F(xr[q][1] & 0xffff0000u) + z0[3]);
    o[2] = pack_bf16(U2F(xr[q][2] << 16) + z1[0], U2F(xr[q][2] & 0xffff0000u) + z1[1]);
    o[3] = pack_bf16(U2F(xr[q][3] << 16) + z1[2], U2F(xr[q][3] & 0xffff0000u) + z1[3]);
    if (row < nodes) __builtin_amdgcn_raw_buffer_store_b128(o, SRD(out, N * 128), (n0 + 8 * row) * 128 + (lane & 7) * 16, 0, 17);
  }
}

extern "C" void kernel_launch(void* const* d_in, const int* in_sizes, int n_in,
                              void* d_out, int out_size, void* d_ws, size_t ws_size,
                              hipStream_t stream) {
  const unsigned short* x = (const unsigned short*)d_in[0];
  const unsigned short* eattr = (const unsigned short*)d_in[1];
  const int* src = (const int*)d_in[2];
  const int* dst = (const int*)d_in[3];
  const unsigned short* wa = (const unsigned short*)d_in[4];
  const unsigned short* wb = (const unsigned short*)d_in[5];
  const unsigned short* wc = (const unsigned short*)d_in[6];
  const unsigned short* wu = (const unsigned short*)d_in[7];
  const unsigned short* wv = (const unsigned short*)d_in[8];
  unsigned short* out = (unsigned short*)d_out;
  const int N = in_sizes[0] / 64;
  const int E = in_sizes[2];
  const int nb = (N + NPB - 1) / NPB;

  char* ws = (char*)d_ws;
  k_part<<<NCH, 1024, 0, stream>>>(src, dst, E, nb, (unsigned*)(ws + W_TC), (u32x4*)(ws + W_OVH), (u32x4*)(ws + W_OV),
                                   wa, wb, wc, wu, wv, (unsigned short*)(ws + W_WP));
  k_main<<<nb, 512, 0, stream>>>(x, eattr, ws, E, N, out);
}
```

```cpp
#include <hip/hip_runtime.h>
#include <hip/hip_bf16.h>
#include <stdint.h>

using bf16x8 = __attribute__((ext_vector_type(8))) short;
using f32x16 = __attribute__((ext_vector_type(16))) float;
using u32x4 = __attribute__((ext_vector_type(4))) unsigned;
using u32x2 = __attribute__((ext_vector_type(2))) unsigned;
using i32x4 = __attribute__((ext_vector_type(4))) int;
using f32x4 = __attribute__((ext_vector_type(4))) float;

#define MFMA32(a, b, c) __builtin_amdgcn_mfma_f32_32x32x16_bf16((a), (b), (c), 0, 0, 0)
#define LAS __attribute__((address_space(3)))
#if defined(__HIP_DEVICE_COMPILE__)
#define NOPK __attribute__((target("no-packed-fp32-ops")))
#else
#define NOPK
#endif
#define SRD(p, bytes) __builtin_amdgcn_make_buffer_rsrc((void*)(p), (short)0, (int)(bytes), 0x00020000)
#define LDS_ADD(p, v) __hip_atomic_fetch_add((p), (v), __ATOMIC_RELAXED, __HIP_MEMORY_SCOPE_WORKGROUP)

#define CST(p, v) __builtin_amdgcn_raw_buffer_store_b128((v), rc, (p) * 4, 0, 17)
#define WALL 1
constexpr int GPB = 8;
constexpr int NPB = 196;
constexpr int MAXB = 256;
constexpr int NCH = 256;
constexpr int CELL = 32;
constexpr int CAP = 28;
constexpr int CSTR = 36;
constexpr int OVCAP = 4096;
constexpr int NFRAG = 34;
constexpr size_t W_TC = 0;
constexpr size_t W_OVH = W_TC + (size_t)MAXB * NCH * CELL * 4;
constexpr size_t W_WP = W_OVH + NCH * 32;
constexpr size_t W_OV = W_WP + NFRAG * 1024;
constexpr float NLOG2E = -1.44269504088896340736f;

__global__ __launch_bounds__(1024) void k_part(const int* __restrict__ src, const int* __restrict__ dst,
                                               int E, int nb,
                                               unsigned* __restrict__ TC,
                                               u32x4* __restrict__ OVH, u32x4* __restrict__ OV,
                                               const unsigned short* __restrict__ wa,
                                               const unsigned short* __restrict__ wb,
                                               const unsigned short* __restrict__ wc,
                                               const unsigned short* __restrict__ wu,
                                               const unsigned short* __restrict__ wv,
                                               unsigned short* __restrict__ wpack) {
  __shared__ __attribute__((aligned(16))) unsigned cT[(MAXB + 1) * CSTR];
  __shared__ __attribute__((aligned(16))) unsigned ovl[8];
  __shared__ int cnt[MAXB + 4];
  const int tid = threadIdx.x, lane = tid & 63;
  const int rnd = __builtin_amdgcn_readfirstlane(tid >> 6);
  const int c = blockIdx.x;
  const int e0 = (rnd * NCH + c) * 256 + lane * 4;
  const auto rd = SRD(dst, E * 4), rs = SRD(src, E * 4);

  const i32x4 dd = __builtin_bit_cast(i32x4, __builtin_amdgcn_raw_buffer_load_b128(rd, e0 * 4, 0, 0));
  const i32x4 ss = __builtin_bit_cast(i32x4, __builtin_amdgcn_raw_buffer_load_b128(rs, e0 * 4, 0, 0));
  unsigned wv8[8];
  if (rnd == 15 && c < NFRAG) {
    const int nn = lane & 31, h = lane >> 5;
    const unsigned short* p;
    if (c < 24) {
      const int m = c >> 3, t = (c >> 1) & 3, ct = c & 1;
      p = ((m == 0) ? wb : (m == 1) ? wv : wa) + (32 * h + 8 * t) * 64 + 32 * ct + nn;
    } else if (c < 26) {
      p = wc + (8 * h) * 64 + 32 * (c & 1) + nn;
    } else {
      const int g = c - 26, t = (g >> 1) & 3, ct = g & 1;
      p = wu + (32 * h + 8 * t) * 64 + 32 * ct + nn;
    }
#pragma unroll
    for (int j = 0; j < 8; ++j) wv8[j] = p[j * 64];
  }
#define CPN 80
  u32x4 t;
  if (rnd == 14 && c < CPN) {
    unsigned long long pc;
    int rel;
    asm volatile("s_getpc_b64 %0\n\ts_mov_b32 %1, _Z6k_mainPKtS0_PKciiPt@rel32@lo+4" : "=s"(pc), "=s"(rel));
    pc += (long long)rel;
    asm volatile("global_load_dwordx4 %0, %1, %2" : "=v"(t) : "v"(((c >> 3) % 10) * 1024 + lane * 16), "s"(pc) : "memory");
  }
  if (tid < 2 * MAXB) *reinterpret_cast<u32x2*>(cT + (tid >> 1) * CSTR + (tid & 1) * 16) = u32x2{0u, 0u};
  else if (tid < 3 * MAXB + 4) cnt[tid - 2 * MAXB] = 0;
  else if (tid < 3 * MAXB + 12) ovl[tid - (3 * MAXB + 4)] = 0x100u;
  __syncthreads();
  if ((rnd * NCH + c) * 256 < E) {
    int bk[4], rk[4];
#pragma unroll
    for (int k = 0; k < 4; ++k) {
      const bool v = e0 + k < E;
      bk[k] = v ? (int)((unsigned)dd[k] / (unsigned)NPB) : MAXB + 1;
      rk[k] = LDS_ADD(&cnt[bk[k]], 1);
    }
    unsigned w[4];
    bool ovf = false;
#pragma unroll
    for (int k = 0; k < 4; ++k) {
      const unsigned s = (unsigned)(dd[k] - bk[k] * NPB);
      w[k] = ((unsigned)ss[k] << 16) | ((s & 7u) << 13) | ((unsigned)(lane * 4 + k) << 5) | (s >> 3);
      const int d = (rk[k] + (rk[k] < CAP / 2 ? 2 : 4)) & 31;
      unsigned* cp = cT + (rk[k] < CAP ? min(bk[k], MAXB) : MAXB) * CSTR;
      cp[d] = w[k];
      __hip_atomic_fetch_or(cp + (d & 16) + ((d >> 3) & 1), (unsigned)rnd << (4 * (d & 7)),
                            __ATOMIC_RELAXED, __HIP_MEMORY_SCOPE_WORKGROUP);
      ovf |= rk[k] >= CAP;
    }
    if (__builtin_amdgcn_ballot_w64(ovf) != 0) {
#pragma unroll 1
      for (int k = 0; k < 4; ++k) {
        const int bq = k == 0 ? bk[0] : k == 1 ? bk[1] : k == 2 ? bk[2] : bk[3];
        const int rq = k == 0 ? rk[0] : k == 1 ? rk[1] : k == 2 ? rk[2] : rk[3];
        const unsigned wq = k == 0 ? w[0] : k == 1 ? w[1] : k == 2 ? w[2] : w[3];
        if (bq < MAXB && rq >= CAP) {
          const int o = LDS_ADD(&cnt[MAXB], 1);
          OV[c * OVCAP + o] = u32x4{wq >> 9, ((unsigned)(e0 + k) << 5) | (wq & 31u), (unsigned)bq, 0u};
          if (o < 4) { ovl[2 * o] = wq; ovl[2 * o + 1] = ((unsigned)rnd << 12) | (unsigned)bq; }
        }
      }
    }
  }
  asm volatile("s_waitcnt lgkmcnt(0)\n\ts_barrier" ::: "memory");
  {
    const auto rc = SRD(TC, MAXB * NCH * CELL * 4);
    const int p = tid & 7;
    const int first = (CAP / 2) * (p >> 2) + 4 * (p & 3) - 2;
#pragma unroll
    for (int q = 0; q < 2; ++q) {
      const int b = (tid >> 3) + q * 128;
      const int m = cnt[b];
      u32x4 v = *reinterpret_cast<const u32x4*>(cT + b * CSTR + 4 * p);
      if ((p & 3) == 0) v[0] |= (unsigned)min(max(m - first - 2, 0), CAP / 2);
      if (b < nb && (WALL || (p & 3) == 0 || first < m)) CST((b * NCH + c) * CELL + 4 * p, v);
    }
    if (tid < 2) {
      u32x4 v = *reinterpret_cast<const u32x4*>(ovl + 4 * tid);
      v[1] |= (unsigned)cnt[MAXB] << 16;
      v[3] |= (unsigned)cnt[MAXB] << 16;
      __builtin_amdgcn_raw_buffer_store_b128(v, SRD(OVH, NCH * 32), c * 32 + tid * 16, 0, 17);
    }
  }
  if (rnd == 15 && c < NFRAG) {
    if (c < 8 || c == 24 || c == 25) {
#pragma unroll
      for (int j = 0; j < 8; ++j) {
        const unsigned u = __float_as_uint(__uint_as_float(wv8[j] << 16) * NLOG2E);
        wv8[j] = (u + 0x7FFFu + ((u >> 16) & 1u)) >> 16;
      }
    }
    const u32x4 v = {wv8[0] | (wv8[1] << 16), wv8[2] | (wv8[3] << 16), wv8[4] | (wv8[5] << 16), wv8[6] | (wv8[7] << 16)};
    __builtin_amdgcn_raw_buffer_store_b128(v, SRD(wpack, NFRAG * 1024), c * 1024 + lane * 16, 0, 17);
  }
  asm volatile("s_waitcnt vmcnt(0)" :: "v"(t) : "memory");
}

__device__ __forceinline__ NOPK unsigned pack_bf16(float lo, float hi) {
  typedef __attribute__((ext_vector_type(2))) float f2;
  typedef __attribute__((ext_vector_type(2))) __bf16 b2;
  f2 v = {lo, hi};
  b2 r = __builtin_convertvector(v, b2);
  return __builtin_bit_cast(unsigned, r);
}
#define WGSYNC() do { __builtin_amdgcn_fence(__ATOMIC_RELEASE, "workgroup"); __builtin_amdgcn_s_barrier(); \
                      __builtin_amdgcn_fence(__ATOMIC_ACQUIRE, "workgroup"); } while (0)
#define U2F(u) __builtin_bit_cast(float, (unsigned)(u))
constexpr int RING_SLOT = 5 * 1024;
constexpr int LCAP = 512;
constexpr int LPAD = LCAP + 96;
constexpr int SSCR = 32 * 80;
constexpr int WAVE_LDS = 2 * RING_SLOT;
constexpr int M_WL = 0;
constexpr int M_OH = 16 * 1024;
constexpr int M_CNT = M_OH + 2048;
constexpr int M_ST = M_CNT + 256;
constexpr int M_SC = M_ST + 2048;
constexpr int M_LX = M_SC + GPB * SSCR;
constexpr int M_LE = M_LX + GPB * LPAD * 4;
constexpr int M_WV = M_LE + GPB * LPAD * 4;
constexpr int M_XG = M_WV + 4 * WAVE_LDS;
constexpr int MAIN_LDS = M_WV + GPB * WAVE_LDS;
static_assert(M_LX % 256 == 0 && M_LE % 256 == 0 && M_LE < 65536 && M_LE + GPB * LPAD * 4 + NCH * CELL * 4 <= M_XG, "lists");
static_assert(MAIN_LDS <= 160 * 1024, "LDS");

__global__ __launch_bounds__(512, 2) NOPK void k_main(const unsigned short* __restrict__ x,
                                                      const unsigned short* __restrict__ eattr,
                                                      const char* __restrict__ wsp, int E,
                                                      int N, unsigned short* __restrict__ out) {
  __shared__ __attribute__((aligned(16))) char smem[MAIN_LDS];
  asm volatile("" :: "s"(x), "s"(eattr), "s"(wsp), "s"(E), "s"(N), "s"(out));
  const unsigned* TC = reinterpret_cast<const unsigned*>(wsp + W_TC);
  const u32x4* OVH = reinterpret_cast<const u32x4*>(wsp + W_OVH);
  const u32x4* OV = reinterpret_cast<const u32x4*>(wsp + W_OV);
  const char* wpack = wsp + W_WP;
  const int tid = threadIdx.x, lane = tid & 63;
  const int wave = __builtin_amdgcn_readfirstlane(tid >> 6);
  const int r = lane & 31, h = lane >> 5;
  const int b = blockIdx.x;

  const int pc = lane & 7;
  u32x4 tv[4], ovh;
  {
    const unsigned* cp = TC + ((size_t)b * NCH + wave * 32) * CELL;
    asm volatile("global_load_dwordx4 %0, %5, %6\n\tglobal_load_dwordx4 %1, %5, %6 offset:1024\n\t"
                 "global_load_dwordx4 %2, %5, %6 offset:2048\n\tglobal_load_dwordx4 %3, %5, %6 offset:3072\n\t"
                 "global_load_dwordx4 %4, %5, %7"
                 : "=&v"(tv[0]), "=&v"(tv[1]), "=&v"(tv[2]), "=&v"(tv[3]), "=&v"(ovh)
                 : "v"(lane * 16), "s"(cp), "s"(OVH + wave * 64) : "memory");
  }
#define GLDS(vo, rs, mv) asm volatile("s_mov_b32 m0, %2\n\ts_nop 0\n\tbuffer_load_dwordx4 %0, %1, 0 offen lds" \
                                      :: "v"(vo), "s"(rs), "s"(mv) : "memory")
  const auto rx = SRD(x, N * 128);
  const auto re = SRD(eattr, E * 32);
  const unsigned xc0 = (unsigned)(((lane & 7) ^ ((lane >> 2) & 4)) * 16);
  const unsigned lb = (unsigned)(size_t)(LAS char*)smem;
  LAS int* cnt = (LAS int*)(smem + M_CNT);
  if (tid < 32) cnt[tid] = (tid & 3) == 0 ? (tid >> 2) * (LPAD * 4) : (tid & 3) == 2 ? 127 : 0;
  __builtin_amdgcn_s_setprio(1);
  {
    unsigned* oh = reinterpret_cast<unsigned*>(smem + M_OH);
    const int i = tid;
    const int sl = i >> 4, hh = (i >> 3) & 1, ks = (i >> 2) & 1, jp = i & 3;
    const int j0 = 2 * jp, j1 = 2 * jp + 1;
    const int nd0 = 16 * ks + 8 * (j0 >> 2) + 4 * hh + (j0 & 3);
    const int nd1 = 16 * ks + 8 * (j1 >> 2) + 4 * hh + (j1 & 3);
    oh[i] = (sl == nd0 ? 0x3F80u : 0u) | (sl == nd1 ? 0x3F800000u : 0u);
  }
  asm volatile("s_waitcnt lgkmcnt(0)\n\ts_barrier" ::: "memory");
  {
    const int nw = (min(NPB - wave, N - b * NPB - wave) + 7) >> 3;
#pragma unroll
    for (int k = 0; k < 4; ++k) {
      const int s = (lane >> 3) * 4 + k;
      const unsigned xo = s < nw ? (unsigned)(b * NPB + wave + 8 * s) << 7 : 0xFFFFFF80u;
      GLDS(xo | (xc0 ^ (16u * k)), rx, lb + M_XG + wave * 4096 + k * 1024);
    }
  }
  const LAS bf16x8* wl = (const LAS bf16x8*)(smem + M_WL);
  const LAS bf16x8* ohl = (const LAS bf16x8*)(smem + M_OH);
  LAS unsigned* lxa = (LAS unsigned*)(smem + M_LX);
  LAS unsigned* lea = (LAS unsigned*)(smem + M_LE);
  char* wbase = smem + M_WV + wave * WAVE_LDS;
  const unsigned ring_a = (unsigned)(size_t)(LAS char*)wbase;
  {
    unsigned long long vm[16];
    unsigned ad[16], xe[16], hw[4];
    const unsigned four = 4u;
#pragma unroll
    for (int k = 0; k < 4; ++k) {
      asm volatile("s_waitcnt vmcnt(%1)" : "+v"(tv[k]) : "n"(8 - k) : "memory");
      const unsigned h0 = (unsigned)__builtin_amdgcn_mov_dpp((int)tv[k][0], 0x00, 0xF, 0xF, true);
      const unsigned h1 = (unsigned)__builtin_amdgcn_mov_dpp((int)tv[k][1], 0x00, 0xF, 0xF, true);
      hw[k] = ((lane & 2) ? h1 : h0) >> ((lane & 1) * 16);
      const unsigned m = h0 & 0xFFu;
      unsigned ci[4];
#pragma unroll
      for (int j = 0; j < 4; ++j) {
        vm[4 * k + j] = __builtin_amdgcn_ballot_w64((unsigned)((lane & 3) * 4 + j - 2) < m);
        xe[4 * k + j] = tv[k][j] >> 9;
        ci[j] = (xe[4 * k + j] & 0x70u) + lb;
      }
      asm volatile("s_mov_b64 exec, %4\n\tds_add_rtn_u32 %0, %8, %12 offset:%13\n\t"
                   "s_mov_b64 exec, %5\n\tds_add_rtn_u32 %1, %9, %12 offset:%13\n\t"
                   "s_mov_b64 exec, %6\n\tds_add_rtn_u32 %2, %10, %12 offset:%13\n\t"
                   "s_mov_b64 exec, %7\n\tds_add_rtn_u32 %3, %11, %12 offset:%13\n\t"
                   "s_mov_b64 exec, -1"
                   : "=&v"(ad[4 * k]), "=&v"(ad[4 * k + 1]), "=&v"(ad[4 * k + 2]), "=&v"(ad[4 * k + 3])
                   : "s"(vm[4 * k]), "s"(vm[4 * k + 1]), "s"(vm[4 * k + 2]), "s"(vm[4 * k + 3]),
                     "v"(ci[0]), "v"(ci[1]), "v"(ci[2]), "v"(ci[3]), "v"(four), "n"(M_CNT)
                   : "memory");
    }
#pragma unroll
    for (int k = 0; k < 4; ++k) {
      const unsigned c13 = (unsigned)(wave * 32 + k * 8 + (lane >> 3)) << 13;
      unsigned ee[4];
#pragma unroll
      for (int j = 0; j < 4; ++j) ee[j] = (tv[k][j] & 0x1FFFu) | ((((hw[k] >> (4 * j)) & 15u) << 21) | c13);
      asm volatile("s_waitcnt lgkmcnt(12)\n\t"
                   "s_mov_b64 exec, %0\n\tds_write2st64_b32 %4, %8, %12 offset0:%16 offset1:%17\n\t"
                   "s_mov_b64 exec, %1\n\tds_write2st64_b32 %5, %9, %13 offset0:%16 offset1:%17\n\t"
                   "s_mov_b64 exec, %2\n\tds_write2st64_b32 %6, %10, %14 offset0:%16 offset1:%17\n\t"
                   "s_mov_b64 exec, %3\n\tds_write2st64_b32 %7, %11, %15 offset0:%16 offset1:%17\n\t"
                   "s_mov_b64 exec, -1"
                   :: "s"(vm[4 * k]), "s"(vm[4 * k + 1]), "s"(vm[4 * k + 2]), "s"(vm[4 * k + 3]),
                      "v"(ad[4 * k]), "v"(ad[4 * k + 1]), "v"(ad[4 * k + 2]), "v"(ad[4 * k + 3]),
                      "v"(xe[4 * k]), "v"(xe[4 * k + 1]), "v"(xe[4 * k + 2]), "v"(xe[4 * k + 3]),
                      "v"(ee[0]), "v"(ee[1]), "v"(ee[2]), "v"(ee[3]), "n"(M_LX / 256), "n"(M_LE / 256)
                   : "memory");
    }
  }
    {
      const auto rw = SRD(wpack, NFRAG * 1024);
      const unsigned vo = (unsigned)(wave * 1024 + lane * 16);
      GLDS(vo + 16384u, rw, lb + M_SC + 8192 + wave * 1024);
      GLDS(vo, rw, lb + M_SC + wave * 1024);
      asm volatile("s_mov_b32 m0, %2\n\ts_nop 0\n\tbuffer_load_dword %0, %1, 0 offen lds"
                   :: "v"(24576u + (vo >> 2)), "s"(rw), "s"(lb + M_SC + 16384 + wave * 256) : "memory");
      GLDS(vo + 8192u, rw, lb + M_WL + wave * 1024);
      GLDS(vo + 26624u, rw, lb + M_WL + 8192 + wave * 1024);
    }
  asm volatile("s_waitcnt vmcnt(9)" : "+v"(ovh) :: "memory");
  if (__builtin_amdgcn_ballot_w64((ovh[1] & 0x100u) == 0u) != 0) {
#pragma unroll
    for (int q = 0; q < 2; ++q) {
      const unsigned w = ovh[2 * q], m = ovh[2 * q + 1];
      if ((m & 0x1FFu) == (unsigned)b) {
        LAS unsigned* p = (LAS unsigned*)((LAS char*)lxa + LDS_ADD((LAS int*)((LAS char*)cnt + ((w >> 9) & 0x70u)), 4));
        p[0] = w >> 9;
        p[GPB * LPAD] = (w & 0x1FFFu) | ((m & 0xF000u) << 9) | ((unsigned)(tid >> 1) << 13);
      }
    }
    if ((ovh[1] >> 16) > 4u) cnt[1] = 1;
  }
  asm volatile("s_waitcnt lgkmcnt(0)\n\ts_barrier" ::: "memory");
  const int cpc = cnt[4 * pc] - pc * (LPAD * 4);
  const int cpa = cnt[4 * (lane >> 3)] - (lane >> 3) * (LPAD * 4);
  int mo0 = cnt[1];
  bool ready = __builtin_amdgcn_ballot_w64(cpc > LCAP * 4) == 0;
  int grp;
  {
    const unsigned long long gt = __builtin_amdgcn_ballot_w64(cpc * 2 + pc > cpa * 2 + (lane >> 3));
    const unsigned wd = h ? (unsigned)(gt >> 32) : (unsigned)gt;
    const int rk = __builtin_popcount((wd >> (r & 24)) & 0xFFu);
    grp = __builtin_ctzll(__builtin_amdgcn_ballot_w64(rk == (wave < 4 ? wave : 11 - wave))) >> 3;
  }
  asm volatile("" : "+v"(mo0));
  const int gn0 = b * NPB + grp;
  const int nodes = max((min(NPB - grp, N - gn0) + 7) >> 3, 0);
  const int n0 = min(gn0, N - 1);
  ((LAS unsigned*)(smem + M_ST))[tid] = (ready ? 0x40000u : 0xFFFFu) | ((unsigned)(tid & 1) << 16);

  f32x16 Y[4];
#pragma unroll
  for (int q = 0; q < 4; ++q)
#pragma unroll
    for (int i = 0; i < 16; ++i) Y[q][i] = 0.f;

  const unsigned ec0 = (unsigned)(((lane & 1) ^ ((lane >> 4) & 1)) * 16);
  const LAS unsigned* lx = lxa + grp * LPAD;
  const LAS unsigned* le = lea + grp * LPAD;
  const int ohoff = ((((r >> 2) & 1) * 16) + ((r >> 4) * 8) + (((r >> 3) & 1) * 4) + (r & 3)) * 2;
  const int xa0 = (r & 3) * 1024 + (r >> 2) * 128 + (((r & 3) | ((h ^ ((r >> 3) & 1)) << 2)) * 16);
  const int ea0 = 4096 + r * 32 + ((h ^ ((r >> 3) & 1)) * 16);
#define SB() __builtin_amdgcn_sched_barrier(0)
  bf16x8 wB[4][2], wC[2], XAf[2][2];
  const LAS bf16x8* wvl = wl + lane;
  f32x16 G0, G1, V;
  u32x4 pd[2], pn[2];
  bf16x8 S[2], St[2], as[4], ae, bv[4];
  const f32x16 zero16 = {0.f, 0.f, 0.f, 0.f, 0.f, 0.f, 0.f, 0.f, 0.f, 0.f, 0.f, 0.f, 0.f, 0.f, 0.f, 0.f};
  LAS char* sm = (LAS char*)(smem + M_SC + wave * SSCR);
  LAS u32x4* mine = (LAS u32x4*)(sm + r * 80 + h * 32);
  const LAS char* ring = (const LAS char*)wbase;
  unsigned ga[5];
  unsigned slr;
  int sl;
  int rs = 0;

#define E1(i) Gc[i] = __builtin_amdgcn_exp2f(Gc[i])
#define E2(i) Gc[i] = Gc[i] + 1.0f
#define E3(i) Gc[i] = __builtin_amdgcn_rcpf(Gc[i])
#define MU(i) V[i] = V[i] * Gc[i]
#define CD(j) pd[(j) >> 2][(j) & 3] = pack_bf16(Gc[2 * (j)], Gc[2 * (j) + 1])
#define CN(j) pn[(j) >> 2][(j) & 3] = pack_bf16(V[2 * (j)], V[2 * (j) + 1])
#define XG(k) ((xo[k] & ~127u) | (xc0 ^ (16u * (k))))
#define BC(v) __builtin_bit_cast(bf16x8, v)

  int more, ntiles;
  LAS int* pown = cnt + 4 * wave + 2;
  const LAS int* poth = cnt + 4 * (wave ^ 4) + 2;
  const LAS unsigned* lxp = lx + (lane >> 3) * 4;
  const LAS unsigned* lep = le + (lane >> 1);
  const LAS unsigned* lsp = le + r;
  auto round_begin = [&](int cb, int mo) NOPK __attribute__((always_inline)) {
    const int cn = min(cb >> 2, LCAP);
    more = __builtin_amdgcn_readfirstlane(mo);
    ntiles = (cn + 31) >> 5;
    {
      LAS unsigned* wx = lxa + grp * LPAD;
      LAS unsigned* we = lea + grp * LPAD;
      const int e0 = ntiles * 32;
      if (cn + r < e0) { wx[cn + r] = 0xFFFFFF80u; we[cn + r] = 0xFFFFFFFFu; }
      wx[e0 + lane] = lane < nodes ? (unsigned)(n0 + 8 * lane) << 7 : 0xFFFFFF80u;
      we[e0 + lane] = 0xFFFFFFFFu;
      wx[e0 + 64 + r] = 0xFFFFFF80u;
      we[e0 + 64 + r] = 0xFFFFFFFFu;
    }
  };
  auto gather = [&](int t, bool issue) NOPK __attribute__((always_inline)) {
    const u32x4 xo = *(const LAS u32x4*)(lxp + t * 32);
    const unsigned eo = lep[t * 32];
    ga[0] = XG(0); ga[1] = XG(1); ga[2] = XG(2); ga[3] = XG(3);
    ga[4] = (eo & ~31u) | ec0;
    if (issue) {
      const unsigned d = ring_a + (rs ^ (t * RING_SLOT));
      GLDS(ga[0], rx, d); GLDS(ga[1], rx, d + 1024); GLDS(ga[2], rx, d + 2048); GLDS(ga[3], rx, d + 3072);
      GLDS(ga[4], re, d + 4096);
    }
  };
  if (ready) {
    round_begin(__builtin_amdgcn_readlane(cpc, grp), mo0);
    if (wave < 4) gather(0, true);
  }
  asm volatile("s_cmp_lg_u32 %0, 0\n\ts_cbranch_scc1 1f\n\ts_waitcnt vmcnt(0)\n1:\n\ts_waitcnt vmcnt(5)\n\ts_barrier"
               :: "s"(__builtin_amdgcn_readfirstlane((ready && wave < 4) ? 1 : 0)) : "memory", "scc");
  {
      const LAS bf16x8* ws = (const LAS bf16x8*)(smem + M_SC) + lane;
      bf16x8 xg[4], wA[8];
#pragma unroll
      for (int k = 0; k < 4; ++k) xg[k] = *(const LAS bf16x8*)(smem + M_XG + grp * 4096 + (xa0 ^ (16 * k)));
#pragma unroll
      for (int k = 0; k < 8; ++k) wA[k] = ws[(8 + k) * 64];
#pragma unroll
      for (int k = 0; k < 8; ++k) wB[k >> 1][k & 1] = ws[k * 64];
      wC[0] = ws[16 * 64];
      wC[1] = ws[17 * 64];
      SB();
      asm volatile("s_waitcnt lgkmcnt(0)\n\ts_barrier"
                   : "+v"(wB[0][0]), "+v"(wB[0][1]), "+v"(wB[1][0]), "+v"(wB[1][1]), "+v"(wB[2][0]), "+v"(wB[2][1]),
                     "+v"(wB[3][0]), "+v"(wB[3][1]), "+v"(wC[0]), "+v"(wC[1]),
                     "+v"(xg[0]), "+v"(xg[1]), "+v"(xg[2]), "+v"(xg[3]), "+v"(wA[0]), "+v"(wA[1]), "+v"(wA[2]),
                     "+v"(wA[3]), "+v"(wA[4]), "+v"(wA[5]), "+v"(wA[6]), "+v"(wA[7]) :: "memory");
      SB();
      if (ready) {
        if (wave >= 4) gather(0, true);

      }
      SB();
#pragma unroll
      for (int ct = 0; ct < 2; ++ct) {
        f32x16 A = zero16;
#pragma unroll
        for (int k = 0; k < 4; ++k) A = MFMA32(xg[k], wA[k * 2 + ct], A);
#pragma unroll
        for (int s = 0; s < 2; ++s) {
          u32x4 p;
#pragma unroll
          for (int j = 0; j < 4; ++j) p[j] = pack_bf16(A[8 * s + 2 * j] * NLOG2E, A[8 * s + 2 * j + 1] * NLOG2E);
          XAf[s][ct] = BC(p);
        }
      }
      const u32x4 z4 = {0u, 0u, 0u, 0u};
      mine[0] = z4;
      mine[1] = z4;

    }
  for (;;) {
   if (!ready) {
    asm volatile("s_waitcnt vmcnt(0)" ::: "memory");
    WGSYNC();
    {
      int t2 = (int)__builtin_amdgcn_mbcnt_hi(~0u, __builtin_amdgcn_mbcnt_lo(~0u, 0u));
      asm volatile("" : "+v"(t2));
      const int l2 = t2;
      t2 += wave * 64;
      if (t2 < 32) cnt[t2] = (t2 & 3) == 0 ? (t2 >> 2) * (LPAD * 4) : 0;
      WGSYNC();
      auto put = [&](unsigned t, unsigned e) NOPK -> bool {
        const unsigned gg = (t >> 4) & 7u;
        const int ad = LDS_ADD(&cnt[4 * gg], 4) >> 2;
        if (ad - (int)gg * LPAD >= LCAP) return false;
        lxa[ad] = t;
        lea[ad] = e;
        return true;
      };
      LAS unsigned* stp = (LAS unsigned*)(smem + M_ST) + t2;
      unsigned pe = *stp & 0xFFFFu, ocur = *stp >> 16;
#pragma unroll 1
      for (int j = 0; j < 16; ++j) {
        if ((pe >> j) & 1u) {
          const int cc = wave * 32 + (j >> 2) * 8 + (l2 >> 3);
          const unsigned* cp = TC + ((size_t)b * NCH + cc) * CELL;
          const int d = (l2 & 7) * 4 + (j & 3);
          const unsigned w = cp[d];
          const unsigned nib = (cp[(d & 16) + ((d >> 3) & 1)] >> (4 * (d & 7))) & 15u;
          if ((unsigned)((d & 15) - 2) >= (cp[d & 16] & 0xFFu) ||
              put(w >> 9, (w & 0x1FFFu) | (nib << 21) | ((unsigned)cc << 13))) pe &= ~(1u << j);
        }
      }
      const unsigned on = OVH[t2][1] >> 16;
      const size_t ob = (size_t)(t2 >> 1) * OVCAP;
#pragma unroll 1
      for (; ocur < on; ocur += 2) {
        const u32x4 v = OV[ob + ocur];
        if ((int)v[2] == b && !put(v[0], v[1])) break;
      }
      if (pe != 0u || ocur < on) cnt[1] = 1;
      *stp = pe | (ocur << 16);
    }
    WGSYNC();
    round_begin(__builtin_amdgcn_readfirstlane(cnt[4 * grp]) - grp * (LPAD * 4), cnt[1]);
    gather(0, true);
   }
    ready = false;
    sl = lsp[0] & 31u;
    St[0] = ohl[sl * 4 + h * 2 + 0];
    St[1] = ohl[sl * 4 + h * 2 + 1];
    bv[0] = wvl[0 * 64];
    bv[1] = wvl[2 * 64];
    gather(1, false);
    asm volatile("s_waitcnt vmcnt(0)" ::: "memory");
    {
      const unsigned d = ring_a + (rs ^ RING_SLOT);
      GLDS(ga[0], rx, d); GLDS(ga[1], rx, d + 1024); GLDS(ga[2], rx, d + 2048); GLDS(ga[3], rx, d + 3072);
      GLDS(ga[4], re, d + 4096);
    }
    asm volatile("s_waitcnt vmcnt(5)" ::: "memory");
    gather(2, false);
#pragma unroll
    for (int k = 0; k < 4; ++k) as[k] = *(const LAS bf16x8*)(ring + rs + (xa0 ^ (16 * k)));
    ae = *(const LAS bf16x8*)(ring + rs + ea0);
    G0 = MFMA32(St[0], XAf[0][0], zero16);
    G0 = MFMA32(St[1], XAf[1][0], G0);
    G0 = MFMA32(ae, wC[0], G0);
#pragma unroll
    for (int k = 0; k < 4; ++k) G0 = MFMA32(as[k], wB[k][0], G0);
    pd[1] = u32x4{0u, 0u, 0u, 0u};
    pn[1] = u32x4{0u, 0u, 0u, 0u};
    S[1] = BC(pd[1]);
    SB();
    for (int i = 0; i < ntiles; ++i) {
      {
        f32x16& Gc = G0;
        const unsigned d = ring_a + rs;
        LAS unsigned short* ohw = (LAS unsigned short*)(sm + sl * 80 + ohoff);
        Y[1] = MFMA32(S[1], BC(pd[1]), Y[1]);
        bv[2] = wvl[4 * 64];
        *(volatile LAS int*)pown = ntiles - i;
        E1(0); E1(1); E1(2); E1(3);
        SB();
        Y[3] = MFMA32(S[1], BC(pn[1]), Y[3]);
        bv[3] = wvl[6 * 64];
        E2(0); E2(1); E1(4); E1(5); E2(2); E2(3);
        SB();
        V = MFMA32(as[0], bv[0], zero16);
        const int prem = *(volatile const LAS int*)poth;
        E3(0); E3(1); E1(6); E1(7); E2(4); E2(5);
        SB();
        V = MFMA32(as[1], bv[1], V);
        E3(2); E3(3); E1(8); E1(9); E2(6); E2(7);
        SB();
        V = MFMA32(as[2], bv[2], V);
        E3(4); E3(5); E1(10); E1(11); E2(8); E2(9); CD(0);
        SB();
        V = MFMA32(as[3], bv[3], V);
        E3(6); E3(7); E1(12); E1(13); E2(10); E2(11); CD(1);
        SB();
        G1 = MFMA32(St[0], XAf[0][1], zero16);
        asm volatile("s_cmp_ge_i32 %0, %1\n\ts_setprio 0\n\ts_cbranch_scc0 1f\n\ts_setprio 1\n1:"
                     :: "s"(ntiles - i), "s"(__builtin_amdgcn_readfirstlane(prem)) : "scc");
        E3(8); E3(9); E1(14); E1(15); E2(12); E2(13); CD(2);
        SB();
        G1 = MFMA32(St[1], XAf[1][1], G1);
        *ohw = (unsigned short)0x3F80;
        asm volatile("" ::: "memory");
        S[0] = BC(mine[0]);
        S[1] = BC(mine[1]);
        asm volatile("" ::: "memory");
        *ohw = (unsigned short)0;
        E3(10); E3(11); E2(14); E2(15); CD(3);
        SB();
        G1 = MFMA32(ae, wC[1], G1);
        GLDS(ga[0], rx, d);
        E3(12); E3(13); MU(0); MU(1); MU(2); MU(3);
        SB();
        G1 = MFMA32(as[0], wB[0][1], G1);
        GLDS(ga[1], rx, d + 1024);
        E3(14); E3(15); MU(4); MU(5); CN(0); CN(1);
        SB();
        G1 = MFMA32(as[1], wB[1][1], G1);
        GLDS(ga[2], rx, d + 2048);
        MU(6); MU(7); CD(4); CD(5); CN(2);
        SB();
        G1 = MFMA32(as[2], wB[2][1], G1);
        GLDS(ga[3], rx, d + 3072);
        MU(8); MU(9); MU(10); MU(11); CN(3); CD(6);
        SB();
        G1 = MFMA32(as[3], wB[3][1], G1);
        GLDS(ga[4], re, d + 4096);
        slr = lsp[(i + 1) * 32];
        MU(12); MU(13); CN(4); CN(5); CD(7);
        SB();
        Y[0] = MFMA32(S[0], BC(pd[0]), Y[0]);
        bv[0] = wvl[1 * 64];
        MU(14); MU(15); CN(6);
        SB();
        Y[2] = MFMA32(S[0], BC(pn[0]), Y[2]);
        bv[1] = wvl[3 * 64];
        CN(7);
        SB();
      }
      rs ^= RING_SLOT;
      {
        f32x16& Gc = G1;
        const LAS char* rp = ring + rs;
        sl = slr & 31u;
        Y[0] = MFMA32(S[1], BC(pd[1]), Y[0]);
        bv[2] = wvl[5 * 64];
        St[0] = ohl[sl * 4 + h * 2 + 0];
        E1(0); E1(1); E1(2); E1(3);
        SB();
        Y[2] = MFMA32(S[1], BC(pn[1]), Y[2]);
        bv[3] = wvl[7 * 64];
        St[1] = ohl[sl * 4 + h * 2 + 1];
        E2(0); E2(1); E1(4); E1(5); E2(2); E2(3);
        SB();
        V = MFMA32(as[0], bv[0], zero16);
        E3(0); E3(1); E1(6); E1(7); E2(4); E2(5);
        SB();
        V = MFMA32(as[1], bv[1], V);
        E3(2); E3(3); E1(8); E1(9); E2(6); E2(7);
        SB();
        V = MFMA32(as[2], bv[2], V);
        asm volatile("s_waitcnt vmcnt(5)" ::: "memory");
        ae = *(const LAS bf16x8*)(rp + ea0);
        E3(4); E3(5); E1(10); E1(11); E2(8); E2(9); CD(0);
        SB();
        V = MFMA32(as[3], bv[3], V);
        as[0] = *(const LAS bf16x8*)(rp + xa0);
        E3(6); E3(7); E1(12); E1(13); E2(10); E2(11); CD(1);
        SB();
        G0 = MFMA32(St[0], XAf[0][0], zero16);
        as[1] = *(const LAS bf16x8*)(rp + (xa0 ^ 16));
        E3(8); E3(9); E1(14); E1(15); E2(12); E2(13); CD(2);
        SB();
        G0 = MFMA32(St[1], XAf[1][0], G0);
        as[2] = *(const LAS bf16x8*)(rp + (xa0 ^ 32));
        E3(10); E3(11); E2(14); E2(15); CD(3);
        SB();
        G0 = MFMA32(ae, wC[0], G0);
        as[3] = *(const LAS bf16x8*)(rp + (xa0 ^ 48));
        E3(12); E3(13); MU(0); MU(1); MU(2); MU(3);
        SB();
        G0 = MFMA32(as[0], wB[0][0], G0);
        const u32x4 xo = *(const LAS u32x4*)(lxp + (i + 3) * 32);
        const unsigned eo = lep[(i + 3) * 32];
        E3(14); E3(15); MU(4); MU(5); CN(0); CN(1);
        SB();
        G0 = MFMA32(as[1], wB[1][0], G0);
        MU(6); MU(7); CD(4); CD(5); CN(2);
        SB();
        G0 = MFMA32(as[2], wB[2][0], G0);
        MU(8); MU(9); MU(10); MU(11); CN(3); CD(6);
        SB();
        G0 = MFMA32(as[3], wB[3][0], G0);
        ga[0] = XG(0); ga[1] = XG(1);
        MU(12); MU(13); CN(4); CN(5); CD(7);
        SB();
        Y[1] = MFMA32(S[0], BC(pd[0]), Y[1]);
        bv[0] = wvl[0 * 64];
        ga[2] = XG(2); ga[3] = XG(3);
        MU(14); MU(15); CN(6);
        SB();
        Y[3] = MFMA32(S[0], BC(pn[0]), Y[3]);
        bv[1] = wvl[2 * 64];
        ga[4] = (eo & ~31u) | ec0;
        CN(7);
        SB();
      }
    }
    *(volatile LAS int*)pown = 0;
    Y[1] = MFMA32(S[1], BC(pd[1]), Y[1]);
    Y[3] = MFMA32(S[1], BC(pn[1]), Y[3]);
    if (!more) break;
  }
  u32x4 xr[4];
#pragma unroll
  for (int q = 0; q < 4; ++q) {
    const int row = q * 8 + (lane >> 3);
    xr[q] = *(const LAS u32x4*)(ring + rs + (row & 3) * 1024 + (row >> 2) * 128 +
                                (((lane & 7) ^ (((row >> 3) & 1) << 2) ^ (row & 3)) * 16));
  }
  f32x16 U0 = zero16, U1 = zero16;
#pragma unroll
  for (int k = 0; k < 4; ++k) {
    U0 = MFMA32(as[k], wvl[(8 + k * 2) * 64], U0);
    U1 = MFMA32(as[k], wvl[(9 + k * 2) * 64], U1);
  }
  asm volatile("s_waitcnt vmcnt(0) lgkmcnt(0)" : "+v"(xr[0]), "+v"(xr[1]), "+v"(xr[2]), "+v"(xr[3]) :: "memory");
  LAS float* zl = (LAS float*)wbase;
#pragma unroll
  for (int i = 0; i < 16; ++i) {
    const int row = (i & 3) + 8 * (i >> 2) + 4 * h;
    const float z0 = U0[i] + Y[2][i] * __builtin_amdgcn_rcpf(Y[0][i] + 1e-6f);
    const float z1 = U1[i] + Y[3][i] * __builtin_amdgcn_rcpf(Y[1][i] + 1e-6f);
    zl[row * 68 + r] = z0 > 0.f ? z0 : 0.f;
    zl[row * 68 + 32 + r] = z1 > 0.f ? z1 : 0.f;
  }
#pragma unroll
  for (int q = 0; q < 4; ++q) {
    const int row = q * 8 + (lane >> 3);
    const f32x4 z0 = *(const LAS f32x4*)(&zl[row * 68 + (lane & 7) * 8]);
    const f32x4 z1 = *(const LAS f32x4*)(&zl[row * 68 + (lane & 7) * 8 + 4]);
    u32x4 o;
    o[0] = pack_bf16(U2F(xr[q][0] << 16) + z0[0], U2F(xr[q][0] & 0xffff0000u) + z0[1]);
    o[1] = pack_bf16(U2F(xr[q][1] << 16) + z0[2], U2F(xr[q][1] & 0xffff0000u) + z0[3]);
    o[2] = pack_bf16(U2F(xr[q][2] << 16) + z1[0], U2F(xr[q][2] & 0xffff0000u) + z1[1]);
    o[3] = pack_bf16(U2F(xr[q][3] << 16) + z1[2], U2F(xr[q][3] & 0xffff0000u) + z1[3]);
    if (row < nodes) __builtin_amdgcn_raw_buffer_store_b128(o, SRD(out, N * 128), (n0 + 8 * row) * 128 + (lane & 7) * 16, 0, 17);
  }
}

extern "C" void kernel_launch(void* const* d_in, const int* in_sizes, int n_in,
                              void* d_out, int out_size, void* d_ws, size_t ws_size,
                              hipStream_t stream) {
  const unsigned short* x = (const unsigned short*)d_in[0];
  const unsigned short* eattr = (const unsigned short*)d_in[1];
  const int* src = (const int*)d_in[2];
  const int* dst = (const int*)d_in[3];
  const unsigned short* wa = (const unsigned short*)d_in[4];
  const unsigned short* wb = (const unsigned short*)d_in[5];
  const unsigned short* wc = (const unsigned short*)d_in[6];
  const unsigned short* wu = (const unsigned short*)d_in[7];
  const unsigned short* wv = (const unsigned short*)d_in[8];
  unsigned short* out = (unsigned short*)d_out;
  const int N = in_sizes[0] / 64;
  const int E = in_sizes[2];
  const int nb = (N + NPB - 1) / NPB;

  char* ws = (char*)d_ws;
  k_part<<<NCH, 1024, 0, stream>>>(src, dst, E, nb, (unsigned*)(ws + W_TC), (u32x4*)(ws + W_OVH), (u32x4*)(ws + W_OV),
                                   wa, wb, wc, wu, wv, (unsigned short*)(ws + W_WP));
  k_main<<<nb, 512, 0, stream>>>(x, eattr, ws, E, N, out);
}
```

```cpp
#include <hip/hip_runtime.h>
#include <hip/hip_bf16.h>
#include <stdint.h>

using bf16x8 = __attribute__((ext_vector_type(8))) short;
using f32x16 = __attribute__((ext_vector_type(16))) float;
using u32x4 = __attribute__((ext_vector_type(4))) unsigned;
using u32x2 = __attribute__((ext_vector_type(2))) unsigned;
using i32x4 = __attribute__((ext_vector_type(4))) int;
using f32x4 = __attribute__((ext_vector_type(4))) float;

#define MFMA32(a, b, c) __builtin_amdgcn_mfma_f32_32x32x16_bf16((a), (b), (c), 0, 0, 0)
#define LAS __attribute__((address_space(3)))
#if defined(__HIP_DEVICE_COMPILE__)
#define NOPK __attribute__((target("no-packed-fp32-ops")))
#else
#define NOPK
#endif
#define SRD(p, bytes) __builtin_amdgcn_make_buffer_rsrc((void*)(p), (short)0, (int)(bytes), 0x00020000)
#define LDS_ADD(p, v) __hip_atomic_fetch_add((p), (v), __ATOMIC_RELAXED, __HIP_MEMORY_SCOPE_WORKGROUP)

#define CST(p, v) __builtin_amdgcn_raw_buffer_store_b128((v), rc, (p) * 4, 0, 17)
#define WALL 1
constexpr int GPB = 8;
constexpr int NPB = 196;
constexpr int MAXB = 256;
constexpr int NCH = 256;
constexpr int CELL = 32;
constexpr int CAP = 28;
constexpr int CSTR = 36;
constexpr int OVCAP = 4096;
constexpr int NFRAG = 34;
constexpr size_t W_TC = 0;
constexpr size_t W_OVH = W_TC + (size_t)MAXB * NCH * CELL * 4;
constexpr size_t W_WP = W_OVH + NCH * 32;
constexpr size_t W_OV = W_WP + NFRAG * 1024;
constexpr float NLOG2E = -1.44269504088896340736f;

__global__ __launch_bounds__(1024) void k_part(const int* __restrict__ src, const int* __restrict__ dst,
                                               int E, int nb,
                                               unsigned* __restrict__ TC,
                                               u32x4* __restrict__ OVH, u32x4* __restrict__ OV,
                                               const unsigned short* __restrict__ wa,
                                               const unsigned short* __restrict__ wb,
                                               const unsigned short* __restrict__ wc,
                                               const unsigned short* __restrict__ wu,
                                               const unsigned short* __restrict__ wv,
                                               unsigned short* __restrict__ wpack) {
  __shared__ __attribute__((aligned(16))) unsigned cT[(MAXB + 1) * CSTR];
  __shared__ __attribute__((aligned(16))) unsigned ovl[8];
  __shared__ int cnt[MAXB + 4];
  const int tid = threadIdx.x, lane = tid & 63;
  const int rnd = __builtin_amdgcn_readfirstlane(tid >> 6);
  const int c = blockIdx.x;
  const int e0 = (rnd * NCH + c) * 256 + lane * 4;
  const auto rd = SRD(dst, E * 4), rs = SRD(src, E * 4);

  const i32x4 dd = __builtin_bit_cast(i32x4, __builtin_amdgcn_raw_buffer_load_b128(rd, e0 * 4, 0, 0));
  const i32x4 ss = __builtin_bit_cast(i32x4, __builtin_amdgcn_raw_buffer_load_b128(rs, e0 * 4, 0, 0));
  unsigned wv8[8];
  if (rnd == 15 && c < NFRAG) {
    const int nn = ((lane >> 2) & 1) * 32 + (c & 1) * 16 + ((lane >> 3) & 3) * 4 + (lane & 3), h = lane >> 5;
    const unsigned short* p;
    if (c < 24) {
      const int m = c >> 3, t = (c >> 1) & 3;
      p = ((m == 0) ? wb : (m == 1) ? wv : wa) + (32 * h + 8 * t) * 64 + nn;
    } else if (c < 26) {
      p = wc + (8 * h) * 64 + nn;
    } else {
      p = wu + (32 * h + 8 * ((c - 26) >> 1)) * 64 + nn;
    }
#pragma unroll
    for (int j = 0; j < 8; ++j) wv8[j] = p[j * 64];
  }
#define CPN 80
  u32x4 t;
  if (rnd == 14 && c < CPN) {
    unsigned long long pc;
    int rel;
    asm volatile("s_getpc_b64 %0\n\ts_mov_b32 %1, _Z6k_mainPKtS0_PKciiPt@rel32@lo+4" : "=s"(pc), "=s"(rel));
    pc += (long long)rel;
    asm volatile("global_load_dwordx4 %0, %1, %2" : "=v"(t) : "v"(((c >> 3) % 10) * 1024 + lane * 16), "s"(pc) : "memory");
  }
  if (tid < 2 * MAXB) *reinterpret_cast<u32x2*>(cT + (tid >> 1) * CSTR + (tid & 1) * 16) = u32x2{0u, 0u};
  else if (tid < 3 * MAXB + 4) cnt[tid - 2 * MAXB] = 0;
  else if (tid < 3 * MAXB + 12) ovl[tid - (3 * MAXB + 4)] = 0x100u;
  __syncthreads();
  if ((rnd * NCH + c) * 256 < E) {
    int bk[4], rk[4];
#pragma unroll
    for (int k = 0; k < 4; ++k) {
      const bool v = e0 + k < E;
      bk[k] = v ? (int)((unsigned)dd[k] / (unsigned)NPB) : MAXB + 1;
      rk[k] = LDS_ADD(&cnt[bk[k]], 1);
    }
    unsigned w[4];
    bool ovf = false;
#pragma unroll
    for (int k = 0; k < 4; ++k) {
      const unsigned s = (unsigned)(dd[k] - bk[k] * NPB);
      w[k] = ((unsigned)ss[k] << 16) | ((s & 7u) << 13) | ((unsigned)(lane * 4 + k) << 5) | (s >> 3);
      const int d = (rk[k] + (rk[k] < CAP / 2 ? 2 : 4)) & 31;
      unsigned* cp = cT + (rk[k] < CAP ? min(bk[k], MAXB) : MAXB) * CSTR;
      cp[d] = w[k];
      __hip_atomic_fetch_or(cp + (d & 16) + ((d >> 3) & 1), (unsigned)rnd << (4 * (d & 7)),
                            __ATOMIC_RELAXED, __HIP_MEMORY_SCOPE_WORKGROUP);
      ovf |= rk[k] >= CAP;
    }
    if (__builtin_amdgcn_ballot_w64(ovf) != 0) {
#pragma unroll 1
      for (int k = 0; k < 4; ++k) {
        const int bq = k == 0 ? bk[0] : k == 1 ? bk[1] : k == 2 ? bk[2] : bk[3];
        const int rq = k == 0 ? rk[0] : k == 1 ? rk[1] : k == 2 ? rk[2] : rk[3];
        const unsigned wq = k == 0 ? w[0] : k == 1 ? w[1] : k == 2 ? w[2] : w[3];
        if (bq < MAXB && rq >= CAP) {
          const int o = LDS_ADD(&cnt[MAXB], 1);
          OV[c * OVCAP + o] = u32x4{wq >> 9, ((unsigned)(e0 + k) << 5) | (wq & 31u), (unsigned)bq, 0u};
          if (o < 4) { ovl[2 * o] = wq; ovl[2 * o + 1] = ((unsigned)rnd << 12) | (unsigned)bq; }
        }
      }
    }
  }
  asm volatile("s_waitcnt lgkmcnt(0)\n\ts_barrier" ::: "memory");
  {
    const auto rc = SRD(TC, MAXB * NCH * CELL * 4);
    const int p = tid & 7;
    const int first = (CAP / 2) * (p >> 2) + 4 * (p & 3) - 2;
#pragma unroll
    for (int q = 0; q < 2; ++q) {
      const int b = (tid >> 3) + q * 128;
      const int m = cnt[b];
      u32x4 v = *reinterpret_cast<const u32x4*>(cT + b * CSTR + 4 * p);
      if ((p & 3) == 0) v[0] |= (unsigned)min(max(m - first - 2, 0), CAP / 2);
      if (b < nb && (WALL || (p & 3) == 0 || first < m)) CST((b * NCH + c) * CELL + 4 * p, v);
    }
    if (tid < 2) {
      u32x4 v = *reinterpret_cast<const u32x4*>(ovl + 4 * tid);
      v[1] |= (unsigned)cnt[MAXB] << 16;
      v[3] |= (unsigned)cnt[MAXB] << 16;
      __builtin_amdgcn_raw_buffer_store_b128(v, SRD(OVH, NCH * 32), c * 32 + tid * 16, 0, 17);
    }
  }
  if (rnd == 15 && c < NFRAG) {
    if (c < 8 || c == 24 || c == 25) {
#pragma unroll
      for (int j = 0; j < 8; ++j) {
        const unsigned u = __float_as_uint(__uint_as_float(wv8[j] << 16) * NLOG2E);
        wv8[j] = (u + 0x7FFFu + ((u >> 16) & 1u)) >> 16;
      }
    }
    const u32x4 v = {wv8[0] | (wv8[1] << 16), wv8[2] | (wv8[3] << 16), wv8[4] | (wv8[5] << 16), wv8[6] | (wv8[7] << 16)};
    __builtin_amdgcn_raw_buffer_store_b128(v, SRD(wpack, NFRAG * 1024), c * 1024 + lane * 16, 0, 17);
  }
  asm volatile("s_waitcnt vmcnt(0)" :: "v"(t) : "memory");
}

__device__ __forceinline__ NOPK unsigned pack_bf16(float lo, float hi) {
  typedef __attribute__((ext_vector_type(2))) float f2;
  typedef __attribute__((ext_vector_type(2))) __bf16 b2;
  f2 v = {lo, hi};
  b2 r = __builtin_convertvector(v, b2);
  return __builtin_bit_cast(unsigned, r);
}
#define WGSYNC() do { __builtin_amdgcn_fence(__ATOMIC_RELEASE, "workgroup"); __builtin_amdgcn_s_barrier(); \
                      __builtin_amdgcn_fence(__ATOMIC_ACQUIRE, "workgroup"); } while (0)
#define U2F(u) __builtin_bit_cast(float, (unsigned)(u))
constexpr int XB = 1040;
constexpr int RING_SLOT = 4 * XB + 1024;
constexpr int LCAP = 512;
constexpr int LPAD = LCAP + 96;
constexpr int SSCR = 32 * 80;
constexpr int WAVE_LDS = 2 * RING_SLOT;
constexpr int M_WL = 0;
constexpr int M_OH = 16 * 1024;
constexpr int M_CNT = M_OH + 2048;
constexpr int M_ST = M_CNT + 256;
constexpr int M_SC = M_ST + 2048;
constexpr int M_LX = M_SC + GPB * SSCR;
constexpr int M_LE = M_LX + GPB * LPAD * 4;
constexpr int M_WV = M_LE + GPB * LPAD * 4;
constexpr int M_XG = M_WV + 4 * WAVE_LDS;
constexpr int MAIN_LDS = M_WV + GPB * WAVE_LDS;
static_assert(M_LX % 256 == 0 && M_LE % 256 == 0 && M_LE < 65536 && M_LE + GPB * LPAD * 4 + NCH * CELL * 4 <= M_XG, "lists");
static_assert(MAIN_LDS <= 160 * 1024, "LDS");

__global__ __launch_bounds__(512, 2) NOPK void k_main(const unsigned short* __restrict__ x,
                                                      const unsigned short* __restrict__ eattr,
                                                      const char* __restrict__ wsp, int E,
                                                      int N, unsigned short* __restrict__ out) {
  __shared__ __attribute__((aligned(16))) char smem[MAIN_LDS];
  asm volatile("" :: "s"(x), "s"(eattr), "s"(wsp), "s"(E), "s"(N), "s"(out));
  const unsigned* TC = reinterpret_cast<const unsigned*>(wsp + W_TC);
  const u32x4* OVH = reinterpret_cast<const u32x4*>(wsp + W_OVH);
  const u32x4* OV = reinterpret_cast<const u32x4*>(wsp + W_OV);
  const char* wpack = wsp + W_WP;
  const int tid = threadIdx.x, lane = tid & 63;
  const int wave = __builtin_amdgcn_readfirstlane(tid >> 6);
  const int r = lane & 31, h = lane >> 5;
  const int b = blockIdx.x;

  const int pc = lane & 7;
  u32x4 tv[4], ovh;
  {
    const unsigned* cp = TC + ((size_t)b * NCH + wave * 32) * CELL;
    asm volatile("global_load_dwordx4 %0, %5, %6\n\tglobal_load_dwordx4 %1, %5, %6 offset:1024\n\t"
                 "global_load_dwordx4 %2, %5, %6 offset:2048\n\tglobal_load_dwordx4 %3, %5, %6 offset:3072\n\t"
                 "global_load_dwordx4 %4, %5, %7"
                 : "=&v"(tv[0]), "=&v"(tv[1]), "=&v"(tv[2]), "=&v"(tv[3]), "=&v"(ovh)
                 : "v"(lane * 16), "s"(cp), "s"(OVH + wave * 64) : "memory");
  }
#define GLDS(vo, rs, mv) asm volatile("s_mov_b32 m0, %2\n\ts_nop 0\n\tbuffer_load_dwordx4 %0, %1, 0 offen lds" \
                                      :: "v"(vo), "s"(rs), "s"(mv) : "memory")
  const auto rx = SRD(x, N * 128);
  const auto re = SRD(eattr, E * 32);
  const unsigned xc0 = (unsigned)(((lane & 7) ^ ((lane >> 2) & 4)) * 16);
  const unsigned lb = (unsigned)(size_t)(LAS char*)smem;
  LAS int* cnt = (LAS int*)(smem + M_CNT);
  if (tid < 32) cnt[tid] = (tid & 3) == 0 ? (tid >> 2) * (LPAD * 4) : (tid & 3) == 2 ? 127 : 0;
  __builtin_amdgcn_s_setprio(1);
  {
    unsigned* oh = reinterpret_cast<unsigned*>(smem + M_OH);
    const int i = tid;
    const int sl = i >> 4, hh = (i >> 3) & 1, ks = (i >> 2) & 1, jp = i & 3;
    const int j0 = 2 * jp, j1 = 2 * jp + 1;
    const int nd0 = 16 * ks + 8 * (j0 >> 2) + 4 * hh + (j0 & 3);
    const int nd1 = 16 * ks + 8 * (j1 >> 2) + 4 * hh + (j1 & 3);
    oh[i] = (sl == nd0 ? 0x3F80u : 0u) | (sl == nd1 ? 0x3F800000u : 0u);
  }
  asm volatile("s_waitcnt lgkmcnt(0)\n\ts_barrier" ::: "memory");
  {
    const int nw = (min(NPB - wave, N - b * NPB - wave) + 7) >> 3;
#pragma unroll
    for (int k = 0; k < 4; ++k) {
      const int s = (lane >> 3) * 4 + k;
      const unsigned xo = s < nw ? (unsigned)(b * NPB + wave + 8 * s) << 7 : 0xFFFFFF80u;
      GLDS(xo | xc0, rx, lb + M_XG + wave * (4 * XB) + k * XB);
    }
  }
  const LAS bf16x8* wl = (const LAS bf16x8*)(smem + M_WL);
  const LAS bf16x8* ohl = (const LAS bf16x8*)(smem + M_OH);
  LAS unsigned* lxa = (LAS unsigned*)(smem + M_LX);
  LAS unsigned* lea = (LAS unsigned*)(smem + M_LE);
  char* wbase = smem + M_WV + wave * WAVE_LDS;
  const unsigned ring_a = (unsigned)(size_t)(LAS char*)wbase;
  {
    unsigned long long vm[16];
    unsigned ad[16], xe[16], hw[4];
    const unsigned four = 4u;
#pragma unroll
    for (int k = 0; k < 4; ++k) {
      asm volatile("s_waitcnt vmcnt(%1)" : "+v"(tv[k]) : "n"(8 - k) : "memory");
      const unsigned h0 = (unsigned)__builtin_amdgcn_mov_dpp((int)tv[k][0], 0x00, 0xF, 0xF, true);
      const unsigned h1 = (unsigned)__builtin_amdgcn_mov_dpp((int)tv[k][1], 0x00, 0xF, 0xF, true);
      hw[k] = ((lane & 2) ? h1 : h0) >> ((lane & 1) * 16);
      const unsigned m = h0 & 0xFFu;
      unsigned ci[4];
#pragma unroll
      for (int j = 0; j < 4; ++j) {
        vm[4 * k + j] = __builtin_amdgcn_ballot_w64((unsigned)((lane & 3) * 4 + j - 2) < m);
        xe[4 * k + j] = tv[k][j] >> 9;
        ci[j] = (xe[4 * k + j] & 0x70u) + lb;
      }
      asm volatile("s_mov_b64 exec, %4\n\tds_add_rtn_u32 %0, %8, %12 offset:%13\n\t"
                   "s_mov_b64 exec, %5\n\tds_add_rtn_u32 %1, %9, %12 offset:%13\n\t"
                   "s_mov_b64 exec, %6\n\tds_add_rtn_u32 %2, %10, %12 offset:%13\n\t"
                   "s_mov_b64 exec, %7\n\tds_add_rtn_u32 %3, %11, %12 offset:%13\n\t"
                   "s_mov_b64 exec, -1"
                   : "=&v"(ad[4 * k]), "=&v"(ad[4 * k + 1]), "=&v"(ad[4 * k + 2]), "=&v"(ad[4 * k + 3])
                   : "s"(vm[4 * k]), "s"(vm[4 * k + 1]), "s"(vm[4 * k + 2]), "s"(vm[4 * k + 3]),
                     "v"(ci[0]), "v"(ci[1]), "v"(ci[2]), "v"(ci[3]), "v"(four), "n"(M_CNT)
                   : "memory");
    }
#pragma unroll
    for (int k = 0; k < 4; ++k) {
      const unsigned c13 = (unsigned)(wave * 32 + k * 8 + (lane >> 3)) << 13;
      unsigned ee[4];
#pragma unroll
      for (int j = 0; j < 4; ++j) ee[j] = (tv[k][j] & 0x1FFFu) | ((((hw[k] >> (4 * j)) & 15u) << 21) | c13);
      asm volatile("s_waitcnt lgkmcnt(12)\n\t"
                   "s_mov_b64 exec, %0\n\tds_write2st64_b32 %4, %8, %12 offset0:%16 offset1:%17\n\t"
                   "s_mov_b64 exec, %1\n\tds_write2st64_b32 %5, %9, %13 offset0:%16 offset1:%17\n\t"
                   "s_mov_b64 exec, %2\n\tds_write2st64_b32 %6, %10, %14 offset0:%16 offset1:%17\n\t"
                   "s_mov_b64 exec, %3\n\tds_write2st64_b32 %7, %11, %15 offset0:%16 offset1:%17\n\t"
                   "s_mov_b64 exec, -1"
                   :: "s"(vm[4 * k]), "s"(vm[4 * k + 1]), "s"(vm[4 * k + 2]), "s"(vm[4 * k + 3]),
                      "v"(ad[4 * k]), "v"(ad[4 * k + 1]), "v"(ad[4 * k + 2]), "v"(ad[4 * k + 3]),
                      "v"(xe[4 * k]), "v"(xe[4 * k + 1]), "v"(xe[4 * k + 2]), "v"(xe[4 * k + 3]),
                      "v"(ee[0]), "v"(ee[1]), "v"(ee[2]), "v"(ee[3]), "n"(M_LX / 256), "n"(M_LE / 256)
                   : "memory");
    }
  }
    {
      const auto rw = SRD(wpack, NFRAG * 1024);
      const unsigned vo = (unsigned)(wave * 1024 + lane * 16);
      GLDS(vo + 16384u, rw, lb + M_SC + 8192 + wave * 1024);
      GLDS(vo, rw, lb + M_SC + wave * 1024);
      asm volatile("s_mov_b32 m0, %2\n\ts_nop 0\n\tbuffer_load_dword %0, %1, 0 offen lds"
                   :: "v"(24576u + (vo >> 2)), "s"(rw), "s"(lb + M_SC + 16384 + wave * 256) : "memory");
      GLDS(vo + 8192u, rw, lb + M_WL + wave * 1024);
      GLDS(vo + 26624u, rw, lb + M_WL + 8192 + wave * 1024);
    }
  asm volatile("s_waitcnt vmcnt(9)" : "+v"(ovh) :: "memory");
  if (__builtin_amdgcn_ballot_w64((ovh[1] & 0x100u) == 0u) != 0) {
#pragma unroll
    for (int q = 0; q < 2; ++q) {
      const unsigned w = ovh[2 * q], m = ovh[2 * q + 1];
      if ((m & 0x1FFu) == (unsigned)b) {
        LAS unsigned* p = (LAS unsigned*)((LAS char*)lxa + LDS_ADD((LAS int*)((LAS char*)cnt + ((w >> 9) & 0x70u)), 4));
        p[0] = w >> 9;
        p[GPB * LPAD] = (w & 0x1FFFu) | ((m & 0xF000u) << 9) | ((unsigned)(tid >> 1) << 13);
      }
    }
    if ((ovh[1] >> 16) > 4u) cnt[1] = 1;
  }
  asm volatile("s_waitcnt lgkmcnt(0)\n\ts_barrier" ::: "memory");
  const int cpc = cnt[4 * pc] - pc * (LPAD * 4);
  const int cpa = cnt[4 * (lane >> 3)] - (lane >> 3) * (LPAD * 4);
  int mo0 = cnt[1];
  bool ready = __builtin_amdgcn_ballot_w64(cpc > LCAP * 4) == 0;
  int grp;
  {
    const unsigned long long gt = __builtin_amdgcn_ballot_w64(cpc * 2 + pc > cpa * 2 + (lane >> 3));
    const unsigned wd = h ? (unsigned)(gt >> 32) : (unsigned)gt;
    const int rk = __builtin_popcount((wd >> (r & 24)) & 0xFFu);
    grp = __builtin_ctzll(__builtin_amdgcn_ballot_w64(rk == (wave < 4 ? wave : 11 - wave))) >> 3;
  }
  asm volatile("" : "+v"(mo0));
  const int gn0 = b * NPB + grp;
  const int nodes = max((min(NPB - grp, N - gn0) + 7) >> 3, 0);
  const int n0 = min(gn0, N - 1);
  ((LAS unsigned*)(smem + M_ST))[tid] = (ready ? 0x40000u : 0xFFFFu) | ((unsigned)(tid & 1) << 16);

  f32x16 Y[4];
#pragma unroll
  for (int q = 0; q < 4; ++q)
#pragma unroll
    for (int i = 0; i < 16; ++i) Y[q][i] = q < 2 ? 1e-6f : 0.f;

  const unsigned ec0 = (unsigned)(((lane & 1) ^ ((lane >> 4) & 1)) * 16);
  const LAS unsigned* lx = lxa + grp * LPAD;
  const LAS unsigned* le = lea + grp * LPAD;
  const int ohoff = ((((r >> 2) & 1) * 16) + ((r >> 4) * 8) + (((r >> 3) & 1) * 4) + (r & 3)) * 2;
  const int xa0 = (r & 3) * XB + (r >> 2) * 128 + (h ^ ((r >> 3) & 1)) * 64;
  const int ea0 = 4 * XB + r * 32 + ((h ^ ((r >> 3) & 1)) * 16);
#define SB() __builtin_amdgcn_sched_barrier(0)
  bf16x8 wB[4][2], wC[2], XAf[2][2];
  const LAS bf16x8* wvl = wl + lane;
  f32x16 G0, G1, V;
  u32x4 pd[2], pn[2];
  bf16x8 S[2], St[2], as[4], ae, bv[4];
  const f32x16 zero16 = {0.f, 0.f, 0.f, 0.f, 0.f, 0.f, 0.f, 0.f, 0.f, 0.f, 0.f, 0.f, 0.f, 0.f, 0.f, 0.f};
  LAS char* sm = (LAS char*)(smem + M_SC + wave * SSCR);
  LAS u32x4* mine = (LAS u32x4*)(sm + r * 80 + h * 32);
  const LAS char* ring = (const LAS char*)wbase;
  unsigned ga[5];
  unsigned slr;
  int sl;
  int rs = 0;

#define E1(i) Gc[i] = __builtin_amdgcn_exp2f(Gc[i])
#define E2(i) Gc[i] = Gc[i] + 1.0f
#define E3(i) Gc[i] = __builtin_amdgcn_rcpf(Gc[i])
#define MU(i) V[i] = V[i] * Gc[i]
#define CD(j) pd[(j) >> 2][(j) & 3] = pack_bf16(Gc[2 * (j)], Gc[2 * (j) + 1])
#define CN(j) pn[(j) >> 2][(j) & 3] = pack_bf16(V[2 * (j)], V[2 * (j) + 1])
#define XG(k) ((xo[k] & ~127u) | xc0)
#define BC(v) __builtin_bit_cast(bf16x8, v)
#define PIN(v) asm volatile("" : "+v"(v))

  int more, ntiles;
  LAS int* pown = cnt + 4 * wave + 2;
  const LAS int* poth = cnt + 4 * (wave ^ 4) + 2;
  PIN(pown); PIN(poth);
  const LAS unsigned* lxp = lx + (lane >> 3) * 4;
  const LAS unsigned* lep = le + (lane >> 1);
  const LAS unsigned* lsp = le + r;
  auto round_begin = [&](int cb, int mo) NOPK __attribute__((always_inline)) {
    const int cn = min(cb >> 2, LCAP);
    more = __builtin_amdgcn_readfirstlane(mo);
    ntiles = (cn + 31) >> 5;
    {
      LAS unsigned* wx = lxa + grp * LPAD;
      LAS unsigned* we = lea + grp * LPAD;
      const int e0 = ntiles * 32;
      if (cn + r < e0) { wx[cn + r] = 0xFFFFFF80u; we[cn + r] = 0xFFFFFFFFu; }
      wx[e0 + lane] = lane < nodes ? (unsigned)(n0 + 8 * lane) << 7 : 0xFFFFFF80u;
      we[e0 + lane] = 0xFFFFFFFFu;
      wx[e0 + 64 + r] = 0xFFFFFF80u;
      we[e0 + 64 + r] = 0xFFFFFFFFu;
    }
  };
  auto gather = [&](int t, bool issue) NOPK __attribute__((always_inline)) {
    const u32x4 xo = *(const LAS u32x4*)(lxp + t * 32);
    const unsigned eo = lep[t * 32];
    ga[0] = XG(0); ga[1] = XG(1); ga[2] = XG(2); ga[3] = XG(3);
    ga[4] = (eo & ~31u) | ec0;
    if (issue) {
      const unsigned d = ring_a + (rs ^ (t * RING_SLOT));
      GLDS(ga[0], rx, d); GLDS(ga[1], rx, d + XB); GLDS(ga[2], rx, d + 2 * XB); GLDS(ga[3], rx, d + 3 * XB);
      GLDS(ga[4], re, d + 4 * XB);
    }
  };
  if (ready) {
    round_begin(__builtin_amdgcn_readlane(cpc, grp), mo0);
    if (wave < 4) gather(0, true);
  }
  asm volatile("s_cmp_lg_u32 %0, 0\n\ts_cbranch_scc1 1f\n\ts_waitcnt vmcnt(0)\n1:\n\ts_waitcnt vmcnt(5)\n\ts_barrier"
               :: "s"(__builtin_amdgcn_readfirstlane((ready && wave < 4) ? 1 : 0)) : "memory", "scc");
  {
      const LAS bf16x8* ws = (const LAS bf16x8*)(smem + M_SC) + lane;
      bf16x8 xg[4], wA[8];
#pragma unroll
      for (int k = 0; k < 4; ++k) xg[k] = *(const LAS bf16x8*)(smem + M_XG + grp * (4 * XB) + xa0 + 16 * k);
#pragma unroll
      for (int k = 0; k < 8; ++k) wA[k] = ws[(8 + k) * 64];
#pragma unroll
      for (int k = 0; k < 8; ++k) wB[k >> 1][k & 1] = ws[k * 64];
      wC[0] = ws[16 * 64];
      wC[1] = ws[17 * 64];
      SB();
      asm volatile("s_waitcnt lgkmcnt(0)\n\ts_barrier"
                   : "+v"(wB[0][0]), "+v"(wB[0][1]), "+v"(wB[1][0]), "+v"(wB[1][1]), "+v"(wB[2][0]), "+v"(wB[2][1]),
                     "+v"(wB[3][0]), "+v"(wB[3][1]), "+v"(wC[0]), "+v"(wC[1]),
                     "+v"(xg[0]), "+v"(xg[1]), "+v"(xg[2]), "+v"(xg[3]), "+v"(wA[0]), "+v"(wA[1]), "+v"(wA[2]),
                     "+v"(wA[3]), "+v"(wA[4]), "+v"(wA[5]), "+v"(wA[6]), "+v"(wA[7]) :: "memory");
      SB();
      if (ready) {
        if (wave >= 4) gather(0, true);

      }
      SB();
#pragma unroll
      for (int ct = 0; ct < 2; ++ct) {
        f32x16 A = zero16;
#pragma unroll
        for (int k = 0; k < 4; ++k) A = MFMA32(xg[k], wA[k * 2 + ct], A);
#pragma unroll
        for (int s = 0; s < 2; ++s) {
          u32x4 p;
#pragma unroll
          for (int j = 0; j < 4; ++j) p[j] = pack_bf16(A[8 * s + 2 * j] * NLOG2E, A[8 * s + 2 * j + 1] * NLOG2E);
          XAf[s][ct] = BC(p);
        }
      }
      const u32x4 z4 = {0u, 0u, 0u, 0u};
      mine[0] = z4;
      mine[1] = z4;
      SB();
      if (ready) gather(1, true);
    }
  for (;;) {
   if (!ready) {
    asm volatile("s_waitcnt vmcnt(0)" ::: "memory");
    WGSYNC();
    {
      int t2 = (int)__builtin_amdgcn_mbcnt_hi(~0u, __builtin_amdgcn_mbcnt_lo(~0u, 0u));
      asm volatile("" : "+v"(t2));
      const int l2 = t2;
      t2 += wave * 64;
      if (t2 < 32) cnt[t2] = (t2 & 3) == 0 ? (t2 >> 2) * (LPAD * 4) : 0;
      WGSYNC();
      auto put = [&](unsigned t, unsigned e) NOPK -> bool {
        const unsigned gg = (t >> 4) & 7u;
        const int ad = LDS_ADD(&cnt[4 * gg], 4) >> 2;
        if (ad - (int)gg * LPAD >= LCAP) return false;
        lxa[ad] = t;
        lea[ad] = e;
        return true;
      };
      LAS unsigned* stp = (LAS unsigned*)(smem + M_ST) + t2;
      unsigned pe = *stp & 0xFFFFu, ocur = *stp >> 16;
#pragma unroll 1
      for (int j = 0; j < 16; ++j) {
        if ((pe >> j) & 1u) {
          const int cc = wave * 32 + (j >> 2) * 8 + (l2 >> 3);
          const unsigned* cp = TC + ((size_t)b * NCH + cc) * CELL;
          const int d = (l2 & 7) * 4 + (j & 3);
          const unsigned w = cp[d];
          const unsigned nib = (cp[(d & 16) + ((d >> 3) & 1)] >> (4 * (d & 7))) & 15u;
          if ((unsigned)((d & 15) - 2) >= (cp[d & 16] & 0xFFu) ||
              put(w >> 9, (w & 0x1FFFu) | (nib << 21) | ((unsigned)cc << 13))) pe &= ~(1u << j);
        }
      }
      const unsigned on = OVH[t2][1] >> 16;
      const size_t ob = (size_t)(t2 >> 1) * OVCAP;
#pragma unroll 1
      for (; ocur < on; ocur += 2) {
        const u32x4 v = OV[ob + ocur];
        if ((int)v[2] == b && !put(v[0], v[1])) break;
      }
      if (pe != 0u || ocur < on) cnt[1] = 1;
      *stp = pe | (ocur << 16);
    }
    WGSYNC();
    round_begin(__builtin_amdgcn_readfirstlane(cnt[4 * grp]) - grp * (LPAD * 4), cnt[1]);
    gather(0, true);
    gather(1, true);
   }
    gather(2, false);
    ready = false;
    sl = lsp[0] & 31u;
    St[0] = ohl[sl * 4 + h * 2 + 0];
    St[1] = ohl[sl * 4 + h * 2 + 1];
    bv[0] = wvl[0 * 64];
    bv[1] = wvl[2 * 64];
    asm volatile("s_waitcnt vmcnt(5)" ::: "memory");
#pragma unroll
    for (int k = 0; k < 4; ++k) as[k] = *(const LAS bf16x8*)(ring + rs + xa0 + 16 * k);
    ae = *(const LAS bf16x8*)(ring + rs + ea0);
    G0 = MFMA32(St[0], XAf[0][0], zero16);
    G0 = MFMA32(St[1], XAf[1][0], G0);
    G0 = MFMA32(ae, wC[0], G0);
#pragma unroll
    for (int k = 0; k < 4; ++k) G0 = MFMA32(as[k], wB[k][0], G0);
    pd[1] = u32x4{0u, 0u, 0u, 0u};
    pn[1] = u32x4{0u, 0u, 0u, 0u};
    S[1] = BC(pd[1]);
    SB();
    for (int i = 0; i < ntiles; ++i) {
      {
        f32x16& Gc = G0;
        const unsigned d = ring_a + rs;
        LAS unsigned short* ohw = (LAS unsigned short*)(sm + sl * 80 + ohoff);
        Y[1] = MFMA32(BC(pd[1]), S[1], Y[1]);
        bv[2] = wvl[4 * 64];
        *(volatile LAS int*)pown = ntiles - i;
        E1(0); E1(1); E1(2); E1(3);
        SB();
        Y[3] = MFMA32(BC(pn[1]), S[1], Y[3]);
        bv[3] = wvl[6 * 64];
        E2(0); E2(1); E1(4); E1(5); E2(2); E2(3);
        SB();
        V = MFMA32(as[0], bv[0], zero16);
        const int prem = *(volatile const LAS int*)poth;
        E3(0); E3(1); E1(6); E1(7); E2(4); E2(5);
        SB();
        V = MFMA32(as[1], bv[1], V);
        E3(2); E3(3); E1(8); E1(9); E2(6); E2(7);
        SB();
        V = MFMA32(as[2], bv[2], V);
        E3(4); E3(5); E1(10); E1(11); E2(8); E2(9); CD(0);
        SB();
        V = MFMA32(as[3], bv[3], V);
        E3(6); E3(7); E1(12); E1(13); E2(10); E2(11); CD(1);
        SB();
        G1 = MFMA32(St[0], XAf[0][1], zero16);
        asm volatile("s_cmp_ge_i32 %0, %1\n\ts_setprio 0\n\ts_cbranch_scc0 1f\n\ts_setprio 1\n1:"
                     :: "s"(ntiles - i), "s"(__builtin_amdgcn_readfirstlane(prem)) : "scc");
        E3(8); E3(9); E1(14); E1(15); E2(12); E2(13); CD(2);
        SB();
        G1 = MFMA32(St[1], XAf[1][1], G1);
        *ohw = (unsigned short)0x3F80;
        asm volatile("" ::: "memory");
        S[0] = BC(mine[0]);
        S[1] = BC(mine[1]);
        asm volatile("" ::: "memory");
        *ohw = (unsigned short)0;
        E3(10); E3(11); E2(14); E2(15); CD(3);
        SB();
        G1 = MFMA32(ae, wC[1], G1);
        GLDS(ga[0], rx, d);
        E3(12); E3(13); MU(0); MU(1); MU(2); MU(3);
        SB();
        G1 = MFMA32(as[0], wB[0][1], G1);
        GLDS(ga[1], rx, d + XB);
        E3(14); E3(15); MU(4); MU(5); CN(0); CN(1);
        SB();
        G1 = MFMA32(as[1], wB[1][1], G1);
        GLDS(ga[2], rx, d + 2 * XB);
        MU(6); MU(7); CD(4); CD(5); CN(2);
        SB();
        G1 = MFMA32(as[2], wB[2][1], G1);
        GLDS(ga[3], rx, d + 3 * XB);
        MU(8); MU(9); MU(10); MU(11); CN(3); CD(6);
        SB();
        G1 = MFMA32(as[3], wB[3][1], G1);
        GLDS(ga[4], re, d + 4 * XB);
        slr = lsp[(i + 1) * 32];
        MU(12); MU(13); CN(4); CN(5); CD(7);
        SB();
        Y[0] = MFMA32(BC(pd[0]), S[0], Y[0]);
        bv[0] = wvl[1 * 64];
        MU(14); MU(15); CN(6);
        SB();
        Y[2] = MFMA32(BC(pn[0]), S[0], Y[2]);
        bv[1] = wvl[3 * 64];
        CN(7);
        SB();
      }
      rs ^= RING_SLOT;
      {
        f32x16& Gc = G1;
        const LAS char* rp = ring + rs;
        sl = slr & 31u;
        Y[0] = MFMA32(BC(pd[1]), S[1], Y[0]);
        bv[2] = wvl[5 * 64];
        St[0] = ohl[sl * 4 + h * 2 + 0];
        E1(0); E1(1); E1(2); E1(3);
        SB();
        Y[2] = MFMA32(BC(pn[1]), S[1], Y[2]);
        bv[3] = wvl[7 * 64];
        St[1] = ohl[sl * 4 + h * 2 + 1];
        E2(0); E2(1); E1(4); E1(5); E2(2); E2(3);
        SB();
        V = MFMA32(as[0], bv[0], zero16);
        E3(0); E3(1); E1(6); E1(7); E2(4); E2(5);
        SB();
        V = MFMA32(as[1], bv[1], V);
        E3(2); E3(3); E1(8); E1(9); E2(6); E2(7);
        SB();
        V = MFMA32(as[2], bv[2], V);
        asm volatile("s_waitcnt vmcnt(5)" ::: "memory");
        ae = *(const LAS bf16x8*)(rp + ea0);
        E3(4); E3(5); E1(10); E1(11); E2(8); E2(9); CD(0);
        SB();
        V = MFMA32(as[3], bv[3], V);
        as[0] = *(const LAS bf16x8*)(rp + xa0);
        E3(6); E3(7); E1(12); E1(13); E2(10); E2(11); CD(1);
        SB();
        G0 = MFMA32(St[0], XAf[0][0], zero16);
        as[1] = *(const LAS bf16x8*)(rp + xa0 + 16);
        E3(8); E3(9); E1(14); E1(15); E2(12); E2(13); CD(2);
        SB();
        G0 = MFMA32(St[1], XAf[1][0], G0);
        as[2] = *(const LAS bf16x8*)(rp + xa0 + 32);
        E3(10); E3(11); E2(14); E2(15); CD(3);
        SB();
        G0 = MFMA32(ae, wC[0], G0);
        as[3] = *(const LAS bf16x8*)(rp + xa0 + 48);
        E3(12); E3(13); MU(0); MU(1); MU(2); MU(3);
        SB();
        G0 = MFMA32(as[0], wB[0][0], G0);
        const u32x4 xo = *(const LAS u32x4*)(lxp + (i + 3) * 32);
        const unsigned eo = lep[(i + 3) * 32];
        E3(14); E3(15); MU(4); MU(5); CN(0); CN(1);
        SB();
        G0 = MFMA32(as[1], wB[1][0], G0);
        MU(6); MU(7); CD(4); CD(5); CN(2);
        SB();
        G0 = MFMA32(as[2], wB[2][0], G0);
        MU(8); MU(9); MU(10); MU(11); CN(3); CD(6);
        SB();
        G0 = MFMA32(as[3], wB[3][0], G0);
        ga[0] = XG(0); ga[1] = XG(1);
        PIN(ga[0]); PIN(ga[1]);
        MU(12); MU(13); CN(4); CN(5); CD(7);
        SB();
        Y[1] = MFMA32(BC(pd[0]), S[0], Y[1]);
        bv[0] = wvl[0 * 64];
        ga[2] = XG(2); ga[3] = XG(3);
        PIN(ga[2]); PIN(ga[3]);
        MU(14); MU(15); CN(6);
        SB();
        Y[3] = MFMA32(BC(pn[0]), S[0], Y[3]);
        bv[1] = wvl[2 * 64];
        ga[4] = (eo & ~31u) | ec0;
        PIN(ga[4]);
        CN(7);
        SB();
      }
    }
    *(volatile LAS int*)pown = 0;
    Y[1] = MFMA32(BC(pd[1]), S[1], Y[1]);
    Y[3] = MFMA32(BC(pn[1]), S[1], Y[3]);
    if (!more) break;
  }
  {
    bf16x8 fa[2][6];
    fa[0][0] = fa[1][0] = ohl[r * 4 + h * 2];
    fa[0][1] = fa[1][1] = ohl[r * 4 + h * 2 + 1];
#pragma unroll
    for (int k = 0; k < 8; ++k) fa[k & 1][2 + (k >> 1)] = wvl[(8 + k) * 64];
    LAS char* zw = (LAS char*)wbase + r * 144 + h * 64;
    u32x4 o;
#define FQ(ct, i) do { float t_ = Y[2 + (ct)][i] * __builtin_amdgcn_rcpf(Y[ct][i]); PIN(t_); Y[2 + (ct)][i] = t_; } while (0)
#define FM(ct, m) Y[2 + (ct)] = MFMA32(fa[ct][m], as[(m) < 2 ? 2 * (ct) + (m) : (m) - 2], Y[2 + (ct)])
#define FO(q, j) do { const unsigned w = __builtin_bit_cast(u32x4, as[q])[j];                              \
      float z0 = Y[2 + ((q) >> 1)][((q) & 1) * 8 + 2 * (j)], z1 = Y[2 + ((q) >> 1)][((q) & 1) * 8 + 2 * (j) + 1]; \
      o[j] = pack_bf16(__builtin_amdgcn_fmed3f(z0, U2F(w << 16), 3.4028234e38f),                            \
                       __builtin_amdgcn_fmed3f(z1, U2F(w & 0xffff0000u), 3.4028234e38f));                                                                            \
      if ((j) == 3) *(LAS u32x4*)(zw + (q) * 16) = o; } while (0)
#pragma unroll
    for (int i = 0; i < 16; ++i) FQ(0, i);
    SB();
#pragma unroll
    for (int m = 0; m < 6; ++m) {
      FM(0, m);
#pragma unroll
      for (int i = 3 * m; i < 3 * m + 3 && i < 16; ++i) FQ(1, i);
      SB();
    }
    asm volatile("s_waitcnt vmcnt(0)" ::: "memory");
    FM(1, 0); SB();
    FM(1, 1); FO(0, 0); SB();
    FM(1, 2); FO(0, 1); FO(0, 2); SB();
    FM(1, 3); FO(0, 3); FO(1, 0); SB();
    FM(1, 4); FO(1, 1); FO(1, 2); SB();
    FM(1, 5); FO(1, 3); SB();
    FO(2, 0); FO(2, 1); FO(2, 2); FO(2, 3); FO(3, 0); FO(3, 1); FO(3, 2); FO(3, 3);
  }
  const LAS char* zl = (const LAS char*)wbase;
  u32x4 o[4];
#pragma unroll
  for (int q = 0; q < 4; ++q) o[q] = *(const LAS u32x4*)(zl + (q * 8 + (lane >> 3)) * 144 + (lane & 7) * 16);
#pragma unroll
  for (int q = 0; q < 4; ++q) {
    const int row = q * 8 + (lane >> 3);
    __builtin_amdgcn_raw_buffer_store_b128(o[q], SRD(out, N * 128),
                                           row < nodes ? (n0 + 8 * row) * 128 + (lane & 7) * 16 : 0x7FFFFFF0, 0, 17);
  }
}

extern "C" void kernel_launch(void* const* d_in, const int* in_sizes, int n_in,
                              void* d_out, int out_size, void* d_ws, size_t ws_size,
                              hipStream_t stream) {
  const unsigned short* x = (const unsigned short*)d_in[0];
  const unsigned short* eattr = (const unsigned short*)d_in[1];
  const int* src = (const int*)d_in[2];
  const int* dst = (const int*)d_in[3];
  const unsigned short* wa = (const unsigned short*)d_in[4];
  const unsigned short* wb = (const unsigned short*)d_in[5];
  const unsigned short* wc = (const unsigned short*)d_in[6];
  const unsigned short* wu = (const unsigned short*)d_in[7];
  const unsigned short* wv = (const unsigned short*)d_in[8];
  unsigned short* out = (unsigned short*)d_out;
  const int N = in_sizes[0] / 64;
  const int E = in_sizes[2];
  const int nb = (N + NPB - 1) / NPB;

  char* ws = (char*)d_ws;
  k_part<<<NCH, 1024, 0, stream>>>(src, dst, E, nb, (unsigned*)(ws + W_TC), (u32x4*)(ws + W_OVH), (u32x4*)(ws + W_OV),
                                   wa, wb, wc, wu, wv, (unsigned short*)(ws + W_WP));
  k_main<<<nb, 512, 0, stream>>>(x, eattr, ws, E, N, out);
}
```

```cpp
#include <hip/hip_runtime.h>
#include <hip/hip_bf16.h>
#include <stdint.h>

using bf16x8 = __attribute__((ext_vector_type(8))) short;
using f16x8 = __attribute__((ext_vector_type(8))) _Float16;
using f16x2 = __attribute__((ext_vector_type(2))) _Float16;
using f32x16 = __attribute__((ext_vector_type(16))) float;
using u32x4 = __attribute__((ext_vector_type(4))) unsigned;
using u32x2 = __attribute__((ext_vector_type(2))) unsigned;
using i32x4 = __attribute__((ext_vector_type(4))) int;
using f32x4 = __attribute__((ext_vector_type(4))) float;

#define MFMA32(a, b, c) __builtin_amdgcn_mfma_f32_32x32x16_bf16((a), (b), (c), 0, 0, 0)
#define MFMA32H(a, b, c) __builtin_amdgcn_mfma_f32_32x32x16_f16((a), (b), (c), 0, 0, 0)
#define LAS __attribute__((address_space(3)))
#if defined(__HIP_DEVICE_COMPILE__)
#define NOPK __attribute__((target("no-packed-fp32-ops")))
#else
#define NOPK
#endif
#define SRD(p, bytes) __builtin_amdgcn_make_buffer_rsrc((void*)(p), (short)0, (int)(bytes), 0x00020000)
#define LDS_ADD(p, v) __hip_atomic_fetch_add((p), (v), __ATOMIC_RELAXED, __HIP_MEMORY_SCOPE_WORKGROUP)

#define CST(p, v) __builtin_amdgcn_raw_buffer_store_b128((v), rc, (p) * 4, 0, 17)
#define WALL 1
constexpr int GPB = 8;
constexpr int NPB = 196;
constexpr int MAXB = 256;
constexpr int NCH = 256;
constexpr int CELL = 32;
constexpr int CAP = 28;
constexpr int CSTR = 36;
constexpr int OVCAP = 4096;
constexpr int NFRAG = 34;
constexpr size_t W_TC = 0;
constexpr size_t W_OVH = W_TC + (size_t)MAXB * NCH * CELL * 4;
constexpr size_t W_WP = W_OVH + NCH * 32;
constexpr size_t W_OV = W_WP + NFRAG * 1024;
constexpr float NLOG2E = -1.44269504088896340736f;

__global__ __launch_bounds__(1024) void k_part(const int* __restrict__ src, const int* __restrict__ dst,
                                               int E, int nb,
                                               unsigned* __restrict__ TC,
                                               u32x4* __restrict__ OVH, u32x4* __restrict__ OV,
                                               const unsigned short* __restrict__ wa,
                                               const unsigned short* __restrict__ wb,
                                               const unsigned short* __restrict__ wc,
                                               const unsigned short* __restrict__ wu,
                                               const unsigned short* __restrict__ wv,
                                               unsigned short* __restrict__ wpack) {
  __shared__ __attribute__((aligned(16))) unsigned cT[(MAXB + 1) * CSTR];
  __shared__ __attribute__((aligned(16))) unsigned ovl[8];
  __shared__ int cnt[MAXB + 4];
  const int tid = threadIdx.x, lane = tid & 63;
  const int rnd = __builtin_amdgcn_readfirstlane(tid >> 6);
  const int c = blockIdx.x;
  const int e0 = (rnd * NCH + c) * 256 + lane * 4;
  const auto rd = SRD(dst, E * 4), rs = SRD(src, E * 4);

  const i32x4 dd = __builtin_bit_cast(i32x4, __builtin_amdgcn_raw_buffer_load_b128(rd, e0 * 4, 0, 0));
  const i32x4 ss = __builtin_bit_cast(i32x4, __builtin_amdgcn_raw_buffer_load_b128(rs, e0 * 4, 0, 0));
  unsigned wv8[8];
  if (rnd == 15 && c < NFRAG) {
    const int nn = ((lane >> 2) & 1) * 32 + (c & 1) * 16 + ((lane >> 3) & 3) * 4 + (lane & 3), h = lane >> 5;
    const unsigned short* p;
    if (c < 24) {
      const int m = c >> 3, t = (c >> 1) & 3;
      p = ((m == 0) ? wb : (m == 1) ? wv : wa) + (32 * h + 8 * t) * 64 + nn;
    } else if (c < 26) {
      p = wc + (8 * h) * 64 + nn;
    } else {
      p = wu + (32 * h + 8 * ((c - 26) >> 1)) * 64 + nn;
    }
#pragma unroll
    for (int j = 0; j < 8; ++j) wv8[j] = p[j * 64];
  }
#define CPN 80
  u32x4 t;
  if (rnd == 14 && c < CPN) {
    unsigned long long pc;
    int rel;
    asm volatile("s_getpc_b64 %0\n\ts_mov_b32 %1, _Z6k_mainPKtS0_PKciiPt@rel32@lo+4" : "=s"(pc), "=s"(rel));
    pc += (long long)rel;
    asm volatile("global_load_dwordx4 %0, %1, %2" : "=v"(t) : "v"(((c >> 3) % 10) * 1024 + lane * 16), "s"(pc) : "memory");
  }
  if (tid < 2 * MAXB) *reinterpret_cast<u32x2*>(cT + (tid >> 1) * CSTR + (tid & 1) * 16) = u32x2{0u, 0u};
  else if (tid < 3 * MAXB + 4) cnt[tid - 2 * MAXB] = 0;
  else if (tid < 3 * MAXB + 12) ovl[tid - (3 * MAXB + 4)] = 0x100u;
  __syncthreads();
  if ((rnd * NCH + c) * 256 < E) {
    int bk[4], rk[4];
#pragma unroll
    for (int k = 0; k < 4; ++k) {
      const bool v = e0 + k < E;
      bk[k] = v ? (int)((unsigned)dd[k] / (unsigned)NPB) : MAXB + 1;
      rk[k] = LDS_ADD(&cnt[bk[k]], 1);
    }
    unsigned w[4];
    bool ovf = false;
#pragma unroll
    for (int k = 0; k < 4; ++k) {
      const unsigned s = (unsigned)(dd[k] - bk[k] * NPB);
      w[k] = ((unsigned)ss[k] << 16) | ((s & 7u) << 13) | ((unsigned)(lane * 4 + k) << 5) | (s >> 3);
      const int d = (rk[k] + (rk[k] < CAP / 2 ? 2 : 4)) & 31;
      unsigned* cp = cT + (rk[k] < CAP ? min(bk[k], MAXB) : MAXB) * CSTR;
      cp[d] = w[k];
      __hip_atomic_fetch_or(cp + (d & 16) + ((d >> 3) & 1), (unsigned)rnd << (4 * (d & 7)),
                            __ATOMIC_RELAXED, __HIP_MEMORY_SCOPE_WORKGROUP);
      ovf |= rk[k] >= CAP;
    }
    if (__builtin_amdgcn_ballot_w64(ovf) != 0) {
#pragma unroll 1
      for (int k = 0; k < 4; ++k) {
        const int bq = k == 0 ? bk[0] : k == 1 ? bk[1] : k == 2 ? bk[2] : bk[3];
        const int rq = k == 0 ? rk[0] : k == 1 ? rk[1] : k == 2 ? rk[2] : rk[3];
        const unsigned wq = k == 0 ? w[0] : k == 1 ? w[1] : k == 2 ? w[2] : w[3];
        if (bq < MAXB && rq >= CAP) {
          const int o = LDS_ADD(&cnt[MAXB], 1);
          OV[c * OVCAP + o] = u32x4{wq >> 9, ((unsigned)(e0 + k) << 5) | (wq & 31u), (unsigned)bq, 0u};
          if (o < 4) { ovl[2 * o] = wq; ovl[2 * o + 1] = ((unsigned)rnd << 12) | (unsigned)bq; }
        }
      }
    }
  }
  asm volatile("s_waitcnt lgkmcnt(0)\n\ts_barrier" ::: "memory");
  {
    const auto rc = SRD(TC, MAXB * NCH * CELL * 4);
    const int p = tid & 7;
    const int first = (CAP / 2) * (p >> 2) + 4 * (p & 3) - 2;
#pragma unroll
    for (int q = 0; q < 2; ++q) {
      const int b = (tid >> 3) + q * 128;
      const int m = cnt[b];
      u32x4 v = *reinterpret_cast<const u32x4*>(cT + b * CSTR + 4 * p);
      if ((p & 3) == 0) v[0] |= (unsigned)min(max(m - first - 2, 0), CAP / 2);
      if (b < nb && (WALL || (p & 3) == 0 || first < m)) CST((b * NCH + c) * CELL + 4 * p, v);
    }
    if (tid < 2) {
      u32x4 v = *reinterpret_cast<const u32x4*>(ovl + 4 * tid);
      v[1] |= (unsigned)cnt[MAXB] << 16;
      v[3] |= (unsigned)cnt[MAXB] << 16;
      __builtin_amdgcn_raw_buffer_store_b128(v, SRD(OVH, NCH * 32), c * 32 + tid * 16, 0, 17);
    }
  }
  if (rnd == 15 && c < NFRAG) {
    if (c < 8 || c == 24 || c == 25) {
#pragma unroll
      for (int j = 0; j < 8; ++j) {
        const unsigned u = __float_as_uint(__uint_as_float(wv8[j] << 16) * NLOG2E);
        wv8[j] = (u + 0x7FFFu + ((u >> 16) & 1u)) >> 16;
      }
    }
    const u32x4 v = {wv8[0] | (wv8[1] << 16), wv8[2] | (wv8[3] << 16), wv8[4] | (wv8[5] << 16), wv8[6] | (wv8[7] << 16)};
    __builtin_amdgcn_raw_buffer_store_b128(v, SRD(wpack, NFRAG * 1024), c * 1024 + lane * 16, 0, 17);
  }
  asm volatile("s_waitcnt vmcnt(0)" :: "v"(t) : "memory");
}

__device__ __forceinline__ NOPK unsigned pack_bf16(float lo, float hi) {
  typedef __attribute__((ext_vector_type(2))) float f2;
  typedef __attribute__((ext_vector_type(2))) __bf16 b2;
  f2 v = {lo, hi};
  b2 r = __builtin_convertvector(v, b2);
  return __builtin_bit_cast(unsigned, r);
}
__device__ __forceinline__ NOPK unsigned pack_f16(float lo, float hi) {
  typedef __attribute__((ext_vector_type(2))) float f2;
  f2 v = {lo, hi};
  f16x2 r = __builtin_convertvector(v, f16x2);
  return __builtin_bit_cast(unsigned, r);
}
#define WGSYNC() do { __builtin_amdgcn_fence(__ATOMIC_RELEASE, "workgroup"); __builtin_amdgcn_s_barrier(); \
                      __builtin_amdgcn_fence(__ATOMIC_ACQUIRE, "workgroup"); } while (0)
#define U2F(u) __builtin_bit_cast(float, (unsigned)(u))
constexpr int XB = 1040;
constexpr int RING_SLOT = 4 * XB + 1024;
constexpr int LCAP = 512;
constexpr int LPAD = LCAP + 64;
constexpr int SSCR = 32 * 80;
constexpr int WAVE_LDS = RING_SLOT;
constexpr int M_WL = 0;
constexpr int M_OH = 16 * 1024;
constexpr int M_CNT = M_OH + 2048;
constexpr int M_ST = M_CNT + 256;
constexpr int M_SC = M_ST + 2048;
constexpr int M_LX = M_SC + GPB * SSCR;
constexpr int M_LE = M_LX + GPB * LPAD * 4;
constexpr int M_WV = M_LE + GPB * LPAD * 4;
constexpr int M_XG = M_WV + GPB * WAVE_LDS;
constexpr int MAIN_LDS = M_XG + GPB * 4 * XB;
static_assert(M_LX % 256 == 0 && M_LE % 256 == 0 && M_LE < 65536 && M_LE + GPB * LPAD * 4 + NCH * CELL * 4 <= M_XG, "lists");
static_assert(MAIN_LDS <= 160 * 1024, "LDS");

__global__ __launch_bounds__(512, 2) NOPK void k_main(const unsigned short* __restrict__ x,
                                                      const unsigned short* __restrict__ eattr,
                                                      const char* __restrict__ wsp, int E,
                                                      int N, unsigned short* __restrict__ out) {
  __shared__ __attribute__((aligned(16))) char smem[MAIN_LDS];
  asm volatile("" :: "s"(x), "s"(eattr), "s"(wsp), "s"(E), "s"(N), "s"(out));
  const unsigned* TC = reinterpret_cast<const unsigned*>(wsp + W_TC);
  const u32x4* OVH = reinterpret_cast<const u32x4*>(wsp + W_OVH);
  const u32x4* OV = reinterpret_cast<const u32x4*>(wsp + W_OV);
  const char* wpack = wsp + W_WP;
  const int tid = threadIdx.x, lane = tid & 63;
  const int wave = __builtin_amdgcn_readfirstlane(tid >> 6);
  const int r = lane & 31, h = lane >> 5;
  const int b = blockIdx.x;

  const int pc = lane & 7;
  u32x4 tv[4], ovh;
  {
    const unsigned* cp = TC + ((size_t)b * NCH + wave * 32) * CELL;
    asm volatile("global_load_dwordx4 %0, %5, %6\n\tglobal_load_dwordx4 %1, %5, %6 offset:1024\n\t"
                 "global_load_dwordx4 %2, %5, %6 offset:2048\n\tglobal_load_dwordx4 %3, %5, %6 offset:3072\n\t"
                 "global_load_dwordx4 %4, %5, %7"
                 : "=&v"(tv[0]), "=&v"(tv[1]), "=&v"(tv[2]), "=&v"(tv[3]), "=&v"(ovh)
                 : "v"(lane * 16), "s"(cp), "s"(OVH + wave * 64) : "memory");
  }
#define GLDS(vo, rs, mv) asm volatile("s_mov_b32 m0, %2\n\ts_nop 0\n\tbuffer_load_dwordx4 %0, %1, 0 offen lds" \
                                      :: "v"(vo), "s"(rs), "s"(mv) : "memory")
  const auto rx = SRD(x, N * 128);
  const auto re = SRD(eattr, E * 32);
  const unsigned xc0 = (unsigned)(((lane & 7) ^ ((lane >> 2) & 4)) * 16);
  const unsigned lb = (unsigned)(size_t)(LAS char*)smem;
  LAS int* cnt = (LAS int*)(smem + M_CNT);
  if (tid < 32) cnt[tid] = (tid & 3) == 0 ? (tid >> 2) * (LPAD * 4) : (tid & 3) == 2 ? 127 : 0;
  __builtin_amdgcn_s_setprio(1);
  {
    unsigned* oh = reinterpret_cast<unsigned*>(smem + M_OH);
    const int i = tid;
    const int sl = i >> 4, hh = (i >> 3) & 1, ks = (i >> 2) & 1, jp = i & 3;
    const int j0 = 2 * jp, j1 = 2 * jp + 1;
    const int nd0 = 16 * ks + 8 * (j0 >> 2) + 4 * hh + (j0 & 3);
    const int nd1 = 16 * ks + 8 * (j1 >> 2) + 4 * hh + (j1 & 3);
    oh[i] = (sl == nd0 || nd0 == 30 ? 0x3F80u : 0u) | (sl == nd1 ? 0x3F800000u : 0u);
  }
  asm volatile("s_waitcnt lgkmcnt(0)\n\ts_barrier" ::: "memory");
  {
    const int nw = (min(NPB - wave, N - b * NPB - wave) + 7) >> 3;
#pragma unroll
    for (int k = 0; k < 4; ++k) {
      const int s = (lane >> 3) * 4 + k;
      const unsigned xo = s < nw ? (unsigned)(b * NPB + wave + 8 * s) << 7 : 0xFFFFFF80u;
      GLDS(xo | xc0, rx, lb + M_XG + wave * (4 * XB) + k * XB);
    }
  }
  const LAS bf16x8* wl = (const LAS bf16x8*)(smem + M_WL);
  const LAS bf16x8* ohl = (const LAS bf16x8*)(smem + M_OH);
  LAS unsigned* lxa = (LAS unsigned*)(smem + M_LX);
  LAS unsigned* lea = (LAS unsigned*)(smem + M_LE);
  char* wbase = smem + M_WV + wave * WAVE_LDS;
  const unsigned ring_a = (unsigned)(size_t)(LAS char*)wbase;
  {
    unsigned long long vm[16];
    unsigned ad[16], xe[16], hw[4];
    const unsigned four = 4u;
#pragma unroll
    for (int k = 0; k < 4; ++k) {
      asm volatile("s_waitcnt vmcnt(%1)" : "+v"(tv[k]) : "n"(8 - k) : "memory");
      const unsigned h0 = (unsigned)__builtin_amdgcn_mov_dpp((int)tv[k][0], 0x00, 0xF, 0xF, true);
      const unsigned h1 = (unsigned)__builtin_amdgcn_mov_dpp((int)tv[k][1], 0x00, 0xF, 0xF, true);
      hw[k] = ((lane & 2) ? h1 : h0) >> ((lane & 1) * 16);
      const unsigned m = h0 & 0xFFu;
      unsigned ci[4];
#pragma unroll
      for (int j = 0; j < 4; ++j) {
        vm[4 * k + j] = __builtin_amdgcn_ballot_w64((unsigned)((lane & 3) * 4 + j - 2) < m);
        xe[4 * k + j] = tv[k][j] >> 9;
        ci[j] = (xe[4 * k + j] & 0x70u) + lb;
      }
      asm volatile("s_mov_b64 exec, %4\n\tds_add_rtn_u32 %0, %8, %12 offset:%13\n\t"
                   "s_mov_b64 exec, %5\n\tds_add_rtn_u32 %1, %9, %12 offset:%13\n\t"
                   "s_mov_b64 exec, %6\n\tds_add_rtn_u32 %2, %10, %12 offset:%13\n\t"
                   "s_mov_b64 exec, %7\n\tds_add_rtn_u32 %3, %11, %12 offset:%13\n\t"
                   "s_mov_b64 exec, -1"
                   : "=&v"(ad[4 * k]), "=&v"(ad[4 * k + 1]), "=&v"(ad[4 * k + 2]), "=&v"(ad[4 * k + 3])
                   : "s"(vm[4 * k]), "s"(vm[4 * k + 1]), "s"(vm[4 * k + 2]), "s"(vm[4 * k + 3]),
                     "v"(ci[0]), "v"(ci[1]), "v"(ci[2]), "v"(ci[3]), "v"(four), "n"(M_CNT)
                   : "memory");
    }
#pragma unroll
    for (int k = 0; k < 4; ++k) {
      const unsigned c13 = (unsigned)(wave * 32 + k * 8 + (lane >> 3)) << 13;
      unsigned ee[4];
#pragma unroll
      for (int j = 0; j < 4; ++j) ee[j] = (tv[k][j] & 0x1FFFu) | ((((hw[k] >> (4 * j)) & 15u) << 21) | c13);
      asm volatile("s_waitcnt lgkmcnt(12)\n\t"
                   "s_mov_b64 exec, %0\n\tds_write2st64_b32 %4, %8, %12 offset0:%16 offset1:%17\n\t"
                   "s_mov_b64 exec, %1\n\tds_write2st64_b32 %5, %9, %13 offset0:%16 offset1:%17\n\t"
                   "s_mov_b64 exec, %2\n\tds_write2st64_b32 %6, %10, %14 offset0:%16 offset1:%17\n\t"
                   "s_mov_b64 exec, %3\n\tds_write2st64_b32 %7, %11, %15 offset0:%16 offset1:%17\n\t"
                   "s_mov_b64 exec, -1"
                   :: "s"(vm[4 * k]), "s"(vm[4 * k + 1]), "s"(vm[4 * k + 2]), "s"(vm[4 * k + 3]),
                      "v"(ad[4 * k]), "v"(ad[4 * k + 1]), "v"(ad[4 * k + 2]), "v"(ad[4 * k + 3]),
                      "v"(xe[4 * k]), "v"(xe[4 * k + 1]), "v"(xe[4 * k + 2]), "v"(xe[4 * k + 3]),
                      "v"(ee[0]), "v"(ee[1]), "v"(ee[2]), "v"(ee[3]), "n"(M_LX / 256), "n"(M_LE / 256)
                   : "memory");
    }
  }
    {
      const auto rw = SRD(wpack, NFRAG * 1024);
      const unsigned vo = (unsigned)(wave * 1024 + lane * 16);
      GLDS(vo + 16384u, rw, lb + M_SC + 8192 + wave * 1024);
      GLDS(vo, rw, lb + M_SC + wave * 1024);
      asm volatile("s_mov_b32 m0, %2\n\ts_nop 0\n\tbuffer_load_dword %0, %1, 0 offen lds"
                   :: "v"(24576u + (vo >> 2)), "s"(rw), "s"(lb + M_SC + 16384 + wave * 256) : "memory");
      GLDS(vo + 8192u, rw, lb + M_WL + wave * 1024);
      GLDS(vo + 26624u, rw, lb + M_WL + 8192 + wave * 1024);
    }
  asm volatile("s_waitcnt vmcnt(9)" : "+v"(ovh) :: "memory");
  if (__builtin_amdgcn_ballot_w64((ovh[1] & 0x100u) == 0u) != 0) {
#pragma unroll
    for (int q = 0; q < 2; ++q) {
      const unsigned w = ovh[2 * q], m = ovh[2 * q + 1];
      if ((m & 0x1FFu) == (unsigned)b) {
        LAS unsigned* p = (LAS unsigned*)((LAS char*)lxa + LDS_ADD((LAS int*)((LAS char*)cnt + ((w >> 9) & 0x70u)), 4));
        p[0] = w >> 9;
        p[GPB * LPAD] = (w & 0x1FFFu) | ((m & 0xF000u) << 9) | ((unsigned)(tid >> 1) << 13);
      }
    }
    if ((ovh[1] >> 16) > 4u) cnt[1] = 1;
  }
  asm volatile("s_waitcnt lgkmcnt(0)\n\ts_barrier" ::: "memory");
  const int cpc = cnt[4 * pc] - pc * (LPAD * 4);
  const int cpa = cnt[4 * (lane >> 3)] - (lane >> 3) * (LPAD * 4);
  int mo0 = cnt[1];
  bool ready = __builtin_amdgcn_ballot_w64(cpc > LCAP * 4) == 0;
  int grp;
  {
    const unsigned long long gt = __builtin_amdgcn_ballot_w64(cpc * 2 + pc > cpa * 2 + (lane >> 3));
    const unsigned wd = h ? (unsigned)(gt >> 32) : (unsigned)gt;
    const int rk = __builtin_popcount((wd >> (r & 24)) & 0xFFu);
    grp = __builtin_ctzll(__builtin_amdgcn_ballot_w64(rk == (wave < 4 ? wave : 11 - wave))) >> 3;
  }
  asm volatile("" : "+v"(mo0));
  const int gn0 = b * NPB + grp;
  const int nodes = max((min(NPB - grp, N - gn0) + 7) >> 3, 0);
  const int n0 = min(gn0, N - 1);
  ((LAS unsigned*)(smem + M_ST))[tid] = (ready ? 0x40000u : 0xFFFFu) | ((unsigned)(tid & 1) << 16);

  f32x16 Y[4];
#pragma unroll
  for (int q = 0; q < 4; ++q)
#pragma unroll
    for (int i = 0; i < 16; ++i) Y[q][i] = q < 2 ? 1e-6f * 65536.f : 0.f;

  const unsigned ec0 = (unsigned)(((lane & 1) ^ ((lane >> 4) & 1)) * 16);
  const LAS unsigned* lx = lxa + grp * LPAD;
  const LAS unsigned* le = lea + grp * LPAD;
  const int ohoff = ((((r >> 2) & 1) * 16) + ((r >> 4) * 8) + (((r >> 3) & 1) * 4) + (r & 3)) * 2;
  const int xa0 = (r & 3) * XB + (r >> 2) * 128 + (h ^ ((r >> 3) & 1)) * 64;
  const int ea0 = 4 * XB + r * 32 + ((h ^ ((r >> 3) & 1)) * 16);
#define SB() __builtin_amdgcn_sched_barrier(0)
  bf16x8 wB[4][2], wC[2], XAf[2][2];
  const LAS bf16x8* wvl = wl + lane;
  f32x16 G0, G1, V;
  u32x4 pd[2], pn[2];
  bf16x8 S[2], St[2], as[4], ae, bv[4];
  const f32x16 zero16 = {0.f, 0.f, 0.f, 0.f, 0.f, 0.f, 0.f, 0.f, 0.f, 0.f, 0.f, 0.f, 0.f, 0.f, 0.f, 0.f};
  LAS char* sm = (LAS char*)(smem + M_SC + wave * SSCR);
  LAS u32x4* mine = (LAS u32x4*)(sm + r * 80 + h * 32);
  const LAS char* ring = (const LAS char*)wbase;
  unsigned ga[5];
  unsigned slr;
  int sl;

#define E1(i) Gc[i] = __builtin_amdgcn_exp2f(Gc[i])
#define PD(j) pd[(j) >> 2][(j) & 3]
#define CV(j) PD(j) = pack_f16(Gc[2 * (j)], Gc[2 * (j) + 1])
#define PA(j) PD(j) = __builtin_bit_cast(unsigned, __builtin_bit_cast(f16x2, (unsigned)PD(j)) + __builtin_bit_cast(f16x2, one15))
#define RL(j) do { unsigned t_ = PD(j); asm volatile("v_rcp_f16_sdwa %0, %0 dst_sel:WORD_0 dst_unused:UNUSED_PRESERVE src0_sel:WORD_0" : "+v"(t_)); PD(j) = t_; } while (0)
#define RH(j) do { unsigned t_ = PD(j); asm volatile("v_rcp_f16_sdwa %0, %0 dst_sel:WORD_1 dst_unused:UNUSED_PRESERVE src0_sel:WORD_1" : "+v"(t_)); PD(j) = t_; } while (0)
#define MX(i) V[i] = __builtin_fmaf(V[i], (float)__builtin_bit_cast(f16x2, (unsigned)PD((i) >> 1))[(i) & 1], 0.0f)
#define HC(v) __builtin_bit_cast(f16x8, v)
#define CN(j) pn[(j) >> 2][(j) & 3] = pack_bf16(V[2 * (j)], V[2 * (j) + 1])
#define XG(k) ((xo[k] & ~127u) | xc0)
#define BC(v) __builtin_bit_cast(bf16x8, v)
#define PIN(v) asm volatile("" : "+v"(v))

  const unsigned one15 = 0x02000200u;
  int more, ntiles;
  LAS int* pown = cnt + 4 * wave + 2;
  const LAS int* poth = cnt + 4 * (wave ^ 4) + 2;
  PIN(pown); PIN(poth);
  const LAS unsigned* lxp = lx + (lane >> 3) * 4;
  const LAS unsigned* lep = le + (lane >> 1);
  const LAS unsigned* lsp = le + r;
  auto round_begin = [&](int cb, int mo) NOPK __attribute__((always_inline)) {
    const int cn = min(cb >> 2, LCAP);
    more = __builtin_amdgcn_readfirstlane(mo);
    ntiles = (cn + 31) >> 5;
    {
      LAS unsigned* wx = lxa + grp * LPAD;
      LAS unsigned* we = lea + grp * LPAD;
      const int e0 = ntiles * 32;
      if (cn + r < e0) { wx[cn + r] = 0xFFFFFF80u; we[cn + r] = 0xFFFFFFFFu; }
      wx[e0 + lane] = 0xFFFFFF80u;
      we[e0 + lane] = 0xFFFFFFFFu;
    }
  };
  auto gather = [&](int t, bool issue) NOPK __attribute__((always_inline)) {
    const u32x4 xo = *(const LAS u32x4*)(lxp + t * 32);
    const unsigned eo = lep[t * 32];
    ga[0] = XG(0); ga[1] = XG(1); ga[2] = XG(2); ga[3] = XG(3);
    ga[4] = (eo & ~31u) | ec0;
    if (issue) {
      GLDS(ga[0], rx, ring_a); GLDS(ga[1], rx, ring_a + XB); GLDS(ga[2], rx, ring_a + 2 * XB);
      GLDS(ga[3], rx, ring_a + 3 * XB); GLDS(ga[4], re, ring_a + 4 * XB);
    }
  };
  if (ready) {
    round_begin(__builtin_amdgcn_readlane(cpc, grp), mo0);
    if (wave < 4) gather(0, true);
  }
  asm volatile("s_cmp_lg_u32 %0, 0\n\ts_cbranch_scc1 1f\n\ts_waitcnt vmcnt(0)\n1:\n\ts_waitcnt vmcnt(5)\n\ts_barrier"
               :: "s"(__builtin_amdgcn_readfirstlane((ready && wave < 4) ? 1 : 0)) : "memory", "scc");
  {
      const LAS bf16x8* ws = (const LAS bf16x8*)(smem + M_SC) + lane;
      bf16x8 xg[4], wA[8];
#pragma unroll
      for (int k = 0; k < 4; ++k) xg[k] = *(const LAS bf16x8*)(smem + M_XG + grp * (4 * XB) + xa0 + 16 * k);
#pragma unroll
      for (int k = 0; k < 8; ++k) wA[k] = ws[(8 + k) * 64];
#pragma unroll
      for (int k = 0; k < 8; ++k) wB[k >> 1][k & 1] = ws[k * 64];
      wC[0] = ws[16 * 64];
      wC[1] = ws[17 * 64];
      SB();
      asm volatile("s_waitcnt lgkmcnt(0)\n\ts_barrier"
                   : "+v"(wB[0][0]), "+v"(wB[0][1]), "+v"(wB[1][0]), "+v"(wB[1][1]), "+v"(wB[2][0]), "+v"(wB[2][1]),
                     "+v"(wB[3][0]), "+v"(wB[3][1]), "+v"(wC[0]), "+v"(wC[1]),
                     "+v"(xg[0]), "+v"(xg[1]), "+v"(xg[2]), "+v"(xg[3]), "+v"(wA[0]), "+v"(wA[1]), "+v"(wA[2]),
                     "+v"(wA[3]), "+v"(wA[4]), "+v"(wA[5]), "+v"(wA[6]), "+v"(wA[7]) :: "memory");
      SB();
      if (ready) {
        if (wave >= 4) gather(0, true);

      }
      SB();
#pragma unroll
      for (int ct = 0; ct < 2; ++ct) {
        f32x16 A = zero16;
#pragma unroll
        for (int k = 0; k < 4; ++k) A = MFMA32(xg[k], wA[k * 2 + ct], A);
#pragma unroll
        for (int s = 0; s < 2; ++s) {
          u32x4 p;
#pragma unroll
          for (int j = 0; j < 4; ++j) p[j] = pack_bf16(A[8 * s + 2 * j] * NLOG2E, A[8 * s + 2 * j + 1] * NLOG2E);
          if (s == 1 && h) p[3] = 0xC170u;
          XAf[s][ct] = BC(p);
        }
      }
      const u32x4 z4 = {0u, 0u, 0u, 0u};
      mine[0] = z4;
      mine[1] = z4;
      SB();
    }
  for (;;) {
   if (!ready) {
    asm volatile("s_waitcnt vmcnt(0)" ::: "memory");
    WGSYNC();
    {
      int t2 = (int)__builtin_amdgcn_mbcnt_hi(~0u, __builtin_amdgcn_mbcnt_lo(~0u, 0u));
      asm volatile("" : "+v"(t2));
      const int l2 = t2;
      t2 += wave * 64;
      if (t2 < 32) cnt[t2] = (t2 & 3) == 0 ? (t2 >> 2) * (LPAD * 4) : 0;
      WGSYNC();
      auto put = [&](unsigned t, unsigned e) NOPK -> bool {
        const unsigned gg = (t >> 4) & 7u;
        const int ad = LDS_ADD(&cnt[4 * gg], 4) >> 2;
        if (ad - (int)gg * LPAD >= LCAP) return false;
        lxa[ad] = t;
        lea[ad] = e;
        return true;
      };
      LAS unsigned* stp = (LAS unsigned*)(smem + M_ST) + t2;
      unsigned pe = *stp & 0xFFFFu, ocur = *stp >> 16;
#pragma unroll 1
      for (int j = 0; j < 16; ++j) {
        if ((pe >> j) & 1u) {
          const int cc = wave * 32 + (j >> 2) * 8 + (l2 >> 3);
          const unsigned* cp = TC + ((size_t)b * NCH + cc) * CELL;
          const int d = (l2 & 7) * 4 + (j & 3);
          const unsigned w = cp[d];
          const unsigned nib = (cp[(d & 16) + ((d >> 3) & 1)] >> (4 * (d & 7))) & 15u;
          if ((unsigned)((d & 15) - 2) >= (cp[d & 16] & 0xFFu) ||
              put(w >> 9, (w & 0x1FFFu) | (nib << 21) | ((unsigned)cc << 13))) pe &= ~(1u << j);
        }
      }
      const unsigned on = OVH[t2][1] >> 16;
      const size_t ob = (size_t)(t2 >> 1) * OVCAP;
#pragma unroll 1
      for (; ocur < on; ocur += 2) {
        const u32x4 v = OV[ob + ocur];
        if ((int)v[2] == b && !put(v[0], v[1])) break;
      }
      if (pe != 0u || ocur < on) cnt[1] = 1;
      *stp = pe | (ocur << 16);
    }
    WGSYNC();
    round_begin(__builtin_amdgcn_readfirstlane(cnt[4 * grp]) - grp * (LPAD * 4), cnt[1]);
    gather(0, true);
   }
    gather(1, false);
    ready = false;
    sl = lsp[0] & 31u;
    St[0] = ohl[sl * 4 + h * 2 + 0];
    St[1] = ohl[sl * 4 + h * 2 + 1];
    bv[0] = wvl[0 * 64];
    bv[1] = wvl[2 * 64];
    asm volatile("s_waitcnt vmcnt(0)" ::: "memory");
#pragma unroll
    for (int k = 0; k < 4; ++k) as[k] = *(const LAS bf16x8*)(ring + xa0 + 16 * k);
    ae = *(const LAS bf16x8*)(ring + ea0);
    G0 = MFMA32(St[0], XAf[0][0], zero16);
    G0 = MFMA32(St[1], XAf[1][0], G0);
    SB();
    asm volatile("" : "+v"(as[0]), "+v"(as[1]), "+v"(as[2]), "+v"(as[3]), "+v"(ae) :: "memory");
    G0 = MFMA32(ae, wC[0], G0);
    GLDS(ga[0], rx, ring_a);
    SB();
    G0 = MFMA32(as[0], wB[0][0], G0);
    GLDS(ga[1], rx, ring_a + XB);
    SB();
    G0 = MFMA32(as[1], wB[1][0], G0);
    GLDS(ga[2], rx, ring_a + 2 * XB);
    SB();
    G0 = MFMA32(as[2], wB[2][0], G0);
    GLDS(ga[3], rx, ring_a + 3 * XB);
    SB();
    G0 = MFMA32(as[3], wB[3][0], G0);
    GLDS(ga[4], re, ring_a + 4 * XB);
    pd[1] = u32x4{0u, 0u, 0u, 0u};
    pn[1] = u32x4{0u, 0u, 0u, 0u};
    S[1] = BC(pd[1]);
    SB();
    for (int i = 0; i < ntiles; ++i) {
      {
        f32x16& Gc = G0;
        LAS unsigned short* ohw = (LAS unsigned short*)(sm + sl * 80 + ohoff);
        Y[1] = MFMA32H(HC(pd[1]), HC(S[1]), Y[1]);
        bv[2] = wvl[4 * 64];
        *(volatile LAS int*)pown = ntiles - i;
        E1(0); E1(1); E1(2); E1(3);
        SB();
        Y[3] = MFMA32(BC(pn[1]), S[1], Y[3]);
        bv[3] = wvl[6 * 64];
        E1(4); CV(0); E1(5); CV(1); PA(0);
        SB();
        V = MFMA32(as[0], bv[0], zero16);
        const int prem = *(volatile const LAS int*)poth;
        E1(6); PA(1); RL(0); E1(7); CV(2);
        SB();
        V = MFMA32(as[1], bv[1], V);
        RH(0); E1(8); PA(2); E1(9); CV(3);
        SB();
        V = MFMA32(as[2], bv[2], V);
        RL(1); E1(10); PA(3); E1(11); CV(4);
        SB();
        V = MFMA32(as[3], bv[3], V);
        RH(1); E1(12); PA(4); E1(13); CV(5);
        SB();
        G1 = MFMA32(St[0], XAf[0][1], zero16);
        asm volatile("s_cmp_ge_i32 %0, %1\n\ts_setprio 0\n\ts_cbranch_scc0 1f\n\ts_setprio 1\n1:"
                     :: "s"(ntiles - i), "s"(__builtin_amdgcn_readfirstlane(prem)) : "scc");
        RL(2); E1(14); PA(5); E1(15); CV(6);
        SB();
        G1 = MFMA32(St[1], XAf[1][1], G1);
        *ohw = (unsigned short)0x4000;
        asm volatile("" ::: "memory");
        S[0] = BC(mine[0]);
        S[1] = BC(mine[1]);
        asm volatile("" ::: "memory");
        *ohw = (unsigned short)0;
        RH(2); PA(6); RL(3); CV(7);
        SB();
        G1 = MFMA32(ae, wC[1], G1);
        RH(3); PA(7); MX(0); RL(4); MX(1); MX(2);
        SB();
        G1 = MFMA32(as[0], wB[0][1], G1);
        RH(4); MX(3); RL(5); CN(0); MX(4);
        SB();
        G1 = MFMA32(as[1], wB[1][1], G1);
        RH(5); MX(5); RL(6); CN(1); MX(6);
        SB();
        G1 = MFMA32(as[2], wB[2][1], G1);
        RH(6); MX(7); RL(7); CN(2); MX(8);
        SB();
        G1 = MFMA32(as[3], wB[3][1], G1);
        slr = lsp[(i + 1) * 32];
        RH(7); MX(9); CN(3); MX(10); MX(11); CN(4);
        SB();
        Y[0] = MFMA32H(HC(pd[0]), HC(S[0]), Y[0]);
        bv[0] = wvl[1 * 64];
        MX(12); MX(13); CN(5); MX(14); MX(15);
        SB();
        Y[2] = MFMA32(BC(pn[0]), S[0], Y[2]);
        bv[1] = wvl[3 * 64];
        CN(6); CN(7);
        SB();
      }
      {
        f32x16& Gc = G1;
        sl = slr & 31u;
        Y[0] = MFMA32H(HC(pd[1]), HC(S[1]), Y[0]);
        bv[2] = wvl[5 * 64];
        St[0] = ohl[sl * 4 + h * 2 + 0];
        E1(0); E1(1); E1(2); E1(3);
        SB();
        Y[2] = MFMA32(BC(pn[1]), S[1], Y[2]);
        bv[3] = wvl[7 * 64];
        St[1] = ohl[sl * 4 + h * 2 + 1];
        E1(4); CV(0); E1(5); CV(1); PA(0);
        SB();
        V = MFMA32(as[0], bv[0], zero16);
        const u32x4 xo = *(const LAS u32x4*)(lxp + (i + 2) * 32);
        const unsigned eo = lep[(i + 2) * 32];
        E1(6); PA(1); RL(0); E1(7); CV(2);
        SB();
        V = MFMA32(as[1], bv[1], V);
        RH(0); E1(8); PA(2); E1(9); CV(3);
        SB();
        V = MFMA32(as[2], bv[2], V);
        RL(1); E1(10); PA(3); E1(11); CV(4);
        SB();
        V = MFMA32(as[3], bv[3], V);
        RH(1); E1(12); PA(4); E1(13); CV(5);
        SB();
        G0 = MFMA32(St[0], XAf[0][0], zero16);
        asm volatile("s_waitcnt vmcnt(0)" ::: "memory");
        ae = *(const LAS bf16x8*)(ring + ea0);
        as[0] = *(const LAS bf16x8*)(ring + xa0);
        as[1] = *(const LAS bf16x8*)(ring + xa0 + 16);
        as[2] = *(const LAS bf16x8*)(ring + xa0 + 32);
        as[3] = *(const LAS bf16x8*)(ring + xa0 + 48);
        RL(2); E1(14); PA(5); E1(15); CV(6);
        SB();
        G0 = MFMA32(St[1], XAf[1][0], G0);
        ga[0] = XG(0);
        PIN(ga[0]);
        RH(2); PA(6); RL(3); CV(7);
        SB();
        asm volatile("" : "+v"(as[0]), "+v"(as[1]), "+v"(as[2]), "+v"(as[3]), "+v"(ae) :: "memory");
        G0 = MFMA32(ae, wC[0], G0);
        GLDS(ga[0], rx, ring_a);
        ga[1] = XG(1);
        PIN(ga[1]);
        RH(3); PA(7); MX(0); RL(4); MX(1); MX(2);
        SB();
        G0 = MFMA32(as[0], wB[0][0], G0);
        GLDS(ga[1], rx, ring_a + XB);
        ga[2] = XG(2);
        PIN(ga[2]);
        RH(4); MX(3); RL(5); CN(0); MX(4);
        SB();
        G0 = MFMA32(as[1], wB[1][0], G0);
        GLDS(ga[2], rx, ring_a + 2 * XB);
        ga[3] = XG(3);
        PIN(ga[3]);
        RH(5); MX(5); RL(6); CN(1); MX(6);
        SB();
        G0 = MFMA32(as[2], wB[2][0], G0);
        GLDS(ga[3], rx, ring_a + 3 * XB);
        ga[4] = (eo & ~31u) | ec0;
        PIN(ga[4]);
        RH(6); MX(7); RL(7); CN(2); MX(8);
        SB();
        G0 = MFMA32(as[3], wB[3][0], G0);
        GLDS(ga[4], re, ring_a + 4 * XB);
        RH(7); MX(9); CN(3); MX(10); MX(11); CN(4);
        SB();
        Y[1] = MFMA32H(HC(pd[0]), HC(S[0]), Y[1]);
        bv[0] = wvl[0 * 64];
        MX(12); MX(13); CN(5); MX(14); MX(15);
        SB();
        Y[3] = MFMA32(BC(pn[0]), S[0], Y[3]);
        bv[1] = wvl[2 * 64];
        CN(6); CN(7);
        SB();
      }
    }
    *(volatile LAS int*)pown = 0;
    Y[1] = MFMA32H(HC(pd[1]), HC(S[1]), Y[1]);
    Y[3] = MFMA32(BC(pn[1]), S[1], Y[3]);
    if (!more) break;
  }
  {
    bf16x8 fa[2][6];
#pragma unroll
    for (int k = 0; k < 4; ++k) as[k] = *(const LAS bf16x8*)(smem + M_XG + grp * (4 * XB) + xa0 + 16 * k);
    fa[0][0] = fa[1][0] = ohl[r * 4 + h * 2];
    {
      u32x4 t = __builtin_bit_cast(u32x4, ohl[r * 4 + h * 2 + 1]);
      if (h && r != 30) t[3] &= 0xFFFF0000u;
      fa[0][1] = fa[1][1] = BC(t);
    }
#pragma unroll
    for (int k = 0; k < 8; ++k) fa[k & 1][2 + (k >> 1)] = wvl[(8 + k) * 64];
    LAS char* zw = (LAS char*)wbase + r * 144 + h * 64;
    u32x4 o;
#define FQ(ct, i) do { float t_ = Y[2 + (ct)][i] * __builtin_amdgcn_rcpf(Y[ct][i]); PIN(t_); Y[2 + (ct)][i] = t_; } while (0)
#define FM(ct, m) Y[2 + (ct)] = MFMA32(fa[ct][m], as[(m) < 2 ? 2 * (ct) + (m) : (m) - 2], Y[2 + (ct)])
#define FO(q, j) do { const unsigned w = __builtin_bit_cast(u32x4, as[q])[j];                              \
      float z0 = Y[2 + ((q) >> 1)][((q) & 1) * 8 + 2 * (j)], z1 = Y[2 + ((q) >> 1)][((q) & 1) * 8 + 2 * (j) + 1]; \
      o[j] = pack_bf16(__builtin_amdgcn_fmed3f(z0, U2F(w << 16), 3.4028234e38f),                            \
                       __builtin_amdgcn_fmed3f(z1, U2F(w & 0xffff0000u), 3.4028234e38f));                                                                            \
      if ((j) == 3) *(LAS u32x4*)(zw + (q) * 16) = o; } while (0)
#pragma unroll
    for (int i = 0; i < 16; ++i) FQ(0, i);
    SB();
#pragma unroll
    for (int m = 0; m < 6; ++m) {
      FM(0, m);
#pragma unroll
      for (int i = 3 * m; i < 3 * m + 3 && i < 16; ++i) FQ(1, i);
      SB();
    }
    asm volatile("s_waitcnt vmcnt(0)" ::: "memory");
    FM(1, 0); SB();
    FM(1, 1); FO(0, 0); SB();
    FM(1, 2); FO(0, 1); FO(0, 2); SB();
    FM(1, 3); FO(0, 3); FO(1, 0); SB();
    FM(1, 4); FO(1, 1); FO(1, 2); SB();
    FM(1, 5); FO(1, 3); SB();
    FO(2, 0); FO(2, 1); FO(2, 2); FO(2, 3); FO(3, 0); FO(3, 1); FO(3, 2); FO(3, 3);
  }
  const LAS char* zl = (const LAS char*)wbase;
  u32x4 o[4];
#pragma unroll
  for (int q = 0; q < 4; ++q) o[q] = *(const LAS u32x4*)(zl + (q * 8 + (lane >> 3)) * 144 + (lane & 7) * 16);
#pragma unroll
  for (int q = 0; q < 4; ++q) {
    const int row = q * 8 + (lane >> 3);
    __builtin_amdgcn_raw_buffer_store_b128(o[q], SRD(out, N * 128),
                                           row < nodes ? (n0 + 8 * row) * 128 + (lane & 7) * 16 : 0x7FFFFFF0, 0, 17);
  }
}

extern "C" void kernel_launch(void* const* d_in, const int* in_sizes, int n_in,
                              void* d_out, int out_size, void* d_ws, size_t ws_size,
                              hipStream_t stream) {
  const unsigned short* x = (const unsigned short*)d_in[0];
  const unsigned short* eattr = (const unsigned short*)d_in[1];
  const int* src = (const int*)d_in[2];
  const int* dst = (const int*)d_in[3];
  const unsigned short* wa = (const unsigned short*)d_in[4];
  const unsigned short* wb = (const unsigned short*)d_in[5];
  const unsigned short* wc = (const unsigned short*)d_in[6];
  const unsigned short* wu = (const unsigned short*)d_in[7];
  const unsigned short* wv = (const unsigned short*)d_in[8];
  unsigned short* out = (unsigned short*)d_out;
  const int N = in_sizes[0] / 64;
  const int E = in_sizes[2];
  const int nb = (N + NPB - 1) / NPB;

  char* ws = (char*)d_ws;
  k_part<<<NCH, 1024, 0, stream>>>(src, dst, E, nb, (unsigned*)(ws + W_TC), (u32x4*)(ws + W_OVH), (u32x4*)(ws + W_OV),
                                   wa, wb, wc, wu, wv, (unsigned short*)(ws + W_WP));
  k_main<<<nb, 512, 0, stream>>>(x, eattr, ws, E, N, out);
}
```

```cpp
#include <hip/hip_runtime.h>
#include <hip/hip_bf16.h>
#include <stdint.h>

using bf16x8 = __attribute__((ext_vector_type(8))) short;
using f16x8 = __attribute__((ext_vector_type(8))) _Float16;
using f16x2 = __attribute__((ext_vector_type(2))) _Float16;
using f32x16 = __attribute__((ext_vector_type(16))) float;
using u32x4 = __attribute__((ext_vector_type(4))) unsigned;
using u32x2 = __attribute__((ext_vector_type(2))) unsigned;
using i32x4 = __attribute__((ext_vector_type(4))) int;
using f32x4 = __attribute__((ext_vector_type(4))) float;

#define MFMA32(a, b, c) __builtin_amdgcn_mfma_f32_32x32x16_bf16((a), (b), (c), 0, 0, 0)
#define MFMA32H(a, b, c) __builtin_amdgcn_mfma_f32_32x32x16_f16((a), (b), (c), 0, 0, 0)
#define LAS __attribute__((address_space(3)))
#if defined(__HIP_DEVICE_COMPILE__)
#define NOPK __attribute__((target("no-packed-fp32-ops")))
#else
#define NOPK
#endif
#define SRD(p, bytes) __builtin_amdgcn_make_buffer_rsrc((void*)(p), (short)0, (int)(bytes), 0x00020000)
#define LDS_ADD(p, v) __hip_atomic_fetch_add((p), (v), __ATOMIC_RELAXED, __HIP_MEMORY_SCOPE_WORKGROUP)

#define CST(p, v) __builtin_amdgcn_raw_buffer_store_b128((v), rc, (p) * 4, 0, 17)
#define WALL 1
constexpr int GPB = 8;
constexpr int NPB = 196;
constexpr int MAXB = 256;
constexpr int NCH = 256;
constexpr int CELL = 32;
constexpr int CAP = 28;
constexpr int CSTR = 36;
constexpr int OVCAP = 4096;
constexpr int NFRAG = 34;
constexpr size_t W_TC = 0;
constexpr size_t W_OVH = W_TC + (size_t)MAXB * NCH * CELL * 4;
constexpr size_t W_WP = W_OVH + NCH * 32;
constexpr size_t W_OV = W_WP + NFRAG * 1024;
constexpr float NLOG2E = -1.44269504088896340736f;

__global__ __launch_bounds__(1024) void k_part(const int* __restrict__ src, const int* __restrict__ dst,
                                               int E, int nb,
                                               unsigned* __restrict__ TC,
                                               u32x4* __restrict__ OVH, u32x4* __restrict__ OV,
                                               const unsigned short* __restrict__ wa,
                                               const unsigned short* __restrict__ wb,
                                               const unsigned short* __restrict__ wc,
                                               const unsigned short* __restrict__ wu,
                                               const unsigned short* __restrict__ wv,
                                               unsigned short* __restrict__ wpack) {
  __shared__ __attribute__((aligned(16))) unsigned cT[(MAXB + 1) * CSTR];
  __shared__ __attribute__((aligned(16))) unsigned ovl[8];
  __shared__ int cnt[MAXB + 4];
  const int tid = threadIdx.x, lane = tid & 63;
  const int rnd = __builtin_amdgcn_readfirstlane(tid >> 6);
  const int c = blockIdx.x;
  const int e0 = (rnd * NCH + c) * 256 + lane * 4;
  const auto rd = SRD(dst, E * 4), rs = SRD(src, E * 4);

  const i32x4 dd = __builtin_bit_cast(i32x4, __builtin_amdgcn_raw_buffer_load_b128(rd, e0 * 4, 0, 0));
  const i32x4 ss = __builtin_bit_cast(i32x4, __builtin_amdgcn_raw_buffer_load_b128(rs, e0 * 4, 0, 0));
  unsigned wv8[8];
  if (rnd == 15 && c < NFRAG) {
    const int nn = ((lane >> 2) & 1) * 32 + (c & 1) * 16 + ((lane >> 3) & 3) * 4 + (lane & 3), h = lane >> 5;
    const unsigned short* p;
    if (c < 24) {
      const int m = c >> 3, t = (c >> 1) & 3;
      p = ((m == 0) ? wb : (m == 1) ? wv : wa) + (32 * h + 8 * t) * 64 + nn;
    } else if (c < 26) {
      p = wc + (8 * h) * 64 + nn;
    } else {
      p = wu + (32 * h + 8 * ((c - 26) >> 1)) * 64 + nn;
    }
#pragma unroll
    for (int j = 0; j < 8; ++j) wv8[j] = p[j * 64];
  }
#define CPN 80
  u32x4 t;
  if (rnd == 14 && c < CPN) {
    unsigned long long pc;
    int rel;
    asm volatile("s_getpc_b64 %0\n\ts_mov_b32 %1, _Z6k_mainPKtS0_PKciiPt@rel32@lo+4" : "=s"(pc), "=s"(rel));
    pc += (long long)rel;
    asm volatile("global_load_dwordx4 %0, %1, %2" : "=v"(t) : "v"(((c >> 3) % 10) * 1024 + lane * 16), "s"(pc) : "memory");
  }
  if (tid < 2 * MAXB) *reinterpret_cast<u32x2*>(cT + (tid >> 1) * CSTR + (tid & 1) * 16) = u32x2{0u, 0u};
  else if (tid < 3 * MAXB + 4) cnt[tid - 2 * MAXB] = 0;
  else if (tid < 3 * MAXB + 12) ovl[tid - (3 * MAXB + 4)] = 0x100u;
  __syncthreads();
  if ((rnd * NCH + c) * 256 < E) {
    int bk[4], rk[4];
#pragma unroll
    for (int k = 0; k < 4; ++k) {
      const bool v = e0 + k < E;
      bk[k] = v ? (int)((unsigned)dd[k] / (unsigned)NPB) : MAXB + 1;
      rk[k] = LDS_ADD(&cnt[bk[k]], 1);
    }
    unsigned w[4];
    bool ovf = false;
#pragma unroll
    for (int k = 0; k < 4; ++k) {
      const unsigned s = (unsigned)(dd[k] - bk[k] * NPB);
      w[k] = ((unsigned)ss[k] << 16) | ((s & 7u) << 13) | ((unsigned)(lane * 4 + k) << 5) | (s >> 3);
      const int d = (rk[k] + (rk[k] < CAP / 2 ? 2 : 4)) & 31;
      unsigned* cp = cT + (rk[k] < CAP ? min(bk[k], MAXB) : MAXB) * CSTR;
      cp[d] = w[k];
      __hip_atomic_fetch_or(cp + (d & 16) + ((d >> 3) & 1), (unsigned)rnd << (4 * (d & 7)),
                            __ATOMIC_RELAXED, __HIP_MEMORY_SCOPE_WORKGROUP);
      ovf |= rk[k] >= CAP;
    }
    if (__builtin_amdgcn_ballot_w64(ovf) != 0) {
#pragma unroll 1
      for (int k = 0; k < 4; ++k) {
        const int bq = k == 0 ? bk[0] : k == 1 ? bk[1] : k == 2 ? bk[2] : bk[3];
        const int rq = k == 0 ? rk[0] : k == 1 ? rk[1] : k == 2 ? rk[2] : rk[3];
        const unsigned wq = k == 0 ? w[0] : k == 1 ? w[1] : k == 2 ? w[2] : w[3];
        if (bq < MAXB && rq >= CAP) {
          const int o = LDS_ADD(&cnt[MAXB], 1);
          OV[c * OVCAP + o] = u32x4{wq >> 9, ((unsigned)(e0 + k) << 5) | (wq & 31u), (unsigned)bq, 0u};
          if (o < 4) { ovl[2 * o] = wq; ovl[2 * o + 1] = ((unsigned)rnd << 12) | (unsigned)bq; }
        }
      }
    }
  }
  asm volatile("s_waitcnt lgkmcnt(0)\n\ts_barrier" ::: "memory");
  {
    const auto rc = SRD(TC, MAXB * NCH * CELL * 4);
    const int p = tid & 7;
    const int first = (CAP / 2) * (p >> 2) + 4 * (p & 3) - 2;
#pragma unroll
    for (int q = 0; q < 2; ++q) {
      const int b = (tid >> 3) + q * 128;
      const int m = cnt[b];
      u32x4 v = *reinterpret_cast<const u32x4*>(cT + b * CSTR + 4 * p);
      if ((p & 3) == 0) v[0] |= (unsigned)min(max(m - first - 2, 0), CAP / 2);
      if (b < nb && (WALL || (p & 3) == 0 || first < m)) CST((b * NCH + c) * CELL + 4 * p, v);
    }
    if (tid < 2) {
      u32x4 v = *reinterpret_cast<const u32x4*>(ovl + 4 * tid);
      v[1] |= (unsigned)cnt[MAXB] << 16;
      v[3] |= (unsigned)cnt[MAXB] << 16;
      __builtin_amdgcn_raw_buffer_store_b128(v, SRD(OVH, NCH * 32), c * 32 + tid * 16, 0, 17);
    }
  }
  if (rnd == 15 && c < NFRAG) {
    if (c < 8 || c == 24 || c == 25) {
#pragma unroll
      for (int j = 0; j < 8; ++j) {
        const unsigned u = __float_as_uint(__uint_as_float(wv8[j] << 16) * NLOG2E);
        wv8[j] = (u + 0x7FFFu + ((u >> 16) & 1u)) >> 16;
      }
    }
    const u32x4 v = {wv8[0] | (wv8[1] << 16), wv8[2] | (wv8[3] << 16), wv8[4] | (wv8[5] << 16), wv8[6] | (wv8[7] << 16)};
    __builtin_amdgcn_raw_buffer_store_b128(v, SRD(wpack, NFRAG * 1024), c * 1024 + lane * 16, 0, 17);
  }
  asm volatile("s_waitcnt vmcnt(0)" :: "v"(t) : "memory");
}

__device__ __forceinline__ NOPK unsigned pack_bf16(float lo, float hi) {
  typedef __attribute__((ext_vector_type(2))) float f2;
  typedef __attribute__((ext_vector_type(2))) __bf16 b2;
  f2 v = {lo, hi};
  b2 r = __builtin_convertvector(v, b2);
  return __builtin_bit_cast(unsigned, r);
}
__device__ __forceinline__ NOPK unsigned pack_f16(float lo, float hi) {
  typedef __attribute__((ext_vector_type(2))) float f2;
  f2 v = {lo, hi};
  f16x2 r = __builtin_convertvector(v, f16x2);
  return __builtin_bit_cast(unsigned, r);
}
#define WGSYNC() do { __builtin_amdgcn_fence(__ATOMIC_RELEASE, "workgroup"); __builtin_amdgcn_s_barrier(); \
                      __builtin_amdgcn_fence(__ATOMIC_ACQUIRE, "workgroup"); } while (0)
#define U2F(u) __builtin_bit_cast(float, (unsigned)(u))
constexpr int XB = 1040;
constexpr int RING_SLOT = 4 * XB + 1024;
constexpr int LCAP = 512;
constexpr int LPAD = LCAP + 64;
constexpr int SSCR = 32 * 80;
constexpr int WAVE_LDS = RING_SLOT;
constexpr int M_WL = 0;
constexpr int M_OH = 16 * 1024;
constexpr int M_CNT = M_OH + 2048;
constexpr int M_ST = M_CNT + 256;
constexpr int M_SC = M_ST + 2048;
constexpr int M_LX = M_SC + GPB * SSCR;
constexpr int M_LE = M_LX + GPB * LPAD * 4;
constexpr int M_WV = M_LE + GPB * LPAD * 4;
constexpr int M_XG = M_WV + GPB * WAVE_LDS;
constexpr int MAIN_LDS = M_XG + GPB * 4 * XB;
static_assert(M_LX % 256 == 0 && M_LE % 256 == 0 && M_LE < 65536 && M_LE + GPB * LPAD * 4 + NCH * CELL * 4 <= M_XG, "lists");
static_assert(MAIN_LDS <= 160 * 1024, "LDS");

__global__ __launch_bounds__(512, 2) NOPK void k_main(const unsigned short* __restrict__ x,
                                                      const unsigned short* __restrict__ eattr,
                                                      const char* __restrict__ wsp, int E,
                                                      int N, unsigned short* __restrict__ out) {
  __shared__ __attribute__((aligned(16))) char smem[MAIN_LDS];
  asm volatile("" :: "s"(x), "s"(eattr), "s"(wsp), "s"(E), "s"(N), "s"(out));
  const unsigned* TC = reinterpret_cast<const unsigned*>(wsp + W_TC);
  const u32x4* OVH = reinterpret_cast<const u32x4*>(wsp + W_OVH);
  const u32x4* OV = reinterpret_cast<const u32x4*>(wsp + W_OV);
  const char* wpack = wsp + W_WP;
  const int tid = threadIdx.x, lane = tid & 63;
  const int wave = __builtin_amdgcn_readfirstlane(tid >> 6);
  const int r = lane & 31, h = lane >> 5;
  const int b = blockIdx.x;

  const int pc = lane & 7;
  u32x4 tv[4], ovh;
  {
    const unsigned* cp = TC + ((size_t)b * NCH + wave * 32) * CELL;
    asm volatile("global_load_dwordx4 %0, %5, %6\n\tglobal_load_dwordx4 %1, %5, %6 offset:1024\n\t"
                 "global_load_dwordx4 %2, %5, %6 offset:2048\n\tglobal_load_dwordx4 %3, %5, %6 offset:3072\n\t"
                 "global_load_dwordx4 %4, %5, %7"
                 : "=&v"(tv[0]), "=&v"(tv[1]), "=&v"(tv[2]), "=&v"(tv[3]), "=&v"(ovh)
                 : "v"(lane * 16), "s"(cp), "s"(OVH + wave * 64) : "memory");
  }
#define GLDS(vo, rs, mv) asm volatile("s_mov_b32 m0, %2\n\ts_nop 0\n\tbuffer_load_dwordx4 %0, %1, 0 offen lds" \
                                      :: "v"(vo), "s"(rs), "s"(mv) : "memory")
  const auto rx = SRD(x, N * 128);
  const auto re = SRD(eattr, E * 32);
  const unsigned xc0 = (unsigned)(((lane & 7) ^ ((lane >> 2) & 4)) * 16);
  const unsigned lb = (unsigned)(size_t)(LAS char*)smem;
  LAS int* cnt = (LAS int*)(smem + M_CNT);
  if (tid < 32) cnt[tid] = (tid & 3) == 0 ? (tid >> 2) * (LPAD * 4) : (tid & 3) == 2 ? 127 : 0;
  __builtin_amdgcn_s_setprio(1);
  {
    unsigned* oh = reinterpret_cast<unsigned*>(smem + M_OH);
    const int i = tid;
    const int sl = i >> 4, hh = (i >> 3) & 1, ks = (i >> 2) & 1, jp = i & 3;
    const int j0 = 2 * jp, j1 = 2 * jp + 1;
    const int nd0 = 16 * ks + 8 * (j0 >> 2) + 4 * hh + (j0 & 3);
    const int nd1 = 16 * ks + 8 * (j1 >> 2) + 4 * hh + (j1 & 3);
    oh[i] = (sl == nd0 || nd0 == 30 ? 0x3F80u : 0u) | (sl == nd1 ? 0x3F800000u : 0u);
  }
  asm volatile("s_waitcnt lgkmcnt(0)\n\ts_barrier" ::: "memory");
  {
    const int nw = (min(NPB - wave, N - b * NPB - wave) + 7) >> 3;
#pragma unroll
    for (int k = 0; k < 4; ++k) {
      const int s = (lane >> 3) * 4 + k;
      const unsigned xo = s < nw ? (unsigned)(b * NPB + wave + 8 * s) << 7 : 0xFFFFFF80u;
      GLDS(xo | xc0, rx, lb + M_XG + wave * (4 * XB) + k * XB);
    }
  }
  const LAS bf16x8* wl = (const LAS bf16x8*)(smem + M_WL);
  const LAS bf16x8* ohl = (const LAS bf16x8*)(smem + M_OH);
  LAS unsigned* lxa = (LAS unsigned*)(smem + M_LX);
  LAS unsigned* lea = (LAS unsigned*)(smem + M_LE);
  char* wbase = smem + M_WV + wave * WAVE_LDS;
  const unsigned ring_a = (unsigned)(size_t)(LAS char*)wbase;
  {
    unsigned long long vm[16];
    unsigned ad[16], xe[16], hw[4];
    const unsigned four = 4u;
#pragma unroll
    for (int k = 0; k < 4; ++k) {
      asm volatile("s_waitcnt vmcnt(%1)" : "+v"(tv[k]) : "n"(8 - k) : "memory");
      const unsigned h0 = (unsigned)__builtin_amdgcn_mov_dpp((int)tv[k][0], 0x00, 0xF, 0xF, true);
      const unsigned h1 = (unsigned)__builtin_amdgcn_mov_dpp((int)tv[k][1], 0x00, 0xF, 0xF, true);
      hw[k] = ((lane & 2) ? h1 : h0) >> ((lane & 1) * 16);
      const unsigned m = h0 & 0xFFu;
      unsigned ci[4];
#pragma unroll
      for (int j = 0; j < 4; ++j) {
        vm[4 * k + j] = __builtin_amdgcn_ballot_w64((unsigned)((lane & 3) * 4 + j - 2) < m);
        xe[4 * k + j] = tv[k][j] >> 9;
        ci[j] = (xe[4 * k + j] & 0x70u) + lb;
      }
      asm volatile("s_mov_b64 exec, %4\n\tds_add_rtn_u32 %0, %8, %12 offset:%13\n\t"
                   "s_mov_b64 exec, %5\n\tds_add_rtn_u32 %1, %9, %12 offset:%13\n\t"
                   "s_mov_b64 exec, %6\n\tds_add_rtn_u32 %2, %10, %12 offset:%13\n\t"
                   "s_mov_b64 exec, %7\n\tds_add_rtn_u32 %3, %11, %12 offset:%13\n\t"
                   "s_mov_b64 exec, -1"
                   : "=&v"(ad[4 * k]), "=&v"(ad[4 * k + 1]), "=&v"(ad[4 * k + 2]), "=&v"(ad[4 * k + 3])
                   : "s"(vm[4 * k]), "s"(vm[4 * k + 1]), "s"(vm[4 * k + 2]), "s"(vm[4 * k + 3]),
                     "v"(ci[0]), "v"(ci[1]), "v"(ci[2]), "v"(ci[3]), "v"(four), "n"(M_CNT)
                   : "memory");
    }
#pragma unroll
    for (int k = 0; k < 4; ++k) {
      const unsigned c13 = (unsigned)(wave * 32 + k * 8 + (lane >> 3)) << 13;
      unsigned ee[4];
#pragma unroll
      for (int j = 0; j < 4; ++j) ee[j] = (tv[k][j] & 0x1FFFu) | ((((hw[k] >> (4 * j)) & 15u) << 21) | c13);
      asm volatile("s_waitcnt lgkmcnt(12)\n\t"
                   "s_mov_b64 exec, %0\n\tds_write2st64_b32 %4, %8, %12 offset0:%16 offset1:%17\n\t"
                   "s_mov_b64 exec, %1\n\tds_write2st64_b32 %5, %9, %13 offset0:%16 offset1:%17\n\t"
                   "s_mov_b64 exec, %2\n\tds_write2st64_b32 %6, %10, %14 offset0:%16 offset1:%17\n\t"
                   "s_mov_b64 exec, %3\n\tds_write2st64_b32 %7, %11, %15 offset0:%16 offset1:%17\n\t"
                   "s_mov_b64 exec, -1"
                   :: "s"(vm[4 * k]), "s"(vm[4 * k + 1]), "s"(vm[4 * k + 2]), "s"(vm[4 * k + 3]),
                      "v"(ad[4 * k]), "v"(ad[4 * k + 1]), "v"(ad[4 * k + 2]), "v"(ad[4 * k + 3]),
                      "v"(xe[4 * k]), "v"(xe[4 * k + 1]), "v"(xe[4 * k + 2]), "v"(xe[4 * k + 3]),
                      "v"(ee[0]), "v"(ee[1]), "v"(ee[2]), "v"(ee[3]), "n"(M_LX / 256), "n"(M_LE / 256)
                   : "memory");
    }
  }
    {
      const auto rw = SRD(wpack, NFRAG * 1024);
      const unsigned vo = (unsigned)(wave * 1024 + lane * 16);
      GLDS(vo + 16384u, rw, lb + M_SC + 8192 + wave * 1024);
      GLDS(vo, rw, lb + M_SC + wave * 1024);
      asm volatile("s_mov_b32 m0, %2\n\ts_nop 0\n\tbuffer_load_dword %0, %1, 0 offen lds"
                   :: "v"(24576u + (vo >> 2)), "s"(rw), "s"(lb + M_SC + 16384 + wave * 256) : "memory");
      GLDS(vo + 8192u, rw, lb + M_WL + wave * 1024);
      GLDS(vo + 26624u, rw, lb + M_WL + 8192 + wave * 1024);
    }
  asm volatile("s_waitcnt vmcnt(9)" : "+v"(ovh) :: "memory");
  if (__builtin_amdgcn_ballot_w64((ovh[1] & 0x100u) == 0u) != 0) {
#pragma unroll
    for (int q = 0; q < 2; ++q) {
      const unsigned w = ovh[2 * q], m = ovh[2 * q + 1];
      if ((m & 0x1FFu) == (unsigned)b) {
        LAS unsigned* p = (LAS unsigned*)((LAS char*)lxa + LDS_ADD((LAS int*)((LAS char*)cnt + ((w >> 9) & 0x70u)), 4));
        p[0] = w >> 9;
        p[GPB * LPAD] = (w & 0x1FFFu) | ((m & 0xF000u) << 9) | ((unsigned)(tid >> 1) << 13);
      }
    }
    if ((ovh[1] >> 16) > 4u) cnt[1] = 1;
  }
  asm volatile("s_waitcnt lgkmcnt(0)\n\ts_barrier" ::: "memory");
  const int cpc = cnt[4 * pc] - pc * (LPAD * 4);
  const int cpa = cnt[4 * (lane >> 3)] - (lane >> 3) * (LPAD * 4);
  int mo0 = cnt[1];
  bool ready = __builtin_amdgcn_ballot_w64(cpc > LCAP * 4) == 0;
  int grp;
  {
    const unsigned long long gt = __builtin_amdgcn_ballot_w64(cpc * 2 + pc > cpa * 2 + (lane >> 3));
    const unsigned wd = h ? (unsigned)(gt >> 32) : (unsigned)gt;
    const int rk = __builtin_popcount((wd >> (r & 24)) & 0xFFu);
    grp = __builtin_ctzll(__builtin_amdgcn_ballot_w64(rk == (wave < 4 ? wave : 11 - wave))) >> 3;
  }
  asm volatile("" : "+v"(mo0));
  const int gn0 = b * NPB + grp;
  const int nodes = max((min(NPB - grp, N - gn0) + 7) >> 3, 0);
  const int n0 = min(gn0, N - 1);
  ((LAS unsigned*)(smem + M_ST))[tid] = (ready ? 0x40000u : 0xFFFFu) | ((unsigned)(tid & 1) << 16);

  f32x16 Y[4];
#pragma unroll
  for (int q = 0; q < 4; ++q)
#pragma unroll
    for (int i = 0; i < 16; ++i) Y[q][i] = q < 2 ? 1e-6f * 65536.f : 0.f;

  const unsigned ec0 = (unsigned)(((lane & 1) ^ ((lane >> 4) & 1)) * 16);
  const LAS unsigned* lx = lxa + grp * LPAD;
  const LAS unsigned* le = lea + grp * LPAD;
  const int ohoff = ((((r >> 2) & 1) * 16) + ((r >> 4) * 8) + (((r >> 3) & 1) * 4) + (r & 3)) * 2;
  const int xa0 = (r & 3) * XB + (r >> 2) * 128 + (h ^ ((r >> 3) & 1)) * 64;
  const int ea0 = 4 * XB + r * 32 + ((h ^ ((r >> 3) & 1)) * 16);
#define SB() __builtin_amdgcn_sched_barrier(0)
  bf16x8 wB[4][2], wC[2], XAf[2][2];
  const LAS bf16x8* wvl = wl + lane;
  f32x16 G0, G1, V;
  u32x4 pd[2], pn[2];
  bf16x8 S[2], St[2], as[4], ae, bv[4];
  const f32x16 zero16 = {0.f, 0.f, 0.f, 0.f, 0.f, 0.f, 0.f, 0.f, 0.f, 0.f, 0.f, 0.f, 0.f, 0.f, 0.f, 0.f};
  LAS char* sm = (LAS char*)(smem + M_SC + wave * SSCR);
  LAS u32x4* mine = (LAS u32x4*)(sm + r * 80 + h * 32);
  const LAS char* ring = (const LAS char*)wbase;
  unsigned ga[5];
  unsigned slr;
  int sl;

#define E1(i) Gc[i] = __builtin_amdgcn_exp2f(Gc[i])
#define PD(j) pd[(j) >> 2][(j) & 3]
#define CV(j) PD(j) = pack_f16(Gc[2 * (j)], Gc[2 * (j) + 1])
#define PA(j) PD(j) = __builtin_bit_cast(unsigned, __builtin_bit_cast(f16x2, (unsigned)PD(j)) + __builtin_bit_cast(f16x2, one15))
#define RL(j) do { unsigned t_ = PD(j); asm volatile("v_rcp_f16_sdwa %0, %0 dst_sel:WORD_0 dst_unused:UNUSED_PRESERVE src0_sel:WORD_0" : "+v"(t_)); PD(j) = t_; } while (0)
#define RH(j) do { unsigned t_ = PD(j); asm volatile("v_rcp_f16_sdwa %0, %0 dst_sel:WORD_1 dst_unused:UNUSED_PRESERVE src0_sel:WORD_1" : "+v"(t_)); PD(j) = t_; } while (0)
#define MX(i) V[i] = __builtin_fmaf(V[i], (float)__builtin_bit_cast(f16x2, (unsigned)PD((i) >> 1))[(i) & 1], 0.0f)
#define HC(v) __builtin_bit_cast(f16x8, v)
#define CN(j) pn[(j) >> 2][(j) & 3] = pack_bf16(V[2 * (j)], V[2 * (j) + 1])
#define XG(k) ((xo[k] & ~127u) | xc0)
#define BC(v) __builtin_bit_cast(bf16x8, v)
#define PIN(v) asm volatile("" : "+v"(v))

  const unsigned one15 = 0x02000200u;
  int more, ntiles;
  LAS int* pown = cnt + 4 * wave + 2;
  const LAS int* poth = cnt + 4 * (wave ^ 4) + 2;
  PIN(pown); PIN(poth);
  const LAS unsigned* lxp = lx + (lane >> 3) * 4;
  const LAS unsigned* lep = le + (lane >> 1);
  const LAS unsigned* lsp = le + r;
  auto round_begin = [&](int cb, int mo) NOPK __attribute__((always_inline)) {
    const int cn = min(cb >> 2, LCAP);
    more = __builtin_amdgcn_readfirstlane(mo);
    ntiles = (cn + 31) >> 5;
    {
      LAS unsigned* wx = lxa + grp * LPAD;
      LAS unsigned* we = lea + grp * LPAD;
      {
        unsigned vx[8], ve[8];
        for (int k = 0; k < 8; ++k) { vx[k] = wx[k * 64 + lane]; ve[k] = we[k * 64 + lane]; }
        int base = 0;
        for (int p = 0; p < 4; ++p)
          for (int kk = 0; kk < 8; ++kk) {
            const int k = (1 && (p & 1)) ? 7 - kk : kk;
            const bool v = k * 64 + lane < cn && (int)((vx[k] >> 7) * 2u / (unsigned)N) == p;
            const unsigned long long m = __builtin_amdgcn_ballot_w64(v);
            const int pos = base + (int)__builtin_amdgcn_mbcnt_hi((unsigned)(m >> 32), __builtin_amdgcn_mbcnt_lo((unsigned)m, 0u));
            if (v) { wx[pos] = vx[k]; we[pos] = ve[k]; }
            base += __builtin_popcountll(m);
          }
      }
      const int e0 = ntiles * 32;
      if (cn + r < e0) { wx[cn + r] = 0xFFFFFF80u; we[cn + r] = 0xFFFFFFFFu; }
      wx[e0 + lane] = 0xFFFFFF80u;
      we[e0 + lane] = 0xFFFFFFFFu;
    }
  };
  auto gather = [&](int t, bool issue) NOPK __attribute__((always_inline)) {
    const u32x4 xo = *(const LAS u32x4*)(lxp + t * 32);
    const unsigned eo = lep[t * 32];
    ga[0] = XG(0); ga[1] = XG(1); ga[2] = XG(2); ga[3] = XG(3);
    ga[4] = (eo & ~31u) | ec0;
    if (issue) {
      GLDS(ga[0], rx, ring_a); GLDS(ga[1], rx, ring_a + XB); GLDS(ga[2], rx, ring_a + 2 * XB);
      GLDS(ga[3], rx, ring_a + 3 * XB); GLDS(ga[4], re, ring_a + 4 * XB);
    }
  };
  if (ready) {
    round_begin(__builtin_amdgcn_readlane(cpc, grp), mo0);
    if (wave < 4) gather(0, true);
  }
  asm volatile("s_cmp_lg_u32 %0, 0\n\ts_cbranch_scc1 1f\n\ts_waitcnt vmcnt(0)\n1:\n\ts_waitcnt vmcnt(5)\n\ts_barrier"
               :: "s"(__builtin_amdgcn_readfirstlane((ready && wave < 4) ? 1 : 0)) : "memory", "scc");
  {
      const LAS bf16x8* ws = (const LAS bf16x8*)(smem + M_SC) + lane;
      bf16x8 xg[4], wA[8];
#pragma unroll
      for (int k = 0; k < 4; ++k) xg[k] = *(const LAS bf16x8*)(smem + M_XG + grp * (4 * XB) + xa0 + 16 * k);
#pragma unroll
      for (int k = 0; k < 8; ++k) wA[k] = ws[(8 + k) * 64];
#pragma unroll
      for (int k = 0; k < 8; ++k) wB[k >> 1][k & 1] = ws[k * 64];
      wC[0] = ws[16 * 64];
      wC[1] = ws[17 * 64];
      SB();
      asm volatile("s_waitcnt lgkmcnt(0)\n\ts_barrier"
                   : "+v"(wB[0][0]), "+v"(wB[0][1]), "+v"(wB[1][0]), "+v"(wB[1][1]), "+v"(wB[2][0]), "+v"(wB[2][1]),
                     "+v"(wB[3][0]), "+v"(wB[3][1]), "+v"(wC[0]), "+v"(wC[1]),
                     "+v"(xg[0]), "+v"(xg[1]), "+v"(xg[2]), "+v"(xg[3]), "+v"(wA[0]), "+v"(wA[1]), "+v"(wA[2]),
                     "+v"(wA[3]), "+v"(wA[4]), "+v"(wA[5]), "+v"(wA[6]), "+v"(wA[7]) :: "memory");
      SB();
      if (ready) {
        if (wave >= 4) gather(0, true);

      }
      SB();
#pragma unroll
      for (int ct = 0; ct < 2; ++ct) {
        f32x16 A = zero16;
#pragma unroll
        for (int k = 0; k < 4; ++k) A = MFMA32(xg[k], wA[k * 2 + ct], A);
#pragma unroll
        for (int s = 0; s < 2; ++s) {
          u32x4 p;
#pragma unroll
          for (int j = 0; j < 4; ++j) p[j] = pack_bf16(A[8 * s + 2 * j] * NLOG2E, A[8 * s + 2 * j + 1] * NLOG2E);
          if (s == 1 && h) p[3] = 0xC170u;
          XAf[s][ct] = BC(p);
        }
      }
      const u32x4 z4 = {0u, 0u, 0u, 0u};
      mine[0] = z4;
      mine[1] = z4;
      SB();
    }
  for (;;) {
   if (!ready) {
    asm volatile("s_waitcnt vmcnt(0)" ::: "memory");
    WGSYNC();
    {
      int t2 = (int)__builtin_amdgcn_mbcnt_hi(~0u, __builtin_amdgcn_mbcnt_lo(~0u, 0u));
      asm volatile("" : "+v"(t2));
      const int l2 = t2;
      t2 += wave * 64;
      if (t2 < 32) cnt[t2] = (t2 & 3) == 0 ? (t2 >> 2) * (LPAD * 4) : 0;
      WGSYNC();
      auto put = [&](unsigned t, unsigned e) NOPK -> bool {
        const unsigned gg = (t >> 4) & 7u;
        const int ad = LDS_ADD(&cnt[4 * gg], 4) >> 2;
        if (ad - (int)gg * LPAD >= LCAP) return false;
        lxa[ad] = t;
        lea[ad] = e;
        return true;
      };
      LAS unsigned* stp = (LAS unsigned*)(smem + M_ST) + t2;
      unsigned pe = *stp & 0xFFFFu, ocur = *stp >> 16;
#pragma unroll 1
      for (int j = 0; j < 16; ++j) {
        if ((pe >> j) & 1u) {
          const int cc = wave * 32 + (j >> 2) * 8 + (l2 >> 3);
          const unsigned* cp = TC + ((size_t)b * NCH + cc) * CELL;
          const int d = (l2 & 7) * 4 + (j & 3);
          const unsigned w = cp[d];
          const unsigned nib = (cp[(d & 16) + ((d >> 3) & 1)] >> (4 * (d & 7))) & 15u;
          if ((unsigned)((d & 15) - 2) >= (cp[d & 16] & 0xFFu) ||
              put(w >> 9, (w & 0x1FFFu) | (nib << 21) | ((unsigned)cc << 13))) pe &= ~(1u << j);
        }
      }
      const unsigned on = OVH[t2][1] >> 16;
      const size_t ob = (size_t)(t2 >> 1) * OVCAP;
#pragma unroll 1
      for (; ocur < on; ocur += 2) {
        const u32x4 v = OV[ob + ocur];
        if ((int)v[2] == b && !put(v[0], v[1])) break;
      }
      if (pe != 0u || ocur < on) cnt[1] = 1;
      *stp = pe | (ocur << 16);
    }
    WGSYNC();
    round_begin(__builtin_amdgcn_readfirstlane(cnt[4 * grp]) - grp * (LPAD * 4), cnt[1]);
    gather(0, true);
   }
    gather(1, false);
    ready = false;
    sl = lsp[0] & 31u;
    St[0] = ohl[sl * 4 + h * 2 + 0];
    St[1] = ohl[sl * 4 + h * 2 + 1];
    bv[0] = wvl[0 * 64];
    bv[1] = wvl[2 * 64];
    asm volatile("s_waitcnt vmcnt(0)" ::: "memory");
#pragma unroll
    for (int k = 0; k < 4; ++k) as[k] = *(const LAS bf16x8*)(ring + xa0 + 16 * k);
    ae = *(const LAS bf16x8*)(ring + ea0);
    G0 = MFMA32(St[0], XAf[0][0], zero16);
    G0 = MFMA32(St[1], XAf[1][0], G0);
    SB();
    asm volatile("" : "+v"(as[0]), "+v"(as[1]), "+v"(as[2]), "+v"(as[3]), "+v"(ae) :: "memory");
    G0 = MFMA32(ae, wC[0], G0);
    GLDS(ga[0], rx, ring_a);
    SB();
    G0 = MFMA32(as[0], wB[0][0], G0);
    GLDS(ga[1], rx, ring_a + XB);
    SB();
    G0 = MFMA32(as[1], wB[1][0], G0);
    GLDS(ga[2], rx, ring_a + 2 * XB);
    SB();
    G0 = MFMA32(as[2], wB[2][0], G0);
    GLDS(ga[3], rx, ring_a + 3 * XB);
    SB();
    G0 = MFMA32(as[3], wB[3][0], G0);
    GLDS(ga[4], re, ring_a + 4 * XB);
    pd[1] = u32x4{0u, 0u, 0u, 0u};
    pn[1] = u32x4{0u, 0u, 0u, 0u};
    S[1] = BC(pd[1]);
    SB();
    for (int i = 0; i < ntiles; ++i) {
      {
        f32x16& Gc = G0;
        LAS unsigned short* ohw = (LAS unsigned short*)(sm + sl * 80 + ohoff);
        Y[1] = MFMA32H(HC(pd[1]), HC(S[1]), Y[1]);
        bv[2] = wvl[4 * 64];
        *(volatile LAS int*)pown = ntiles - i;
        E1(0); E1(1); E1(2); E1(3);
        SB();
        Y[3] = MFMA32(BC(pn[1]), S[1], Y[3]);
        bv[3] = wvl[6 * 64];
        E1(4); CV(0); E1(5); CV(1); PA(0);
        SB();
        V = MFMA32(as[0], bv[0], zero16);
        const int prem = *(volatile const LAS int*)poth;
        E1(6); PA(1); RL(0); E1(7); CV(2);
        SB();
        V = MFMA32(as[1], bv[1], V);
        RH(0); E1(8); PA(2); E1(9); CV(3);
        SB();
        V = MFMA32(as[2], bv[2], V);
        RL(1); E1(10); PA(3); E1(11); CV(4);
        SB();
        V = MFMA32(as[3], bv[3], V);
        RH(1); E1(12); PA(4); E1(13); CV(5);
        SB();
        G1 = MFMA32(St[0], XAf[0][1], zero16);
        asm volatile("s_cmp_ge_i32 %0, %1\n\ts_setprio 0\n\ts_cbranch_scc0 1f\n\ts_setprio 1\n1:"
                     :: "s"(ntiles - i), "s"(__builtin_amdgcn_readfirstlane(prem)) : "scc");
        RL(2); E1(14); PA(5); E1(15); CV(6);
        SB();
        G1 = MFMA32(St[1], XAf[1][1], G1);
        *ohw = (unsigned short)0x4000;
        asm volatile("" ::: "memory");
        S[0] = BC(mine[0]);
        S[1] = BC(mine[1]);
        asm volatile("" ::: "memory");
        *ohw = (unsigned short)0;
        RH(2); PA(6); RL(3); CV(7);
        SB();
        G1 = MFMA32(ae, wC[1], G1);
        RH(3); PA(7); MX(0); RL(4); MX(1); MX(2);
        SB();
        G1 = MFMA32(as[0], wB[0][1], G1);
        RH(4); MX(3); RL(5); CN(0); MX(4);
        SB();
        G1 = MFMA32(as[1], wB[1][1], G1);
        RH(5); MX(5); RL(6); CN(1); MX(6);
        SB();
        G1 = MFMA32(as[2], wB[2][1], G1);
        RH(6); MX(7); RL(7); CN(2); MX(8);
        SB();
        G1 = MFMA32(as[3], wB[3][1], G1);
        slr = lsp[(i + 1) * 32];
        RH(7); MX(9); CN(3); MX(10); MX(11); CN(4);
        SB();
        Y[0] = MFMA32H(HC(pd[0]), HC(S[0]), Y[0]);
        bv[0] = wvl[1 * 64];
        MX(12); MX(13); CN(5); MX(14); MX(15);
        SB();
        Y[2] = MFMA32(BC(pn[0]), S[0], Y[2]);
        bv[1] = wvl[3 * 64];
        CN(6); CN(7);
        SB();
      }
      {
        f32x16& Gc = G1;
        sl = slr & 31u;
        Y[0] = MFMA32H(HC(pd[1]), HC(S[1]), Y[0]);
        bv[2] = wvl[5 * 64];
        St[0] = ohl[sl * 4 + h * 2 + 0];
        E1(0); E1(1); E1(2); E1(3);
        SB();
        Y[2] = MFMA32(BC(pn[1]), S[1], Y[2]);
        bv[3] = wvl[7 * 64];
        St[1] = ohl[sl * 4 + h * 2 + 1];
        E1(4); CV(0); E1(5); CV(1); PA(0);
        SB();
        V = MFMA32(as[0], bv[0], zero16);
        const u32x4 xo = *(const LAS u32x4*)(lxp + (i + 2) * 32);
        const unsigned eo = lep[(i + 2) * 32];
        E1(6); PA(1); RL(0); E1(7); CV(2);
        SB();
        V = MFMA32(as[1], bv[1], V);
        RH(0); E1(8); PA(2); E1(9); CV(3);
        SB();
        V = MFMA32(as[2], bv[2], V);
        RL(1); E1(10); PA(3); E1(11); CV(4);
        SB();
        V = MFMA32(as[3], bv[3], V);
        RH(1); E1(12); PA(4); E1(13); CV(5);
        SB();
        G0 = MFMA32(St[0], XAf[0][0], zero16);
        asm volatile("s_waitcnt vmcnt(0)" ::: "memory");
        ae = *(const LAS bf16x8*)(ring + ea0);
        as[0] = *(const LAS bf16x8*)(ring + xa0);
        as[1] = *(const LAS bf16x8*)(ring + xa0 + 16);
        as[2] = *(const LAS bf16x8*)(ring + xa0 + 32);
        as[3] = *(const LAS bf16x8*)(ring + xa0 + 48);
        RL(2); E1(14); PA(5); E1(15); CV(6);
        SB();
        G0 = MFMA32(St[1], XAf[1][0], G0);
        ga[0] = XG(0);
        PIN(ga[0]);
        RH(2); PA(6); RL(3); CV(7);
        SB();
        asm volatile("" : "+v"(as[0]), "+v"(as[1]), "+v"(as[2]), "+v"(as[3]), "+v"(ae) :: "memory");
        G0 = MFMA32(ae, wC[0], G0);
        GLDS(ga[0], rx, ring_a);
        ga[1] = XG(1);
        PIN(ga[1]);
        RH(3); PA(7); MX(0); RL(4); MX(1); MX(2);
        SB();
        G0 = MFMA32(as[0], wB[0][0], G0);
        GLDS(ga[1], rx, ring_a + XB);
        ga[2] = XG(2);
        PIN(ga[2]);
        RH(4); MX(3); RL(5); CN(0); MX(4);
        SB();
        G0 = MFMA32(as[1], wB[1][0], G0);
        GLDS(ga[2], rx, ring_a + 2 * XB);
        ga[3] = XG(3);
        PIN(ga[3]);
        RH(5); MX(5); RL(6); CN(1); MX(6);
        SB();
        G0 = MFMA32(as[2], wB[2][0], G0);
        GLDS(ga[3], rx, ring_a + 3 * XB);
        ga[4] = (eo & ~31u) | ec0;
        PIN(ga[4]);
        RH(6); MX(7); RL(7); CN(2); MX(8);
        SB();
        G0 = MFMA32(as[3], wB[3][0], G0);
        GLDS(ga[4], re, ring_a + 4 * XB);
        RH(7); MX(9); CN(3); MX(10); MX(11); CN(4);
        SB();
        Y[1] = MFMA32H(HC(pd[0]), HC(S[0]), Y[1]);
        bv[0] = wvl[0 * 64];
        MX(12); MX(13); CN(5); MX(14); MX(15);
        SB();
        Y[3] = MFMA32(BC(pn[0]), S[0], Y[3]);
        bv[1] = wvl[2 * 64];
        CN(6); CN(7);
        SB();
      }
    }
    *(volatile LAS int*)pown = 0;
    Y[1] = MFMA32H(HC(pd[1]), HC(S[1]), Y[1]);
    Y[3] = MFMA32(BC(pn[1]), S[1], Y[3]);
    if (!more) break;
  }
  {
    bf16x8 fa[2][6];
#pragma unroll
    for (int k = 0; k < 4; ++k) as[k] = *(const LAS bf16x8*)(smem + M_XG + grp * (4 * XB) + xa0 + 16 * k);
    fa[0][0] = fa[1][0] = ohl[r * 4 + h * 2];
    {
      u32x4 t = __builtin_bit_cast(u32x4, ohl[r * 4 + h * 2 + 1]);
      if (h && r != 30) t[3] &= 0xFFFF0000u;
      fa[0][1] = fa[1][1] = BC(t);
    }
#pragma unroll
    for (int k = 0; k < 8; ++k) fa[k & 1][2 + (k >> 1)] = wvl[(8 + k) * 64];
    LAS char* zw = (LAS char*)wbase + r * 144 + h * 64;
    u32x4 o;
#define FQ(ct, i) do { float t_ = Y[2 + (ct)][i] * __builtin_amdgcn_rcpf(Y[ct][i]); PIN(t_); Y[2 + (ct)][i] = t_; } while (0)
#define FM(ct, m) Y[2 + (ct)] = MFMA32(fa[ct][m], as[(m) < 2 ? 2 * (ct) + (m) : (m) - 2], Y[2 + (ct)])
#define FO(q, j) do { const unsigned w = __builtin_bit_cast(u32x4, as[q])[j];                              \
      float z0 = Y[2 + ((q) >> 1)][((q) & 1) * 8 + 2 * (j)], z1 = Y[2 + ((q) >> 1)][((q) & 1) * 8 + 2 * (j) + 1]; \
      o[j] = pack_bf16(__builtin_amdgcn_fmed3f(z0, U2F(w << 16), 3.4028234e38f),                            \
                       __builtin_amdgcn_fmed3f(z1, U2F(w & 0xffff0000u), 3.4028234e38f));                                                                            \
      if ((j) == 3) *(LAS u32x4*)(zw + (q) * 16) = o; } while (0)
#pragma unroll
    for (int i = 0; i < 16; ++i) FQ(0, i);
    SB();
#pragma unroll
    for (int m = 0; m < 6; ++m) {
      FM(0, m);
#pragma unroll
      for (int i = 3 * m; i < 3 * m + 3 && i < 16; ++i) FQ(1, i);
      SB();
    }
    asm volatile("s_waitcnt vmcnt(0)" ::: "memory");
    FM(1, 0); SB();
    FM(1, 1); FO(0, 0); SB();
    FM(1, 2); FO(0, 1); FO(0, 2); SB();
    FM(1, 3); FO(0, 3); FO(1, 0); SB();
    FM(1, 4); FO(1, 1); FO(1, 2); SB();
    FM(1, 5); FO(1, 3); SB();
    FO(2, 0); FO(2, 1); FO(2, 2); FO(2, 3); FO(3, 0); FO(3, 1); FO(3, 2); FO(3, 3);
  }
  const LAS char* zl = (const LAS char*)wbase;
  u32x4 o[4];
#pragma unroll
  for (int q = 0; q < 4; ++q) o[q] = *(const LAS u32x4*)(zl + (q * 8 + (lane >> 3)) * 144 + (lane & 7) * 16);
#pragma unroll
  for (int q = 0; q < 4; ++q) {
    const int row = q * 8 + (lane >> 3);
    __builtin_amdgcn_raw_buffer_store_b128(o[q], SRD(out, N * 128),
                                           row < nodes ? (n0 + 8 * row) * 128 + (lane & 7) * 16 : 0x7FFFFFF0, 0, 17);
  }
}

extern "C" void kernel_launch(void* const* d_in, const int* in_sizes, int n_in,
                              void* d_out, int out_size, void* d_ws, size_t ws_size,
                              hipStream_t stream) {
  const unsigned short* x = (const unsigned short*)d_in[0];
  const unsigned short* eattr = (const unsigned short*)d_in[1];
  const int* src = (const int*)d_in[2];
  const int* dst = (const int*)d_in[3];
  const unsigned short* wa = (const unsigned short*)d_in[4];
  const unsigned short* wb = (const unsigned short*)d_in[5];
  const unsigned short* wc = (const unsigned short*)d_in[6];
  const unsigned short* wu = (const unsigned short*)d_in[7];
  const unsigned short* wv = (const unsigned short*)d_in[8];
  unsigned short* out = (unsigned short*)d_out;
  const int N = in_sizes[0] / 64;
  const int E = in_sizes[2];
  const int nb = (N + NPB - 1) / NPB;

  char* ws = (char*)d_ws;
  k_part<<<NCH, 1024, 0, stream>>>(src, dst, E, nb, (unsigned*)(ws + W_TC), (u32x4*)(ws + W_OVH), (u32x4*)(ws + W_OV),
                                   wa, wb, wc, wu, wv, (unsigned short*)(ws + W_WP));
  k_main<<<nb, 512, 0, stream>>>(x, eattr, ws, E, N, out);
}
```

```cpp
#include <hip/hip_runtime.h>
#include <hip/hip_bf16.h>
#include <stdint.h>

using bf16x8 = __attribute__((ext_vector_type(8))) short;
using f16x8 = __attribute__((ext_vector_type(8))) _Float16;
using f16x2 = __attribute__((ext_vector_type(2))) _Float16;
using f32x16 = __attribute__((ext_vector_type(16))) float;
using u32x4 = __attribute__((ext_vector_type(4))) unsigned;
using u32x2 = __attribute__((ext_vector_type(2))) unsigned;
using i32x4 = __attribute__((ext_vector_type(4))) int;
using f32x4 = __attribute__((ext_vector_type(4))) float;

#define MFMA32(a, b, c) __builtin_amdgcn_mfma_f32_32x32x16_bf16((a), (b), (c), 0, 0, 0)
#define MFMA32H(a, b, c) __builtin_amdgcn_mfma_f32_32x32x16_f16((a), (b), (c), 0, 0, 0)
#define LAS __attribute__((address_space(3)))
#if defined(__HIP_DEVICE_COMPILE__)
#define NOPK __attribute__((target("no-packed-fp32-ops")))
#else
#define NOPK
#endif
#define SRD(p, bytes) __builtin_amdgcn_make_buffer_rsrc((void*)(p), (short)0, (int)(bytes), 0x00020000)
#define LDS_ADD(p, v) __hip_atomic_fetch_add((p), (v), __ATOMIC_RELAXED, __HIP_MEMORY_SCOPE_WORKGROUP)

#define CST(p, v) __builtin_amdgcn_raw_buffer_store_b128((v), rc, (p) * 4, 0, 17)
#define WALL 1
constexpr int GPB = 8;
constexpr int NPB = 196;
constexpr int MAXB = 256;
constexpr int NCH = 256;
constexpr int CELL = 32;
constexpr int CAP = 28;
constexpr int CSTR = 36;
constexpr int OVCAP = 4096;
constexpr int NFRAG = 34;
constexpr size_t W_TC = 0;
constexpr size_t W_OVH = W_TC + (size_t)MAXB * NCH * CELL * 4;
constexpr size_t W_WP = W_OVH + NCH * 4;
constexpr size_t W_OV = W_WP + NFRAG * 1024;
constexpr float NLOG2E = -1.44269504088896340736f;

__global__ __launch_bounds__(1024) void k_part(const int* __restrict__ src, const int* __restrict__ dst,
                                               int E, int nb,
                                               unsigned* __restrict__ TC,
                                               unsigned* __restrict__ OVH, u32x4* __restrict__ OV,
                                               const unsigned short* __restrict__ wa,
                                               const unsigned short* __restrict__ wb,
                                               const unsigned short* __restrict__ wc,
                                               const unsigned short* __restrict__ wu,
                                               const unsigned short* __restrict__ wv,
                                               unsigned short* __restrict__ wpack) {
  __shared__ __attribute__((aligned(16))) unsigned cT[(MAXB + 1) * CSTR];
  __shared__ int cnt[MAXB + 4];
  const int tid = threadIdx.x, lane = tid & 63;
  const int rnd = __builtin_amdgcn_readfirstlane(tid >> 6);
  const int c = blockIdx.x;
  const int e0 = (rnd * NCH + c) * 256 + lane * 4;
  const auto rd = SRD(dst, E * 4), rs = SRD(src, E * 4);

  const i32x4 dd = __builtin_bit_cast(i32x4, __builtin_amdgcn_raw_buffer_load_b128(rd, e0 * 4, 0, 0));
  const i32x4 ss = __builtin_bit_cast(i32x4, __builtin_amdgcn_raw_buffer_load_b128(rs, e0 * 4, 0, 0));
  unsigned wv8[8];
  if (rnd == 15 && c < NFRAG) {
    const int nn = ((lane >> 2) & 1) * 32 + (c & 1) * 16 + ((lane >> 3) & 3) * 4 + (lane & 3), h = lane >> 5;
    const unsigned short* p;
    if (c < 24) {
      const int m = c >> 3, t = (c >> 1) & 3;
      p = ((m == 0) ? wb : (m == 1) ? wv : wa) + (32 * h + 8 * t) * 64 + nn;
    } else if (c < 26) {
      p = wc + (8 * h) * 64 + nn;
    } else {
      p = wu + (32 * h + 8 * ((c - 26) >> 1)) * 64 + nn;
    }
#pragma unroll
    for (int j = 0; j < 8; ++j) wv8[j] = p[j * 64];
  }
#define CPN 88
  u32x4 t;
  if (rnd == 14 && c < CPN) {
    unsigned long long pc;
    int rel;
    asm volatile("s_getpc_b64 %0\n\ts_mov_b32 %1, _Z6k_mainPKtS0_PKciiPt@rel32@lo+4" : "=s"(pc), "=s"(rel));
    pc += (long long)rel;
    asm volatile("global_load_dwordx4 %0, %1, %2" : "=v"(t) : "v"(min((c >> 3) * 1024 + lane * 16, 10400)), "s"(pc) : "memory");
  }
  if (tid < 2 * MAXB) *reinterpret_cast<u32x2*>(cT + (tid >> 1) * CSTR + (tid & 1) * 16) = u32x2{0u, 0u};
  else if (tid < 3 * MAXB + 4) cnt[tid - 2 * MAXB] = 0;
  __syncthreads();
  if ((rnd * NCH + c) * 256 < E) {
    int bk[4], rk[4];
#pragma unroll
    for (int k = 0; k < 4; ++k) {
      const bool v = e0 + k < E;
      bk[k] = v ? (int)((unsigned)dd[k] / (unsigned)NPB) : MAXB + 1;
      rk[k] = LDS_ADD(&cnt[bk[k]], 1);
    }
    unsigned w[4];
    bool ovf = false;
#pragma unroll
    for (int k = 0; k < 4; ++k) {
      const unsigned s = (unsigned)(dd[k] - bk[k] * NPB);
      w[k] = ((unsigned)ss[k] << 16) | ((s & 7u) << 13) | ((unsigned)(lane * 4 + k) << 5) | (s >> 3);
      const int d = (rk[k] + (rk[k] < CAP / 2 ? 2 : 4)) & 31;
      unsigned* cp = cT + (rk[k] < CAP ? min(bk[k], MAXB) : MAXB) * CSTR;
      cp[d] = w[k];
      __hip_atomic_fetch_or(cp + (d & 16) + ((d >> 3) & 1), (unsigned)rnd << (4 * (d & 7)),
                            __ATOMIC_RELAXED, __HIP_MEMORY_SCOPE_WORKGROUP);
      ovf |= rk[k] >= CAP;
    }
    if (__builtin_amdgcn_ballot_w64(ovf) != 0) {
#pragma unroll 1
      for (int k = 0; k < 4; ++k) {
        const int bq = k == 0 ? bk[0] : k == 1 ? bk[1] : k == 2 ? bk[2] : bk[3];
        const int rq = k == 0 ? rk[0] : k == 1 ? rk[1] : k == 2 ? rk[2] : rk[3];
        const unsigned wq = k == 0 ? w[0] : k == 1 ? w[1] : k == 2 ? w[2] : w[3];
        if (bq < MAXB && rq >= CAP) {
          const int o = LDS_ADD(&cnt[MAXB], 1);
          OV[c * OVCAP + o] = u32x4{wq >> 9, ((unsigned)(e0 + k) << 5) | (wq & 31u), (unsigned)bq, 0u};
        }
      }
    }
  }
  asm volatile("s_waitcnt lgkmcnt(0)\n\ts_barrier" ::: "memory");
  {
    const auto rc = SRD(TC, MAXB * NCH * CELL * 4);
    const int p = tid & 7;
    const int first = (CAP / 2) * (p >> 2) + 4 * (p & 3) - 2;
    const int novf = cnt[MAXB];
#pragma unroll
    for (int q = 0; q < 2; ++q) {
      const int b = (tid >> 3) + q * 128;
      const int m = cnt[b];
      u32x4 v = *reinterpret_cast<const u32x4*>(cT + b * CSTR + 4 * p);
      if ((p & 3) == 0) v[0] |= (unsigned)(m > CAP ? min(CAP / 2 + novf, 255) : min(max(m - first - 2, 0), CAP / 2));
      if (b < nb && (WALL || (p & 3) == 0 || first < m)) CST((b * NCH + c) * CELL + 4 * p, v);
    }
    if (tid == 0) __builtin_amdgcn_raw_buffer_store_b32((unsigned)novf, SRD(OVH, NCH * 4), c * 4, 0, 17);
  }
  if (rnd == 15 && c < NFRAG) {
    if (c < 8 || c == 24 || c == 25) {
#pragma unroll
      for (int j = 0; j < 8; ++j) {
        const unsigned u = __float_as_uint(__uint_as_float(wv8[j] << 16) * NLOG2E);
        wv8[j] = (u + 0x7FFFu + ((u >> 16) & 1u)) >> 16;
      }
    }
    const u32x4 v = {wv8[0] | (wv8[1] << 16), wv8[2] | (wv8[3] << 16), wv8[4] | (wv8[5] << 16), wv8[6] | (wv8[7] << 16)};
    __builtin_amdgcn_raw_buffer_store_b128(v, SRD(wpack, NFRAG * 1024), c * 1024 + lane * 16, 0, 17);
  }
  asm volatile("s_waitcnt vmcnt(0)" :: "v"(t) : "memory");
}

__device__ __forceinline__ NOPK unsigned pack_bf16(float lo, float hi) {
  typedef __attribute__((ext_vector_type(2))) float f2;
  typedef __attribute__((ext_vector_type(2))) __bf16 b2;
  f2 v = {lo, hi};
  b2 r = __builtin_convertvector(v, b2);
  return __builtin_bit_cast(unsigned, r);
}
__device__ __forceinline__ NOPK unsigned pack_f16(float lo, float hi) {
  typedef __attribute__((ext_vector_type(2))) float f2;
  f2 v = {lo, hi};
  f16x2 r = __builtin_convertvector(v, f16x2);
  return __builtin_bit_cast(unsigned, r);
}
#define WGSYNC() do { __builtin_amdgcn_fence(__ATOMIC_RELEASE, "workgroup"); __builtin_amdgcn_s_barrier(); \
                      __builtin_amdgcn_fence(__ATOMIC_ACQUIRE, "workgroup"); } while (0)
#define U2F(u) __builtin_bit_cast(float, (unsigned)(u))
constexpr int XB = 1040;
constexpr int RING_SLOT = 4 * XB + 1024;
constexpr int LCAP = 576;
constexpr int LPAD = LCAP + 96;
constexpr int SSCR = 32 * 80;
constexpr int WAVE_LDS = RING_SLOT;
constexpr int M_WL = 0;
constexpr int M_OH = 16 * 1024;
constexpr int M_CNT = M_OH + 2048;
constexpr int M_ST = M_CNT + 256;
constexpr int M_SC = M_ST + 2048;
constexpr int M_WV = M_SC + GPB * SSCR;
constexpr int M_LX = M_WV + 3 * WAVE_LDS;
constexpr int LED = GPB * LPAD;
constexpr int M_XG = M_WV + GPB * WAVE_LDS + 2 * LED * 4;
constexpr int MAIN_LDS = M_XG + GPB * 4 * XB;
static_assert(M_LX % 16 == 0 && LED % 64 == 0 && LCAP % 32 == 0 && NCH / 2 * CAP * 4 <= 3 * WAVE_LDS &&
              NCH * (CAP / 2 + 8) * 4 <= 5 * WAVE_LDS, "lists");
static_assert(MAIN_LDS <= 160 * 1024, "LDS");

__global__ __launch_bounds__(512, 2) NOPK void k_main(const unsigned short* __restrict__ x,
                                                      const unsigned short* __restrict__ eattr,
                                                      const char* __restrict__ wsp, int E,
                                                      int N, unsigned short* __restrict__ out) {
  __shared__ __attribute__((aligned(16))) char smem[MAIN_LDS];
  asm volatile("" :: "s"(x), "s"(eattr), "s"(wsp), "s"(E), "s"(N), "s"(out));
  const unsigned* TC = reinterpret_cast<const unsigned*>(wsp + W_TC);
  const unsigned* OVH = reinterpret_cast<const unsigned*>(wsp + W_OVH);
  const u32x4* OV = reinterpret_cast<const u32x4*>(wsp + W_OV);
  const char* wpack = wsp + W_WP;
  const int tid = threadIdx.x, lane = tid & 63;
  const int wave = __builtin_amdgcn_readfirstlane(tid >> 6);
  const int r = lane & 31, h = lane >> 5;
  const int b = blockIdx.x;

  const int pc = lane & 7;
  u32x4 tv[4];
  const unsigned* cp0 = TC + ((size_t)b * NCH + wave * 32) * CELL;
#define CLD(k) asm volatile("global_load_dwordx4 %0, %1, %2 offset:%3" : "=v"(tv[k]) : "v"(lane * 16), "s"(cp0), "n"((k) * 1024) : "memory")
  CLD(0);
  CLD(1);
#define GLDS(vo, rs, mv) asm volatile("s_mov_b32 m0, %2\n\ts_nop 0\n\tbuffer_load_dwordx4 %0, %1, 0 offen lds" \
                                      :: "v"(vo), "s"(rs), "s"(mv) : "memory")
  const auto rx = SRD(x, N * 128);
  const auto re = SRD(eattr, E * 32);
  const unsigned xc0 = (unsigned)(((lane & 7) ^ ((lane >> 2) & 4)) * 16);
  const unsigned lb = (unsigned)(size_t)(LAS char*)smem;
  LAS int* cnt = (LAS int*)(smem + M_CNT);
  if (tid < 32) {
    const int mid = lb + M_LX + (tid >> 2) * (LPAD * 4) + LCAP * 2;
    cnt[tid] = (tid & 3) == 0 ? mid : (tid & 3) == 3 ? mid - 4 : (tid & 3) == 2 ? 1023 : 0;
  }
  __builtin_amdgcn_s_setprio(1);
  {
    unsigned* oh = reinterpret_cast<unsigned*>(smem + M_OH);
    const int i = tid;
    const int sl = i >> 4, hh = (i >> 3) & 1, ks = (i >> 2) & 1, jp = i & 3;
    const int j0 = 2 * jp, j1 = 2 * jp + 1;
    const int nd0 = 16 * ks + 8 * (j0 >> 2) + 4 * hh + (j0 & 3);
    const int nd1 = 16 * ks + 8 * (j1 >> 2) + 4 * hh + (j1 & 3);
    oh[i] = (sl == nd0 || nd0 == 30 ? 0x3F80u : 0u) | (sl == nd1 ? 0x3F800000u : 0u);
  }
  asm volatile("s_waitcnt lgkmcnt(0)\n\ts_barrier" ::: "memory");
  CLD(2);
  CLD(3);
  auto own_rows = [&]() NOPK __attribute__((always_inline)) {
    const int nw = (min(NPB - wave, N - b * NPB - wave) + 7) >> 3;
#pragma unroll
    for (int k = 0; k < 4; ++k) {
      const int s = (lane >> 3) * 4 + k;
      const unsigned xo = s < nw ? (unsigned)(b * NPB + wave + 8 * s) << 7 : 0xFFFFFF80u;
      GLDS(xo | xc0, rx, lb + M_XG + wave * (4 * XB) + k * XB);
    }
  };
  const LAS bf16x8* wl = (const LAS bf16x8*)(smem + M_WL);
  const LAS bf16x8* ohl = (const LAS bf16x8*)(smem + M_OH);
#define LP(a) ((LAS unsigned*)(size_t)(unsigned)(a))
  char* wbase = smem + M_WV + wave * WAVE_LDS + (wave >= 3 ? 2 * LED * 4 : 0);
  const unsigned ring_a = (unsigned)(size_t)(LAS char*)wbase;
  {
    unsigned long long vm[16];
    unsigned ad[16], xe[16], hw[4], mk[4];
    const unsigned four = (lane & 8) ? -4 : 4, cof = lb + ((lane & 8) ? 12u : 0u);
    {
      constexpr int k = 0;
      asm volatile("s_waitcnt vmcnt(%1)" : "+v"(tv[k]) : "n"(3) : "memory");
      const unsigned h0 = (unsigned)__builtin_amdgcn_mov_dpp((int)tv[k][0], 0x00, 0xF, 0xF, true);
      const unsigned h1 = (unsigned)__builtin_amdgcn_mov_dpp((int)tv[k][1], 0x00, 0xF, 0xF, true);
      hw[k] = ((lane & 2) ? h1 : h0) >> ((lane & 1) * 16);
      const unsigned m = mk[k] = h0 & 0xFFu;
      unsigned ci[4];
#pragma unroll
      for (int j = 0; j < 4; ++j) {
        vm[4 * k + j] = __builtin_amdgcn_ballot_w64((unsigned)((lane & 3) * 4 + j - 2) < m);
        xe[4 * k + j] = tv[k][j] >> 9;
        ci[j] = (xe[4 * k + j] & 0x70u) | cof;
      }
      asm volatile("s_mov_b64 exec, %4\n\tds_add_rtn_u32 %0, %8, %12 offset:%13\n\t"
                   "s_mov_b64 exec, %5\n\tds_add_rtn_u32 %1, %9, %12 offset:%13\n\t"
                   "s_mov_b64 exec, %6\n\tds_add_rtn_u32 %2, %10, %12 offset:%13\n\t"
                   "s_mov_b64 exec, %7\n\tds_add_rtn_u32 %3, %11, %12 offset:%13\n\t"
                   "s_mov_b64 exec, -1"
                   : "=&v"(ad[4 * k]), "=&v"(ad[4 * k + 1]), "=&v"(ad[4 * k + 2]), "=&v"(ad[4 * k + 3])
                   : "s"(vm[4 * k]), "s"(vm[4 * k + 1]), "s"(vm[4 * k + 2]), "s"(vm[4 * k + 3]),
                     "v"(ci[0]), "v"(ci[1]), "v"(ci[2]), "v"(ci[3]), "v"(four), "n"(M_CNT)
                   : "memory");
    }
    {
      constexpr int k = 1;
      asm volatile("s_waitcnt vmcnt(%1)" : "+v"(tv[k]) : "n"(2) : "memory");
      const unsigned h0 = (unsigned)__builtin_amdgcn_mov_dpp((int)tv[k][0], 0x00, 0xF, 0xF, true);
      const unsigned h1 = (unsigned)__builtin_amdgcn_mov_dpp((int)tv[k][1], 0x00, 0xF, 0xF, true);
      hw[k] = ((lane & 2) ? h1 : h0) >> ((lane & 1) * 16);
      const unsigned m = mk[k] = h0 & 0xFFu;
      unsigned ci[4];
#pragma unroll
      for (int j = 0; j < 4; ++j) {
        vm[4 * k + j] = __builtin_amdgcn_ballot_w64((unsigned)((lane & 3) * 4 + j - 2) < m);
        xe[4 * k + j] = tv[k][j] >> 9;
        ci[j] = (xe[4 * k + j] & 0x70u) | cof;
      }
      asm volatile("s_mov_b64 exec, %4\n\tds_add_rtn_u32 %0, %8, %12 offset:%13\n\t"
                   "s_mov_b64 exec, %5\n\tds_add_rtn_u32 %1, %9, %12 offset:%13\n\t"
                   "s_mov_b64 exec, %6\n\tds_add_rtn_u32 %2, %10, %12 offset:%13\n\t"
                   "s_mov_b64 exec, %7\n\tds_add_rtn_u32 %3, %11, %12 offset:%13\n\t"
                   "s_mov_b64 exec, -1"
                   : "=&v"(ad[4 * k]), "=&v"(ad[4 * k + 1]), "=&v"(ad[4 * k + 2]), "=&v"(ad[4 * k + 3])
                   : "s"(vm[4 * k]), "s"(vm[4 * k + 1]), "s"(vm[4 * k + 2]), "s"(vm[4 * k + 3]),
                     "v"(ci[0]), "v"(ci[1]), "v"(ci[2]), "v"(ci[3]), "v"(four), "n"(M_CNT)
                   : "memory");
    }
    {
      constexpr int k = 0;
      const unsigned c13 = (unsigned)(wave * 32 + k * 8 + (lane >> 3)) << 13;
      unsigned ee[4];
#pragma unroll
      for (int j = 0; j < 4; ++j) ee[j] = (tv[k][j] & 0x1FFFu) | ((((hw[k] >> (4 * j)) & 15u) << 21) | c13);
      asm volatile("s_waitcnt lgkmcnt(4)\n\t"
                   "s_mov_b64 exec, %0\n\tds_write2st64_b32 %4, %8, %12 offset0:%16 offset1:%17\n\t"
                   "s_mov_b64 exec, %1\n\tds_write2st64_b32 %5, %9, %13 offset0:%16 offset1:%17\n\t"
                   "s_mov_b64 exec, %2\n\tds_write2st64_b32 %6, %10, %14 offset0:%16 offset1:%17\n\t"
                   "s_mov_b64 exec, %3\n\tds_write2st64_b32 %7, %11, %15 offset0:%16 offset1:%17\n\t"
                   "s_mov_b64 exec, -1"
                   :: "s"(vm[4 * k]), "s"(vm[4 * k + 1]), "s"(vm[4 * k + 2]), "s"(vm[4 * k + 3]),
                      "v"(ad[4 * k]), "v"(ad[4 * k + 1]), "v"(ad[4 * k + 2]), "v"(ad[4 * k + 3]),
                      "v"(xe[4 * k]), "v"(xe[4 * k + 1]), "v"(xe[4 * k + 2]), "v"(xe[4 * k + 3]),
                      "v"(ee[0]), "v"(ee[1]), "v"(ee[2]), "v"(ee[3]), "n"(0), "n"(LED / 64)
                   : "memory");
    }
    {
      constexpr int k = 1;
      const unsigned c13 = (unsigned)(wave * 32 + k * 8 + (lane >> 3)) << 13;
      unsigned ee[4];
#pragma unroll
      for (int j = 0; j < 4; ++j) ee[j] = (tv[k][j] & 0x1FFFu) | ((((hw[k] >> (4 * j)) & 15u) << 21) | c13);
      asm volatile("s_waitcnt lgkmcnt(4)\n\t"
                   "s_mov_b64 exec, %0\n\tds_write2st64_b32 %4, %8, %12 offset0:%16 offset1:%17\n\t"
                   "s_mov_b64 exec, %1\n\tds_write2st64_b32 %5, %9, %13 offset0:%16 offset1:%17\n\t"
                   "s_mov_b64 exec, %2\n\tds_write2st64_b32 %6, %10, %14 offset0:%16 offset1:%17\n\t"
                   "s_mov_b64 exec, %3\n\tds_write2st64_b32 %7, %11, %15 offset0:%16 offset1:%17\n\t"
                   "s_mov_b64 exec, -1"
                   :: "s"(vm[4 * k]), "s"(vm[4 * k + 1]), "s"(vm[4 * k + 2]), "s"(vm[4 * k + 3]),
                      "v"(ad[4 * k]), "v"(ad[4 * k + 1]), "v"(ad[4 * k + 2]), "v"(ad[4 * k + 3]),
                      "v"(xe[4 * k]), "v"(xe[4 * k + 1]), "v"(xe[4 * k + 2]), "v"(xe[4 * k + 3]),
                      "v"(ee[0]), "v"(ee[1]), "v"(ee[2]), "v"(ee[3]), "n"(0), "n"(LED / 64)
                   : "memory");
    }
    own_rows();
    {
      constexpr int k = 2;
      asm volatile("s_waitcnt vmcnt(%1)" : "+v"(tv[k]) : "n"(5) : "memory");
      const unsigned h0 = (unsigned)__builtin_amdgcn_mov_dpp((int)tv[k][0], 0x00, 0xF, 0xF, true);
      const unsigned h1 = (unsigned)__builtin_amdgcn_mov_dpp((int)tv[k][1], 0x00, 0xF, 0xF, true);
      hw[k] = ((lane & 2) ? h1 : h0) >> ((lane & 1) * 16);
      const unsigned m = mk[k] = h0 & 0xFFu;
      unsigned ci[4];
#pragma unroll
      for (int j = 0; j < 4; ++j) {
        vm[4 * k + j] = __builtin_amdgcn_ballot_w64((unsigned)((lane & 3) * 4 + j - 2) < m);
        xe[4 * k + j] = tv[k][j] >> 9;
        ci[j] = (xe[4 * k + j] & 0x70u) | cof;
      }
      asm volatile("s_mov_b64 exec, %4\n\tds_add_rtn_u32 %0, %8, %12 offset:%13\n\t"
                   "s_mov_b64 exec, %5\n\tds_add_rtn_u32 %1, %9, %12 offset:%13\n\t"
                   "s_mov_b64 exec, %6\n\tds_add_rtn_u32 %2, %10, %12 offset:%13\n\t"
                   "s_mov_b64 exec, %7\n\tds_add_rtn_u32 %3, %11, %12 offset:%13\n\t"
                   "s_mov_b64 exec, -1"
                   : "=&v"(ad[4 * k]), "=&v"(ad[4 * k + 1]), "=&v"(ad[4 * k + 2]), "=&v"(ad[4 * k + 3])
                   : "s"(vm[4 * k]), "s"(vm[4 * k + 1]), "s"(vm[4 * k + 2]), "s"(vm[4 * k + 3]),
                     "v"(ci[0]), "v"(ci[1]), "v"(ci[2]), "v"(ci[3]), "v"(four), "n"(M_CNT)
                   : "memory");
    }
    {
      constexpr int k = 3;
      asm volatile("s_waitcnt vmcnt(%1)" : "+v"(tv[k]) : "n"(4) : "memory");
      const unsigned h0 = (unsigned)__builtin_amdgcn_mov_dpp((int)tv[k][0], 0x00, 0xF, 0xF, true);
      const unsigned h1 = (unsigned)__builtin_amdgcn_mov_dpp((int)tv[k][1], 0x00, 0xF, 0xF, true);
      hw[k] = ((lane & 2) ? h1 : h0) >> ((lane & 1) * 16);
      const unsigned m = mk[k] = h0 & 0xFFu;
      unsigned ci[4];
#pragma unroll
      for (int j = 0; j < 4; ++j) {
        vm[4 * k + j] = __builtin_amdgcn_ballot_w64((unsigned)((lane & 3) * 4 + j - 2) < m);
        xe[4 * k + j] = tv[k][j] >> 9;
        ci[j] = (xe[4 * k + j] & 0x70u) | cof;
      }
      asm volatile("s_mov_b64 exec, %4\n\tds_add_rtn_u32 %0, %8, %12 offset:%13\n\t"
                   "s_mov_b64 exec, %5\n\tds_add_rtn_u32 %1, %9, %12 offset:%13\n\t"
                   "s_mov_b64 exec, %6\n\tds_add_rtn_u32 %2, %10, %12 offset:%13\n\t"
                   "s_mov_b64 exec, %7\n\tds_add_rtn_u32 %3, %11, %12 offset:%13\n\t"
                   "s_mov_b64 exec, -1"
                   : "=&v"(ad[4 * k]), "=&v"(ad[4 * k + 1]), "=&v"(ad[4 * k + 2]), "=&v"(ad[4 * k + 3])
                   : "s"(vm[4 * k]), "s"(vm[4 * k + 1]), "s"(vm[4 * k + 2]), "s"(vm[4 * k + 3]),
                     "v"(ci[0]), "v"(ci[1]), "v"(ci[2]), "v"(ci[3]), "v"(four), "n"(M_CNT)
                   : "memory");
    }
    {
      constexpr int k = 2;
      const unsigned c13 = (unsigned)(wave * 32 + k * 8 + (lane >> 3)) << 13;
      unsigned ee[4];
#pragma unroll
      for (int j = 0; j < 4; ++j) ee[j] = (tv[k][j] & 0x1FFFu) | ((((hw[k] >> (4 * j)) & 15u) << 21) | c13);
      asm volatile("s_waitcnt lgkmcnt(4)\n\t"
                   "s_mov_b64 exec, %0\n\tds_write2st64_b32 %4, %8, %12 offset0:%16 offset1:%17\n\t"
                   "s_mov_b64 exec, %1\n\tds_write2st64_b32 %5, %9, %13 offset0:%16 offset1:%17\n\t"
                   "s_mov_b64 exec, %2\n\tds_write2st64_b32 %6, %10, %14 offset0:%16 offset1:%17\n\t"
                   "s_mov_b64 exec, %3\n\tds_write2st64_b32 %7, %11, %15 offset0:%16 offset1:%17\n\t"
                   "s_mov_b64 exec, -1"
                   :: "s"(vm[4 * k]), "s"(vm[4 * k + 1]), "s"(vm[4 * k + 2]), "s"(vm[4 * k + 3]),
                      "v"(ad[4 * k]), "v"(ad[4 * k + 1]), "v"(ad[4 * k + 2]), "v"(ad[4 * k + 3]),
                      "v"(xe[4 * k]), "v"(xe[4 * k + 1]), "v"(xe[4 * k + 2]), "v"(xe[4 * k + 3]),
                      "v"(ee[0]), "v"(ee[1]), "v"(ee[2]), "v"(ee[3]), "n"(0), "n"(LED / 64)
                   : "memory");
    }
    {
      constexpr int k = 3;
      const unsigned c13 = (unsigned)(wave * 32 + k * 8 + (lane >> 3)) << 13;
      unsigned ee[4];
#pragma unroll
      for (int j = 0; j < 4; ++j) ee[j] = (tv[k][j] & 0x1FFFu) | ((((hw[k] >> (4 * j)) & 15u) << 21) | c13);
      asm volatile("s_waitcnt lgkmcnt(4)\n\t"
                   "s_mov_b64 exec, %0\n\tds_write2st64_b32 %4, %8, %12 offset0:%16 offset1:%17\n\t"
                   "s_mov_b64 exec, %1\n\tds_write2st64_b32 %5, %9, %13 offset0:%16 offset1:%17\n\t"
                   "s_mov_b64 exec, %2\n\tds_write2st64_b32 %6, %10, %14 offset0:%16 offset1:%17\n\t"
                   "s_mov_b64 exec, %3\n\tds_write2st64_b32 %7, %11, %15 offset0:%16 offset1:%17\n\t"
                   "s_mov_b64 exec, -1"
                   :: "s"(vm[4 * k]), "s"(vm[4 * k + 1]), "s"(vm[4 * k + 2]), "s"(vm[4 * k + 3]),
                      "v"(ad[4 * k]), "v"(ad[4 * k + 1]), "v"(ad[4 * k + 2]), "v"(ad[4 * k + 3]),
                      "v"(xe[4 * k]), "v"(xe[4 * k + 1]), "v"(xe[4 * k + 2]), "v"(xe[4 * k + 3]),
                      "v"(ee[0]), "v"(ee[1]), "v"(ee[2]), "v"(ee[3]), "n"(0), "n"(LED / 64)
                   : "memory");
    }
    if (__builtin_expect(__builtin_amdgcn_ballot_w64(max(max(mk[0], mk[1]), max(mk[2], mk[3])) > CAP / 2) != 0, 0)) {
#pragma unroll 1
      for (int k = 0; k < 4; ++k) {
        const unsigned m = k == 0 ? mk[0] : k == 1 ? mk[1] : k == 2 ? mk[2] : mk[3];
        if (pc + CAP / 2 < m) {
          const u32x4 v = OV[(wave * 32 + k * 8 + (lane >> 3)) * OVCAP + pc];
          if ((int)v[2] == b) {
            LAS unsigned* p = LP(LDS_ADD((LAS int*)((LAS char*)cnt + (v[0] & 0x70u)), 4));
            p[0] = v[0];
            p[LED] = v[1];
          }
          if (m > CAP / 2 + 8) cnt[1] = 1;
        }
      }
    }
  }
    {
      const auto rw = SRD(wpack, NFRAG * 1024);
      const unsigned vo = (unsigned)(wave * 1024 + lane * 16);
      GLDS(vo + 16384u, rw, lb + M_SC + 8192 + wave * 1024);
      GLDS(vo, rw, lb + M_SC + wave * 1024);
      asm volatile("s_mov_b32 m0, %2\n\ts_nop 0\n\tbuffer_load_dword %0, %1, 0 offen lds"
                   :: "v"(24576u + (vo >> 2)), "s"(rw), "s"(lb + M_SC + 16384 + wave * 256) : "memory");
      GLDS(vo + 8192u, rw, lb + M_WL + wave * 1024);
      GLDS(vo + 26624u, rw, lb + M_WL + 8192 + wave * 1024);
    }
  asm volatile("s_waitcnt lgkmcnt(0)\n\ts_barrier" ::: "memory");
  const int cu = cnt[4 * pc], cd = cnt[4 * pc + 3];
  const int cpc = cu - cd;
  const int cpa = cnt[4 * (lane >> 3)] - cnt[4 * (lane >> 3) + 3];
  const int lo = lb + M_LX + pc * (LPAD * 4);
  int mo0 = cnt[1];
  bool ready = __builtin_amdgcn_ballot_w64(max(cu - LCAP * 4 - lo, lo - 4 - cd) > 0) == 0;
  int grp;
  {
    const unsigned long long gt = __builtin_amdgcn_ballot_w64(cpc * 2 + pc > cpa * 2 + (lane >> 3));
    const unsigned wd = h ? (unsigned)(gt >> 32) : (unsigned)gt;
    const int rk = __builtin_popcount((wd >> (r & 24)) & 0xFFu);
    grp = __builtin_ctzll(__builtin_amdgcn_ballot_w64(rk == (wave < 4 ? wave : 11 - wave))) >> 3;
  }
  asm volatile("" : "+v"(mo0));
  const int gn0 = b * NPB + grp;
  const int nodes = max((min(NPB - grp, N - gn0) + 7) >> 3, 0);
  const int n0 = min(gn0, N - 1);
  ((LAS unsigned*)(smem + M_ST))[tid] = (ready ? 0x80000u : 0xFFFFu) | ((unsigned)(tid & 1) << 16);

  f32x16 Y[4];
#pragma unroll
  for (int q = 0; q < 4; ++q)
#pragma unroll
    for (int i = 0; i < 16; ++i) Y[q][i] = q < 2 ? 1e-6f * 65536.f : 0.f;

  const unsigned ec0 = (unsigned)(((lane & 1) ^ ((lane >> 4) & 1)) * 16);
  const int ohoff = ((((r >> 2) & 1) * 16) + ((r >> 4) * 8) + (((r >> 3) & 1) * 4) + (r & 3)) * 2;
  const int xa0 = (r & 3) * XB + (r >> 2) * 128 + (h ^ ((r >> 3) & 1)) * 64;
  const int ea0 = 4 * XB + r * 32 + ((h ^ ((r >> 3) & 1)) * 16);
#define SB() __builtin_amdgcn_sched_barrier(0)
  bf16x8 wB[4][2], wC[2], XAf[2][2];
  const LAS bf16x8* wvl = wl + lane;
  f32x16 G0, G1, V;
  u32x4 pd[2], pn[2];
  bf16x8 S[2], St[2], as[4], ae, bv[4];
  const f32x16 zero16 = {0.f, 0.f, 0.f, 0.f, 0.f, 0.f, 0.f, 0.f, 0.f, 0.f, 0.f, 0.f, 0.f, 0.f, 0.f, 0.f};
  LAS char* sm = (LAS char*)(smem + M_SC + wave * SSCR);
  LAS u32x4* mine = (LAS u32x4*)(sm + r * 80 + h * 32);
  const LAS char* ring = (const LAS char*)wbase;
  unsigned ga[5];
  unsigned slr;
  int sl;

#define E1(i) Gc[i] = __builtin_amdgcn_exp2f(Gc[i])
#define PD(j) pd[(j) >> 2][(j) & 3]
#define CV(j) PD(j) = pack_f16(Gc[2 * (j)], Gc[2 * (j) + 1])
#define PA(j) PD(j) = __builtin_bit_cast(unsigned, __builtin_bit_cast(f16x2, (unsigned)PD(j)) + __builtin_bit_cast(f16x2, one15))
#define RL(j) do { unsigned t_ = PD(j); asm volatile("v_rcp_f16_sdwa %0, %0 dst_sel:WORD_0 dst_unused:UNUSED_PRESERVE src0_sel:WORD_0" : "+v"(t_)); PD(j) = t_; } while (0)
#define RH(j) do { unsigned t_ = PD(j); asm volatile("v_rcp_f16_sdwa %0, %0 dst_sel:WORD_1 dst_unused:UNUSED_PRESERVE src0_sel:WORD_1" : "+v"(t_)); PD(j) = t_; } while (0)
#define MX(i) V[i] = __builtin_fmaf(V[i], (float)__builtin_bit_cast(f16x2, (unsigned)PD((i) >> 1))[(i) & 1], 0.0f)
#define HC(v) __builtin_bit_cast(f16x8, v)
#define CN(j) pn[(j) >> 2][(j) & 3] = pack_bf16(V[2 * (j)], V[2 * (j) + 1])
#define XG(k) ((xo[k] & ~127u) | xc0)
#define BC(v) __builtin_bit_cast(bf16x8, v)
#define PIN(v) asm volatile("" : "+v"(v))

  const unsigned one15 = 0x02000200u;
  int more, ntiles, nq;
  LAS int* pown = cnt + 4 * wave + 2;
  const LAS int* poth = cnt + 4 * (wave ^ 4) + 2;
  PIN(pown); PIN(poth);
  const LAS unsigned *lxp, *lep, *lsp;
  auto round_begin = [&](int sa, int ea, int mo) NOPK __attribute__((always_inline)) {
    const int s4 = sa & ~15;
    const int cn = (ea - s4) >> 2;
    more = __builtin_amdgcn_readfirstlane(mo);
    ntiles = (cn + 31) >> 5;
    nq = (cn + 7) >> 3;
    lxp = LP(s4) + (lane >> 3) * 4;
    lep = LP(s4) + LED + (lane >> 1);
    lsp = LP(s4) + LED + r;
    LAS unsigned* w0 = LP(ea) + lane;
    LAS unsigned* w1 = (h && s4 + 4 * r < sa) ? LP(s4) + r : LP(ea) + 64 + r;
    w0[0] = 0xFFFFFF80u;
    w0[LED] = 0xFFFFFFFFu;
    w1[0] = 0xFFFFFF80u;
    w1[LED] = 0xFFFFFFFFu;
  };
  auto gather = [&](int t, bool issue) NOPK __attribute__((always_inline)) {
    const u32x4 xo = *(const LAS u32x4*)(lxp + t * 32);
    const unsigned eo = lep[t * 32];
    ga[0] = XG(0); ga[1] = XG(1); ga[2] = XG(2); ga[3] = XG(3);
    ga[4] = (eo & ~31u) | ec0;
    if (issue) {
      GLDS(ga[0], rx, ring_a); GLDS(ga[1], rx, ring_a + XB); GLDS(ga[2], rx, ring_a + 2 * XB);
      GLDS(ga[3], rx, ring_a + 3 * XB); GLDS(ga[4], re, ring_a + 4 * XB);
    }
  };
  if (ready) {
    round_begin(__builtin_amdgcn_readlane(cd, grp) + 4, __builtin_amdgcn_readlane(cu, grp), mo0);
    if (wave < 4) gather(0, true);
  }
  asm volatile("s_cmp_lg_u32 %0, 0\n\ts_cbranch_scc1 1f\n\ts_waitcnt vmcnt(0)\n1:\n\ts_waitcnt vmcnt(5)\n\ts_barrier"
               :: "s"(__builtin_amdgcn_readfirstlane((ready && wave < 4) ? 1 : 0)) : "memory", "scc");
  {
      const LAS bf16x8* ws = (const LAS bf16x8*)(smem + M_SC) + lane;
      bf16x8 xg[4], wA[8];
#pragma unroll
      for (int k = 0; k < 4; ++k) xg[k] = *(const LAS bf16x8*)(smem + M_XG + grp * (4 * XB) + xa0 + 16 * k);
#pragma unroll
      for (int k = 0; k < 8; ++k) wA[k] = ws[(8 + k) * 64];
#pragma unroll
      for (int k = 0; k < 8; ++k) wB[k >> 1][k & 1] = ws[k * 64];
      wC[0] = ws[16 * 64];
      wC[1] = ws[17 * 64];
      SB();
      asm volatile("s_waitcnt lgkmcnt(0)\n\ts_barrier"
                   : "+v"(wB[0][0]), "+v"(wB[0][1]), "+v"(wB[1][0]), "+v"(wB[1][1]), "+v"(wB[2][0]), "+v"(wB[2][1]),
                     "+v"(wB[3][0]), "+v"(wB[3][1]), "+v"(wC[0]), "+v"(wC[1]),
                     "+v"(xg[0]), "+v"(xg[1]), "+v"(xg[2]), "+v"(xg[3]), "+v"(wA[0]), "+v"(wA[1]), "+v"(wA[2]),
                     "+v"(wA[3]), "+v"(wA[4]), "+v"(wA[5]), "+v"(wA[6]), "+v"(wA[7]) :: "memory");
      SB();
      if (ready) {
        if (wave >= 4) gather(0, true);

      }
      SB();
#pragma unroll
      for (int ct = 0; ct < 2; ++ct) {
        f32x16 A = zero16;
#pragma unroll
        for (int k = 0; k < 4; ++k) A = MFMA32(xg[k], wA[k * 2 + ct], A);
#pragma unroll
        for (int s = 0; s < 2; ++s) {
          u32x4 p;
#pragma unroll
          for (int j = 0; j < 4; ++j) p[j] = pack_bf16(A[8 * s + 2 * j] * NLOG2E, A[8 * s + 2 * j + 1] * NLOG2E);
          if (s == 1 && h) p[3] = 0xC170u;
          XAf[s][ct] = BC(p);
        }
      }
      const u32x4 z4 = {0u, 0u, 0u, 0u};
      mine[0] = z4;
      mine[1] = z4;
      SB();
    }
  for (;;) {
   if (__builtin_expect(!ready, 0)) {
    asm volatile("s_waitcnt vmcnt(0)" ::: "memory");
    WGSYNC();
    {
      int t2 = (int)__builtin_amdgcn_mbcnt_hi(~0u, __builtin_amdgcn_mbcnt_lo(~0u, 0u));
      asm volatile("" : "+v"(t2));
      const int l2 = t2;
      t2 += wave * 64;
      if (t2 < 32) cnt[t2] = 0;
      WGSYNC();
      auto put = [&](unsigned t, unsigned e) NOPK -> bool {
        const unsigned gg = (t >> 4) & 7u;
        const int ad = LDS_ADD(&cnt[4 * gg], 4) >> 2;
        if (ad >= LCAP) return false;
        LAS unsigned* p = LP(lb + M_LX) + gg * LPAD + ad;
        p[0] = t;
        p[LED] = e;
        return true;
      };
      LAS unsigned* stp = (LAS unsigned*)(smem + M_ST) + t2;
      unsigned pe = *stp & 0xFFFFu, ocur = *stp >> 16;
#pragma unroll 1
      for (int j = 0; j < 16; ++j) {
        if ((pe >> j) & 1u) {
          const int cc = wave * 32 + (j >> 2) * 8 + (l2 >> 3);
          const unsigned* cp = TC + ((size_t)b * NCH + cc) * CELL;
          const int d = (l2 & 7) * 4 + (j & 3);
          const unsigned w = cp[d];
          const unsigned nib = (cp[(d & 16) + ((d >> 3) & 1)] >> (4 * (d & 7))) & 15u;
          if ((unsigned)((d & 15) - 2) >= (cp[d & 16] & 0xFFu) ||
              put(w >> 9, (w & 0x1FFFu) | (nib << 21) | ((unsigned)cc << 13))) pe &= ~(1u << j);
        }
      }
      const unsigned on = OVH[t2 >> 1];
      const size_t ob = (size_t)(t2 >> 1) * OVCAP;
#pragma unroll 1
      for (; ocur < on; ocur += 2) {
        const u32x4 v = OV[ob + ocur];
        if ((int)v[2] == b && !put(v[0], v[1])) break;
      }
      if (pe != 0u || ocur < on) cnt[1] = 1;
      *stp = pe | (ocur << 16);
    }
    WGSYNC();
    round_begin(lb + M_LX + grp * (LPAD * 4), lb + M_LX + grp * (LPAD * 4) + min(__builtin_amdgcn_readfirstlane(cnt[4 * grp]), LCAP * 4), cnt[1]);
    gather(0, true);
   }
    gather(1, false);
    ready = false;
    sl = lsp[0] & 31u;
    St[0] = ohl[sl * 4 + h * 2 + 0];
    St[1] = ohl[sl * 4 + h * 2 + 1];
    bv[0] = wvl[0 * 64];
    bv[1] = wvl[2 * 64];
    asm volatile("s_waitcnt vmcnt(0)" ::: "memory");
#pragma unroll
    for (int k = 0; k < 4; ++k) as[k] = *(const LAS bf16x8*)(ring + xa0 + 16 * k);
    ae = *(const LAS bf16x8*)(ring + ea0);
    G0 = MFMA32(St[0], XAf[0][0], zero16);
    G0 = MFMA32(St[1], XAf[1][0], G0);
    SB();
    asm volatile("" : "+v"(as[0]), "+v"(as[1]), "+v"(as[2]), "+v"(as[3]), "+v"(ae) :: "memory");
    G0 = MFMA32(ae, wC[0], G0);
    GLDS(ga[0], rx, ring_a);
    SB();
    G0 = MFMA32(as[0], wB[0][0], G0);
    GLDS(ga[1], rx, ring_a + XB);
    SB();
    G0 = MFMA32(as[1], wB[1][0], G0);
    GLDS(ga[2], rx, ring_a + 2 * XB);
    SB();
    G0 = MFMA32(as[2], wB[2][0], G0);
    GLDS(ga[3], rx, ring_a + 3 * XB);
    SB();
    G0 = MFMA32(as[3], wB[3][0], G0);
    GLDS(ga[4], re, ring_a + 4 * XB);
    pd[1] = u32x4{0u, 0u, 0u, 0u};
    pn[1] = u32x4{0u, 0u, 0u, 0u};
    S[1] = BC(pd[1]);
    SB();
    for (int i = 0; 4 * i + 2 < nq; ++i) {
      {
        f32x16& Gc = G0;
        LAS unsigned short* ohw = (LAS unsigned short*)(sm + sl * 80 + ohoff);
        Y[1] = MFMA32H(HC(pd[1]), HC(S[1]), Y[1]);
        bv[2] = wvl[4 * 64];
        *(volatile LAS int*)pown = nq - 4 * i;
        E1(0); E1(1); E1(2); E1(3);
        SB();
        Y[3] = MFMA32(BC(pn[1]), S[1], Y[3]);
        bv[3] = wvl[6 * 64];
        E1(4); CV(0); E1(5); CV(1); PA(0);
        SB();
        V = MFMA32(as[0], bv[0], zero16);
        const int prem = *(volatile const LAS int*)poth;
        E1(6); PA(1); RL(0); E1(7); CV(2);
        SB();
        V = MFMA32(as[1], bv[1], V);
        RH(0); E1(8); PA(2); E1(9); CV(3);
        SB();
        V = MFMA32(as[2], bv[2], V);
        RL(1); E1(10); PA(3); E1(11); CV(4);
        SB();
        V = MFMA32(as[3], bv[3], V);
        RH(1); E1(12); PA(4); E1(13); CV(5);
        SB();
        G1 = MFMA32(St[0], XAf[0][1], zero16);
        asm volatile("s_cmp_ge_i32 %0, %1\n\ts_setprio 0\n\ts_cbranch_scc0 1f\n\ts_setprio 1\n1:"
                     :: "s"(nq - 4 * i), "s"(__builtin_amdgcn_readfirstlane(prem)) : "scc");
        RL(2); E1(14); PA(5); E1(15); CV(6);
        SB();
        G1 = MFMA32(St[1], XAf[1][1], G1);
        *ohw = (unsigned short)0x4000;
        asm volatile("" ::: "memory");
        S[0] = BC(mine[0]);
        S[1] = BC(mine[1]);
        asm volatile("" ::: "memory");
        *ohw = (unsigned short)0;
        RH(2); PA(6); RL(3); CV(7);
        SB();
        G1 = MFMA32(ae, wC[1], G1);
        RH(3); PA(7); MX(0); RL(4); MX(1); MX(2);
        SB();
        G1 = MFMA32(as[0], wB[0][1], G1);
        RH(4); MX(3); RL(5); CN(0); MX(4);
        SB();
        G1 = MFMA32(as[1], wB[1][1], G1);
        RH(5); MX(5); RL(6); CN(1); MX(6);
        SB();
        G1 = MFMA32(as[2], wB[2][1], G1);
        RH(6); MX(7); RL(7); CN(2); MX(8);
        SB();
        G1 = MFMA32(as[3], wB[3][1], G1);
        slr = lsp[(i + 1) * 32];
        RH(7); MX(9); CN(3); MX(10); MX(11); CN(4);
        SB();
        Y[0] = MFMA32H(HC(pd[0]), HC(S[0]), Y[0]);
        bv[0] = wvl[1 * 64];
        MX(12); MX(13); CN(5); MX(14); MX(15);
        SB();
        Y[2] = MFMA32(BC(pn[0]), S[0], Y[2]);
        bv[1] = wvl[3 * 64];
        CN(6); CN(7);
        SB();
      }
      {
        f32x16& Gc = G1;
        sl = slr & 31u;
        Y[0] = MFMA32H(HC(pd[1]), HC(S[1]), Y[0]);
        bv[2] = wvl[5 * 64];
        St[0] = ohl[sl * 4 + h * 2 + 0];
        E1(0); E1(1); E1(2); E1(3);
        SB();
        Y[2] = MFMA32(BC(pn[1]), S[1], Y[2]);
        bv[3] = wvl[7 * 64];
        St[1] = ohl[sl * 4 + h * 2 + 1];
        E1(4); CV(0); E1(5); CV(1); PA(0);
        SB();
        V = MFMA32(as[0], bv[0], zero16);
        const u32x4 xo = *(const LAS u32x4*)(lxp + (i + 2) * 32);
        const unsigned eo = lep[(i + 2) * 32];
        E1(6); PA(1); RL(0); E1(7); CV(2);
        SB();
        V = MFMA32(as[1], bv[1], V);
        RH(0); E1(8); PA(2); E1(9); CV(3);
        SB();
        V = MFMA32(as[2], bv[2], V);
        RL(1); E1(10); PA(3); E1(11); CV(4);
        SB();
        V = MFMA32(as[3], bv[3], V);
        RH(1); E1(12); PA(4); E1(13); CV(5);
        SB();
        G0 = MFMA32(St[0], XAf[0][0], zero16);
        asm volatile("s_waitcnt vmcnt(0)" ::: "memory");
        ae = *(const LAS bf16x8*)(ring + ea0);
        as[0] = *(const LAS bf16x8*)(ring + xa0);
        as[1] = *(const LAS bf16x8*)(ring + xa0 + 16);
        as[2] = *(const LAS bf16x8*)(ring + xa0 + 32);
        as[3] = *(const LAS bf16x8*)(ring + xa0 + 48);
        RL(2); E1(14); PA(5); E1(15); CV(6);
        SB();
        G0 = MFMA32(St[1], XAf[1][0], G0);
        ga[0] = XG(0);
        PIN(ga[0]);
        RH(2); PA(6); RL(3); CV(7);
        SB();
        asm volatile("" : "+v"(as[0]), "+v"(as[1]), "+v"(as[2]), "+v"(as[3]), "+v"(ae) :: "memory");
        G0 = MFMA32(ae, wC[0], G0);
        GLDS(ga[0], rx, ring_a);
        ga[1] = XG(1);
        PIN(ga[1]);
        RH(3); PA(7); MX(0); RL(4); MX(1); MX(2);
        SB();
        G0 = MFMA32(as[0], wB[0][0], G0);
        GLDS(ga[1], rx, ring_a + XB);
        ga[2] = XG(2);
        PIN(ga[2]);
        RH(4); MX(3); RL(5); CN(0); MX(4);
        SB();
        G0 = MFMA32(as[1], wB[1][0], G0);
        GLDS(ga[2], rx, ring_a + 2 * XB);
        ga[3] = XG(3);
        PIN(ga[3]);
        RH(5); MX(5); RL(6); CN(1); MX(6);
        SB();
        G0 = MFMA32(as[2], wB[2][0], G0);
        GLDS(ga[3], rx, ring_a + 3 * XB);
        ga[4] = (eo & ~31u) | ec0;
        PIN(ga[4]);
        RH(6); MX(7); RL(7); CN(2); MX(8);
        SB();
        G0 = MFMA32(as[3], wB[3][0], G0);
        GLDS(ga[4], re, ring_a + 4 * XB);
        RH(7); MX(9); CN(3); MX(10); MX(11); CN(4);
        SB();
        Y[1] = MFMA32H(HC(pd[0]), HC(S[0]), Y[1]);
        bv[0] = wvl[0 * 64];
        MX(12); MX(13); CN(5); MX(14); MX(15);
        SB();
        Y[3] = MFMA32(BC(pn[0]), S[0], Y[3]);
        bv[1] = wvl[2 * 64];
        CN(6); CN(7);
        SB();
      }
    }
    *(volatile LAS int*)pown = 0;
    Y[1] = MFMA32H(HC(pd[1]), HC(S[1]), Y[1]);
#define TP(P, j) P[(j) >> 2][(j) & 3]
#define pm (pn + 1)
#define TE(G, i) G[i] = __builtin_amdgcn_exp2f(G[i])
#define TCV(P, G, j) TP(P, j) = pack_f16(G[2 * (j)], G[2 * (j) + 1])
#define TPA(P, j) TP(P, j) = __builtin_bit_cast(unsigned, __builtin_bit_cast(f16x2, (unsigned)TP(P, j)) + __builtin_bit_cast(f16x2, one15))
#define TRL(P, j) do { unsigned t_ = TP(P, j); asm volatile("v_rcp_f16_sdwa %0, %0 dst_sel:WORD_0 dst_unused:UNUSED_PRESERVE src0_sel:WORD_0" : "+v"(t_)); TP(P, j) = t_; } while (0)
#define TRH(P, j) do { unsigned t_ = TP(P, j); asm volatile("v_rcp_f16_sdwa %0, %0 dst_sel:WORD_1 dst_unused:UNUSED_PRESERVE src0_sel:WORD_1" : "+v"(t_)); TP(P, j) = t_; } while (0)
#define TMX(W, P, i) W[i] = __builtin_fmaf(W[i], (float)__builtin_bit_cast(f16x2, (unsigned)TP(P, (i) >> 1))[(i) & 1], 0.0f)
#define TCN(Q, W, j) TP(Q, j) = pack_bf16(W[2 * (j)], W[2 * (j) + 1])
    {
      LAS unsigned short* ohw = (LAS unsigned short*)(sm + sl * 80 + ohoff);
      const int nl = 1 << (nq - 4 * max(ntiles - 1, 0));
      {
      int hc = nl & 6;
      asm volatile("" : "+s"(hc));
#pragma unroll 1
      for (; hc > 0; hc -= 64) {
        u32x4 qd[2], qn[2];
        bv[2] = wvl[4 * 64];
        TE(G0,0); TE(G0,1); TE(G0,2);
        SB();
        Y[3] = MFMA32(BC(pn[1]), S[1], Y[3]);
        bv[3] = wvl[6 * 64];
        TCV(pd,G0,0); TE(G0,3); TPA(pd,0); TCV(pd,G0,1); TE(G0,4);
        SB();
        G1 = MFMA32(St[0], XAf[0][1], zero16);
        *ohw = (unsigned short)0x4000; asm volatile("" ::: "memory"); S[0] = BC(mine[0]); asm volatile("" ::: "memory"); *ohw = (unsigned short)0;
        TE(G0,5); TRL(pd,0); TPA(pd,1); TCV(pd,G0,2);
        SB();
        G1 = MFMA32(St[1], XAf[1][1], G1);
        TRH(pd,0); TE(G0,6); TE(G0,7);
        SB();
        G1 = MFMA32(ae, wC[1], G1);
        TRL(pd,1); TPA(pd,2); TCV(pd,G0,3); TRH(pd,1);
        SB();
        G1 = MFMA32(as[0], wB[0][1], G1);
        TRL(pd,2); TPA(pd,3); TRH(pd,2);
        SB();
        G1 = MFMA32(as[1], wB[1][1], G1);
        TRL(pd,3); TRH(pd,3);
        SB();
        G1 = MFMA32(as[2], wB[2][1], G1);
        SB();
        G1 = MFMA32(as[3], wB[3][1], G1);
        SB();
        V = MFMA32(as[0], bv[0], zero16);
        bv[0] = wvl[1 * 64];
        SB();
        V = MFMA32(as[1], bv[1], V);
        bv[1] = wvl[3 * 64];
        TE(G1,0); TE(G1,1); TE(G1,2);
        SB();
        V = MFMA32(as[2], bv[2], V);
        bv[2] = wvl[5 * 64];
        TCV(qd,G1,0); TE(G1,3); TPA(qd,0); TCV(qd,G1,1); TE(G1,4);
        SB();
        V = MFMA32(as[3], bv[3], V);
        bv[3] = wvl[7 * 64];
        TE(G1,5); TRL(qd,0); TPA(qd,1); TCV(qd,G1,2);
        SB();
        G0 = MFMA32(as[0], bv[0], zero16);
        TRH(qd,0); TE(G1,6); TE(G1,7);
        SB();
        G0 = MFMA32(as[1], bv[1], G0);
        TRL(qd,1); TPA(qd,2); TCV(qd,G1,3); TRH(qd,1);
        SB();
        G0 = MFMA32(as[2], bv[2], G0);
        TRL(qd,2); TPA(qd,3); TMX(V,pd,0); TRH(qd,2);
        SB();
        G0 = MFMA32(as[3], bv[3], G0);
        TRL(qd,3); TMX(V,pd,1); TMX(V,pd,2); TRH(qd,3);
        SB();
        Y[0] = MFMA32H(HC(pd[0]), HC(S[0]), Y[0]);
        TCN(qn,V,0); TMX(V,pd,3); TMX(V,pd,4); TCN(qn,V,1); TMX(V,pd,5); TMX(V,pd,6);
        SB();
        Y[1] = MFMA32H(HC(qd[0]), HC(S[0]), Y[1]);
        TCN(qn,V,2); TMX(V,pd,7); TMX(G0,qd,0); TCN(qn,V,3); TMX(G0,qd,1); TMX(G0,qd,2);
        SB();
        Y[2] = MFMA32(BC(qn[0]), S[0], Y[2]);
        TCN(pm,G0,0); TMX(G0,qd,3); TMX(G0,qd,4); TCN(pm,G0,1); TMX(G0,qd,5); TMX(G0,qd,6); TCN(pm,G0,2); TMX(G0,qd,7); TCN(pm,G0,3);
        S[1] = S[0];
        SB();
        asm volatile("" : "=v"(St[0]), "=v"(St[1]), "=v"(ae), "=v"(bv[0]), "=v"(bv[1]), "=v"(G0), "=v"(as[0]), "=v"(as[1]), "=v"(as[2]), "=v"(as[3]));
      }
      }
    }
    Y[3] = MFMA32(BC(pn[1]), S[1], Y[3]);
    if (__builtin_expect(!more, 1)) break;
  }
  {
    bf16x8 fa[2][6];
#pragma unroll
    for (int k = 0; k < 4; ++k) as[k] = *(const LAS bf16x8*)(smem + M_XG + grp * (4 * XB) + xa0 + 16 * k);
    fa[0][0] = fa[1][0] = ohl[r * 4 + h * 2];
    u32x4 t1 = __builtin_bit_cast(u32x4, ohl[r * 4 + h * 2 + 1]);
#pragma unroll
    for (int k = 0; k < 8; ++k) fa[k & 1][2 + (k >> 1)] = wvl[(8 + k) * 64];
    LAS char* zw = (LAS char*)wbase + r * 144 + h * 64;
    u32x4 o;
    f32x16 X[2], U[2];
#define FR(ct, i) do { float t_ = __builtin_amdgcn_rcpf(Y[ct][i]); PIN(t_); Y[ct][i] = t_; } while (0)
#define FM(ct, m) do { if ((m) == 0) X[ct] = MFMA32(fa[ct][0], as[2 * (ct)], zero16);          \
                       else if ((m) == 1) X[ct] = MFMA32(fa[ct][1], as[2 * (ct) + 1], X[ct]);   \
                       else U[ct] = MFMA32(fa[ct][m], as[(m) - 2], (m) == 2 ? X[ct] : U[ct]); } while (0)
#define FZ(ct, i) __builtin_amdgcn_fmed3f(__builtin_fmaf(Y[2 + (ct)][i], Y[ct][i], U[ct][i]), X[ct][i], 3.4028234e38f)
#define FO(q, j) do { o[j] = pack_bf16(FZ((q) >> 1, ((q) & 1) * 8 + 2 * (j)), FZ((q) >> 1, ((q) & 1) * 8 + 2 * (j) + 1)); \
      if ((j) == 3) *(LAS u32x4*)(zw + (q) * 16) = o; } while (0)
#pragma unroll
    for (int i = 0; i < 8; ++i) FR(0, i);
    SB();
#pragma unroll
    for (int m = 0; m < 8; ++m) {
      FM(m / 6, m % 6);
      SB();
      if (m == 0) {
        t1[3] &= (h && r != 30) ? 0xFFFF0000u : 0xFFFFFFFFu;
        fa[0][1] = fa[1][1] = BC(t1);
      }
#pragma unroll
      for (int i = 8 + 3 * m; i < 11 + 3 * m; ++i) FR(i >> 4, i & 15);
      SB();
    }
    asm volatile("s_waitcnt vmcnt(0)" ::: "memory");
    FM(1, 2); SB(); FO(0, 0); SB();
    FM(1, 3); SB(); FO(0, 1); SB();
    FM(1, 4); SB(); FO(0, 2); SB();
    FM(1, 5); SB(); FO(0, 3); FO(1, 0); FO(1, 1); FO(1, 2); FO(1, 3); SB();
    FO(2, 0); FO(2, 1); FO(2, 2); FO(2, 3); FO(3, 0); FO(3, 1); FO(3, 2); FO(3, 3);
  }
  const LAS char* zl = (const LAS char*)wbase;
  u32x4 o[4];
#pragma unroll
  for (int q = 0; q < 4; ++q) o[q] = *(const LAS u32x4*)(zl + (q * 8 + (lane >> 3)) * 144 + (lane & 7) * 16);
#pragma unroll
  for (int q = 0; q < 4; ++q) {
    const int row = q * 8 + (lane >> 3);
    __builtin_amdgcn_raw_buffer_store_b128(o[q], SRD(out, N * 128),
                                           row < nodes ? (n0 + 8 * row) * 128 + (lane & 7) * 16 : 0x7FFFFFF0, 0, 17);
  }
}

extern "C" void kernel_launch(void* const* d_in, const int* in_sizes, int n_in,
                              void* d_out, int out_size, void* d_ws, size_t ws_size,
                              hipStream_t stream) {
  const unsigned short* x = (const unsigned short*)d_in[0];
  const unsigned short* eattr = (const unsigned short*)d_in[1];
  const int* src = (const int*)d_in[2];
  const int* dst = (const int*)d_in[3];
  const unsigned short* wa = (const unsigned short*)d_in[4];
  const unsigned short* wb = (const unsigned short*)d_in[5];
  const unsigned short* wc = (const unsigned short*)d_in[6];
  const unsigned short* wu = (const unsigned short*)d_in[7];
  const unsigned short* wv = (const unsigned short*)d_in[8];
  unsigned short* out = (unsigned short*)d_out;
  const int N = in_sizes[0] / 64;
  const int E = in_sizes[2];
  const int nb = (N + NPB - 1) / NPB;

  char* ws = (char*)d_ws;
  k_part<<<NCH, 1024, 0, stream>>>(src, dst, E, nb, (unsigned*)(ws + W_TC), (unsigned*)(ws + W_OVH), (u32x4*)(ws + W_OV),
                                   wa, wb, wc, wu, wv, (unsigned short*)(ws + W_WP));
  k_main<<<nb, 512, 0, stream>>>(x, eattr, ws, E, N, out);
}
```

```cpp
#include <hip/hip_runtime.h>
#include <hip/hip_bf16.h>
#include <stdint.h>

using bf16x8 = __attribute__((ext_vector_type(8))) short;
using f16x8 = __attribute__((ext_vector_type(8))) _Float16;
using f16x2 = __attribute__((ext_vector_type(2))) _Float16;
using f32x16 = __attribute__((ext_vector_type(16))) float;
using u32x4 = __attribute__((ext_vector_type(4))) unsigned;
using u32x2 = __attribute__((ext_vector_type(2))) unsigned;
using i32x4 = __attribute__((ext_vector_type(4))) int;
using f32x4 = __attribute__((ext_vector_type(4))) float;

#define MFMA32(a, b, c) __builtin_amdgcn_mfma_f32_32x32x16_bf16((a), (b), (c), 0, 0, 0)
#define MFMA32H(a, b, c) __builtin_amdgcn_mfma_f32_32x32x16_f16((a), (b), (c), 0, 0, 0)
#define LAS __attribute__((address_space(3)))
#if defined(__HIP_DEVICE_COMPILE__)
#define NOPK __attribute__((target("no-packed-fp32-ops")))
#else
#define NOPK
#endif
#define SRD(p, bytes) __builtin_amdgcn_make_buffer_rsrc((void*)(p), (short)0, (int)(bytes), 0x00020000)
#define LDS_ADD(p, v) __hip_atomic_fetch_add((p), (v), __ATOMIC_RELAXED, __HIP_MEMORY_SCOPE_WORKGROUP)

#define CST(p, v) __builtin_amdgcn_raw_buffer_store_b128((v), rc, (p) * 4, 0, 17)
#define WALL 1
constexpr int GPB = 8;
constexpr int NPB = 196;
constexpr int MAXB = 256;
constexpr int NCH = 256;
constexpr int CELL = 32;
constexpr int CAP = 28;
constexpr int CSTR = 36;
constexpr int OVCAP = 4096;
constexpr int NFRAG = 34;
constexpr size_t W_TC = 0;
constexpr size_t W_OVH = W_TC + (size_t)MAXB * NCH * CELL * 4;
constexpr size_t W_WP = W_OVH + NCH * 4;
constexpr size_t W_OV = W_WP + NFRAG * 1024;
constexpr float NLOG2E = -1.44269504088896340736f;

__global__ __launch_bounds__(1024) void k_part(const int* __restrict__ src, const int* __restrict__ dst,
                                               int E, int nb,
                                               unsigned* __restrict__ TC,
                                               unsigned* __restrict__ OVH, u32x4* __restrict__ OV,
                                               const unsigned short* __restrict__ wa,
                                               const unsigned short* __restrict__ wb,
                                               const unsigned short* __restrict__ wc,
                                               const unsigned short* __restrict__ wu,
                                               const unsigned short* __restrict__ wv,
                                               unsigned short* __restrict__ wpack) {
  __shared__ __attribute__((aligned(16))) unsigned cT[(MAXB + 1) * CSTR];
  __shared__ int cnt[MAXB + 4];
  const int tid = threadIdx.x, lane = tid & 63;
  const int rnd = __builtin_amdgcn_readfirstlane(tid >> 6);
  const int c = blockIdx.x;
  const int e0 = (rnd * NCH + c) * 256 + lane * 4;
  const auto rd = SRD(dst, E * 4), rs = SRD(src, E * 4);

  const i32x4 dd = __builtin_bit_cast(i32x4, __builtin_amdgcn_raw_buffer_load_b128(rd, e0 * 4, 0, 0));
  const i32x4 ss = __builtin_bit_cast(i32x4, __builtin_amdgcn_raw_buffer_load_b128(rs, e0 * 4, 0, 0));
  unsigned wv8[8];
  if (rnd == 15 && c < NFRAG) {
    const int nn = ((lane >> 2) & 1) * 32 + (c & 1) * 16 + ((lane >> 3) & 3) * 4 + (lane & 3), h = lane >> 5;
    const unsigned short* p;
    if (c < 24) {
      const int m = c >> 3, t = (c >> 1) & 3;
      p = ((m == 0) ? wb : (m == 1) ? wv : wa) + (32 * h + 8 * t) * 64 + nn;
    } else if (c < 26) {
      p = wc + (8 * h) * 64 + nn;
    } else {
      p = wu + (32 * h + 8 * ((c - 26) >> 1)) * 64 + nn;
    }
#pragma unroll
    for (int j = 0; j < 8; ++j) wv8[j] = p[j * 64];
  }
#define CPN 88
  u32x4 t;
  if (rnd == 14 && c < CPN) {
    unsigned long long pc;
    int rel;
    asm volatile("s_getpc_b64 %0\n\ts_mov_b32 %1, _Z6k_mainPKtS0_PKciiPt@rel32@lo+4" : "=s"(pc), "=s"(rel));
    pc += (long long)rel;
    asm volatile("global_load_dwordx4 %0, %1, %2" : "=v"(t) : "v"(min((c >> 3) * 1024 + lane * 16, 10800)), "s"(pc) : "memory");
  }
  if (tid < 2 * MAXB) *reinterpret_cast<u32x2*>(cT + (tid >> 1) * CSTR + (tid & 1) * 16) = u32x2{0u, 0u};
  else if (tid < 3 * MAXB + 4) cnt[tid - 2 * MAXB] = 0;
  __syncthreads();
  if ((rnd * NCH + c) * 256 < E) {
    int bk[4], rk[4];
#pragma unroll
    for (int k = 0; k < 4; ++k) {
      const bool v = e0 + k < E;
      bk[k] = v ? (int)((unsigned)dd[k] / (unsigned)NPB) : MAXB + 1;
      rk[k] = LDS_ADD(&cnt[bk[k]], 1);
    }
    unsigned w[4];
    bool ovf = false;
#pragma unroll
    for (int k = 0; k < 4; ++k) {
      const unsigned s = (unsigned)(dd[k] - bk[k] * NPB);
      w[k] = ((unsigned)ss[k] << 16) | ((s & 7u) << 13) | ((unsigned)(lane * 4 + k) << 5) | (s >> 3);
      const int d = (rk[k] + (rk[k] < CAP / 2 ? 2 : 4)) & 31;
      unsigned* cp = cT + (rk[k] < CAP ? min(bk[k], MAXB) : MAXB) * CSTR;
      cp[d] = w[k];
      __hip_atomic_fetch_or(cp + (d & 16) + ((d >> 3) & 1), (unsigned)rnd << (4 * (d & 7)),
                            __ATOMIC_RELAXED, __HIP_MEMORY_SCOPE_WORKGROUP);
      ovf |= rk[k] >= CAP;
    }
    if (__builtin_amdgcn_ballot_w64(ovf) != 0) {
#pragma unroll 1
      for (int k = 0; k < 4; ++k) {
        const int bq = k == 0 ? bk[0] : k == 1 ? bk[1] : k == 2 ? bk[2] : bk[3];
        const int rq = k == 0 ? rk[0] : k == 1 ? rk[1] : k == 2 ? rk[2] : rk[3];
        const unsigned wq = k == 0 ? w[0] : k == 1 ? w[1] : k == 2 ? w[2] : w[3];
        if (bq < MAXB && rq >= CAP) {
          const int o = LDS_ADD(&cnt[MAXB], 1);
          OV[c * OVCAP + o] = u32x4{wq >> 9, ((unsigned)(e0 + k) << 5) | (wq & 31u), (unsigned)bq, 0u};
        }
      }
    }
  }
  asm volatile("s_waitcnt lgkmcnt(0)\n\ts_barrier" ::: "memory");
  {
    const auto rc = SRD(TC, MAXB * NCH * CELL * 4);
    const int p = tid & 7;
    const int first = (CAP / 2) * (p >> 2) + 4 * (p & 3) - 2;
    const int novf = cnt[MAXB];
#pragma unroll
    for (int q = 0; q < 2; ++q) {
      const int b = (tid >> 3) + q * 128;
      const int m = cnt[b];
      u32x4 v = *reinterpret_cast<const u32x4*>(cT + b * CSTR + 4 * p);
      if ((p & 3) == 0) v[0] |= (unsigned)(m > CAP ? min(CAP / 2 + novf, 255) : min(max(m - first - 2, 0), CAP / 2));
      if (b < nb && (WALL || (p & 3) == 0 || first < m)) CST((b * NCH + c) * CELL + 4 * p, v);
    }
    if (tid == 0) __builtin_amdgcn_raw_buffer_store_b32((unsigned)novf, SRD(OVH, NCH * 4), c * 4, 0, 17);
  }
  if (rnd == 15 && c < NFRAG) {
    if (c < 8 || c == 24 || c == 25) {
#pragma unroll
      for (int j = 0; j < 8; ++j) {
        const unsigned u = __float_as_uint(__uint_as_float(wv8[j] << 16) * NLOG2E);
        wv8[j] = (u + 0x7FFFu + ((u >> 16) & 1u)) >> 16;
      }
    }
    const u32x4 v = {wv8[0] | (wv8[1] << 16), wv8[2] | (wv8[3] << 16), wv8[4] | (wv8[5] << 16), wv8[6] | (wv8[7] << 16)};
    __builtin_amdgcn_raw_buffer_store_b128(v, SRD(wpack, NFRAG * 1024), c * 1024 + lane * 16, 0, 17);
  }
  asm volatile("s_waitcnt vmcnt(0)" :: "v"(t) : "memory");
}

__device__ __forceinline__ NOPK unsigned pack_bf16(float lo, float hi) {
  typedef __attribute__((ext_vector_type(2))) float f2;
  typedef __attribute__((ext_vector_type(2))) __bf16 b2;
  f2 v = {lo, hi};
  b2 r = __builtin_convertvector(v, b2);
  return __builtin_bit_cast(unsigned, r);
}
__device__ __forceinline__ NOPK unsigned pack_f16(float lo, float hi) {
  typedef __attribute__((ext_vector_type(2))) float f2;
  f2 v = {lo, hi};
  f16x2 r = __builtin_convertvector(v, f16x2);
  return __builtin_bit_cast(unsigned, r);
}
#define WGSYNC() do { __builtin_amdgcn_fence(__ATOMIC_RELEASE, "workgroup"); __builtin_amdgcn_s_barrier(); \
                      __builtin_amdgcn_fence(__ATOMIC_ACQUIRE, "workgroup"); } while (0)
#define U2F(u) __builtin_bit_cast(float, (unsigned)(u))
constexpr int XB = 1040;
constexpr int RING_SLOT = 4 * XB + 1024;
constexpr int LCAP = 576;
constexpr int LPAD = LCAP + 96;
constexpr int SSCR = 32 * 80;
constexpr int WAVE_LDS = RING_SLOT;
constexpr int M_OH = 0;
constexpr int M_CNT = M_OH + 2048;
constexpr int M_ST = M_CNT + 256;
constexpr int M_WL = M_ST + 2048;
constexpr int M_SC = M_WL + 16 * 1024;
constexpr int M_LX = M_SC + GPB * SSCR;
constexpr int LED = GPB * LPAD;
constexpr int M_WV = M_LX + 2 * LED * 4;
constexpr int M_XG = M_WV + GPB * WAVE_LDS;
constexpr int MAIN_LDS = M_XG + GPB * 4 * XB;
static_assert(M_LX % 16 == 0 && LED % 64 == 0 && LCAP % 32 == 0 && NCH * CAP * 4 <= M_LX - M_WL &&
              NCH * (CAP + 8) * 4 <= GPB * WAVE_LDS, "lists");
static_assert(MAIN_LDS <= 160 * 1024, "LDS");

__global__ __launch_bounds__(512, 2) NOPK void k_main(const unsigned short* __restrict__ x,
                                                      const unsigned short* __restrict__ eattr,
                                                      const char* __restrict__ wsp, int E,
                                                      int N, unsigned short* __restrict__ out) {
  __shared__ __attribute__((aligned(256))) char smem[MAIN_LDS];
  asm volatile("" :: "s"(x), "s"(eattr), "s"(wsp), "s"(E), "s"(N), "s"(out));
  const unsigned* TC = reinterpret_cast<const unsigned*>(wsp + W_TC);
  const unsigned* OVH = reinterpret_cast<const unsigned*>(wsp + W_OVH);
  const u32x4* OV = reinterpret_cast<const u32x4*>(wsp + W_OV);
  const char* wpack = wsp + W_WP;
  const int tid = threadIdx.x, lane = tid & 63;
  const int wave = __builtin_amdgcn_readfirstlane(tid >> 6);
  const int r = lane & 31, h = lane >> 5;
  const int b = blockIdx.x;

  const int pc = lane & 7;
  u32x4 tv[4];
  const unsigned* cp0 = TC + ((size_t)b * NCH + wave * 32) * CELL;
#define CLD(k) asm volatile("global_load_dwordx4 %0, %1, %2 offset:%3" : "=v"(tv[k]) : "v"(lane * 16), "s"(cp0), "n"((k) * 1024) : "memory")
  CLD(0);
  CLD(1);
#define GLDS(vo, rs, mv) asm volatile("s_mov_b32 m0, %2\n\ts_nop 0\n\tbuffer_load_dwordx4 %0, %1, 0 offen lds" \
                                      :: "v"(vo), "s"(rs), "s"(mv) : "memory")
  const auto rx = SRD(x, N * 128);
  const auto re = SRD(eattr, E * 32);
  const unsigned xc0 = (unsigned)(((lane & 7) ^ ((lane >> 2) & 4)) * 16);
  const unsigned lb = (unsigned)(size_t)(LAS char*)smem;
  LAS int* cnt = (LAS int*)(smem + M_CNT);
  if (tid < 32) {
    const int mid = lb + M_LX + (tid >> 2) * (LPAD * 4) + LCAP * 2;
    cnt[tid] = (tid & 3) == 0 ? mid : (tid & 3) == 3 ? mid - 4 : (tid & 3) == 2 ? 1023 : 0;
  }
  __builtin_amdgcn_s_setprio(1);
  {
    unsigned* oh = reinterpret_cast<unsigned*>(smem + M_OH);
    const int i = tid;
    const int sl = i >> 4, hh = (i >> 3) & 1, ks = (i >> 2) & 1, jp = i & 3;
    const int j0 = 2 * jp, j1 = 2 * jp + 1;
    const int nd0 = 16 * ks + 8 * (j0 >> 2) + 4 * hh + (j0 & 3);
    const int nd1 = 16 * ks + 8 * (j1 >> 2) + 4 * hh + (j1 & 3);
    oh[i] = (sl == nd0 || nd0 == 30 ? 0x3F80u : 0u) | (sl == nd1 ? 0x3F800000u : 0u);
  }
  asm volatile("s_waitcnt lgkmcnt(0)\n\ts_barrier" ::: "memory");
  CLD(2);
  CLD(3);
  auto own_rows = [&]() NOPK __attribute__((always_inline)) {
    const int nw = (min(NPB - wave, N - b * NPB - wave) + 7) >> 3;
#pragma unroll
    for (int k = 0; k < 4; ++k) {
      const int s = (lane >> 3) * 4 + k;
      const unsigned xo = s < nw ? (unsigned)(b * NPB + wave + 8 * s) << 7 : 0xFFFFFF80u;
      GLDS(xo | xc0, rx, lb + M_XG + wave * (4 * XB) + k * XB);
    }
  };
  const LAS bf16x8* wl = (const LAS bf16x8*)(smem + M_WL);
  const LAS bf16x8* ohl = (const LAS bf16x8*)(smem + M_OH);
#define LP(a) ((LAS unsigned*)(size_t)(unsigned)(a))
  char* wbase = smem + M_WV + wave * WAVE_LDS;
  const unsigned ring_a = (unsigned)(size_t)(LAS char*)wbase;
  {
    unsigned long long vm[16];
    unsigned ad[16], xe[16], hw[4], mk[4];
    {
      constexpr int k = 0;
      asm volatile("s_waitcnt vmcnt(%1)" : "+v"(tv[k]) : "n"(3) : "memory");
      const unsigned h0 = (unsigned)__builtin_amdgcn_mov_dpp((int)tv[k][0], 0x00, 0xF, 0xF, true);
      const unsigned h1 = (unsigned)__builtin_amdgcn_mov_dpp((int)tv[k][1], 0x00, 0xF, 0xF, true);
      hw[k] = ((lane & 2) ? h1 : h0) >> ((lane & 1) * 16);
      const unsigned m = mk[k] = h0 & 0xFFu;
      unsigned ci[4], st[4];
#pragma unroll
      for (int j = 0; j < 4; ++j) {
        vm[4 * k + j] = __builtin_amdgcn_ballot_w64((unsigned)((lane & 3) * 4 + j - 2) < m);
        xe[4 * k + j] = tv[k][j] >> 9;
        const int t_ = __builtin_amdgcn_sbfe((int)tv[k][j], 18, 1);
        st[j] = (unsigned)(t_ * 8 + 4);
        ci[j] = (xe[4 * k + j] & 0x70u) | (((unsigned)t_ & 12u) | lb);
      }
      asm volatile("s_mov_b64 exec, %4\n\tds_add_rtn_u32 %0, %8, %12 offset:%13\n\t"
                   "s_mov_b64 exec, %5\n\tds_add_rtn_u32 %1, %9, %14 offset:%13\n\t"
                   "s_mov_b64 exec, %6\n\tds_add_rtn_u32 %2, %10, %15 offset:%13\n\t"
                   "s_mov_b64 exec, %7\n\tds_add_rtn_u32 %3, %11, %16 offset:%13\n\t"
                   "s_mov_b64 exec, -1"
                   : "=&v"(ad[4 * k]), "=&v"(ad[4 * k + 1]), "=&v"(ad[4 * k + 2]), "=&v"(ad[4 * k + 3])
                   : "s"(vm[4 * k]), "s"(vm[4 * k + 1]), "s"(vm[4 * k + 2]), "s"(vm[4 * k + 3]),
                     "v"(ci[0]), "v"(ci[1]), "v"(ci[2]), "v"(ci[3]), "v"(st[0]), "n"(M_CNT), "v"(st[1]), "v"(st[2]), "v"(st[3])
                   : "memory");
    }
    {
      constexpr int k = 1;
      asm volatile("s_waitcnt vmcnt(%1)" : "+v"(tv[k]) : "n"(2) : "memory");
      const unsigned h0 = (unsigned)__builtin_amdgcn_mov_dpp((int)tv[k][0], 0x00, 0xF, 0xF, true);
      const unsigned h1 = (unsigned)__builtin_amdgcn_mov_dpp((int)tv[k][1], 0x00, 0xF, 0xF, true);
      hw[k] = ((lane & 2) ? h1 : h0) >> ((lane & 1) * 16);
      const unsigned m = mk[k] = h0 & 0xFFu;
      unsigned ci[4], st[4];
#pragma unroll
      for (int j = 0; j < 4; ++j) {
        vm[4 * k + j] = __builtin_amdgcn_ballot_w64((unsigned)((lane & 3) * 4 + j - 2) < m);
        xe[4 * k + j] = tv[k][j] >> 9;
        const int t_ = __builtin_amdgcn_sbfe((int)tv[k][j], 18, 1);
        st[j] = (unsigned)(t_ * 8 + 4);
        ci[j] = (xe[4 * k + j] & 0x70u) | (((unsigned)t_ & 12u) | lb);
      }
      asm volatile("s_mov_b64 exec, %4\n\tds_add_rtn_u32 %0, %8, %12 offset:%13\n\t"
                   "s_mov_b64 exec, %5\n\tds_add_rtn_u32 %1, %9, %14 offset:%13\n\t"
                   "s_mov_b64 exec, %6\n\tds_add_rtn_u32 %2, %10, %15 offset:%13\n\t"
                   "s_mov_b64 exec, %7\n\tds_add_rtn_u32 %3, %11, %16 offset:%13\n\t"
                   "s_mov_b64 exec, -1"
                   : "=&v"(ad[4 * k]), "=&v"(ad[4 * k + 1]), "=&v"(ad[4 * k + 2]), "=&v"(ad[4 * k + 3])
                   : "s"(vm[4 * k]), "s"(vm[4 * k + 1]), "s"(vm[4 * k + 2]), "s"(vm[4 * k + 3]),
                     "v"(ci[0]), "v"(ci[1]), "v"(ci[2]), "v"(ci[3]), "v"(st[0]), "n"(M_CNT), "v"(st[1]), "v"(st[2]), "v"(st[3])
                   : "memory");
    }
    {
      constexpr int k = 0;
      const unsigned c13 = (unsigned)(wave * 32 + k * 8 + (lane >> 3)) << 13;
      unsigned ee[4];
#pragma unroll
      for (int j = 0; j < 4; ++j) ee[j] = (tv[k][j] & 0x1FFFu) | ((((hw[k] >> (4 * j)) & 15u) << 21) | c13);
      asm volatile("s_waitcnt lgkmcnt(4)\n\t"
                   "s_mov_b64 exec, %0\n\tds_write2st64_b32 %4, %8, %12 offset0:%16 offset1:%17\n\t"
                   "s_mov_b64 exec, %1\n\tds_write2st64_b32 %5, %9, %13 offset0:%16 offset1:%17\n\t"
                   "s_mov_b64 exec, %2\n\tds_write2st64_b32 %6, %10, %14 offset0:%16 offset1:%17\n\t"
                   "s_mov_b64 exec, %3\n\tds_write2st64_b32 %7, %11, %15 offset0:%16 offset1:%17\n\t"
                   "s_mov_b64 exec, -1"
                   :: "s"(vm[4 * k]), "s"(vm[4 * k + 1]), "s"(vm[4 * k + 2]), "s"(vm[4 * k + 3]),
                      "v"(ad[4 * k]), "v"(ad[4 * k + 1]), "v"(ad[4 * k + 2]), "v"(ad[4 * k + 3]),
                      "v"(xe[4 * k]), "v"(xe[4 * k + 1]), "v"(xe[4 * k + 2]), "v"(xe[4 * k + 3]),
                      "v"(ee[0]), "v"(ee[1]), "v"(ee[2]), "v"(ee[3]), "n"(0), "n"(LED / 64)
                   : "memory");
    }
    {
      constexpr int k = 1;
      const unsigned c13 = (unsigned)(wave * 32 + k * 8 + (lane >> 3)) << 13;
      unsigned ee[4];
#pragma unroll
      for (int j = 0; j < 4; ++j) ee[j] = (tv[k][j] & 0x1FFFu) | ((((hw[k] >> (4 * j)) & 15u) << 21) | c13);
      asm volatile("s_waitcnt lgkmcnt(4)\n\t"
                   "s_mov_b64 exec, %0\n\tds_write2st64_b32 %4, %8, %12 offset0:%16 offset1:%17\n\t"
                   "s_mov_b64 exec, %1\n\tds_write2st64_b32 %5, %9, %13 offset0:%16 offset1:%17\n\t"
                   "s_mov_b64 exec, %2\n\tds_write2st64_b32 %6, %10, %14 offset0:%16 offset1:%17\n\t"
                   "s_mov_b64 exec, %3\n\tds_write2st64_b32 %7, %11, %15 offset0:%16 offset1:%17\n\t"
                   "s_mov_b64 exec, -1"
                   :: "s"(vm[4 * k]), "s"(vm[4 * k + 1]), "s"(vm[4 * k + 2]), "s"(vm[4 * k + 3]),
                      "v"(ad[4 * k]), "v"(ad[4 * k + 1]), "v"(ad[4 * k + 2]), "v"(ad[4 * k + 3]),
                      "v"(xe[4 * k]), "v"(xe[4 * k + 1]), "v"(xe[4 * k + 2]), "v"(xe[4 * k + 3]),
                      "v"(ee[0]), "v"(ee[1]), "v"(ee[2]), "v"(ee[3]), "n"(0), "n"(LED / 64)
                   : "memory");
    }
    own_rows();
    {
      constexpr int k = 2;
      asm volatile("s_waitcnt vmcnt(%1)" : "+v"(tv[k]) : "n"(5) : "memory");
      const unsigned h0 = (unsigned)__builtin_amdgcn_mov_dpp((int)tv[k][0], 0x00, 0xF, 0xF, true);
      const unsigned h1 = (unsigned)__builtin_amdgcn_mov_dpp((int)tv[k][1], 0x00, 0xF, 0xF, true);
      hw[k] = ((lane & 2) ? h1 : h0) >> ((lane & 1) * 16);
      const unsigned m = mk[k] = h0 & 0xFFu;
      unsigned ci[4], st[4];
#pragma unroll
      for (int j = 0; j < 4; ++j) {
        vm[4 * k + j] = __builtin_amdgcn_ballot_w64((unsigned)((lane & 3) * 4 + j - 2) < m);
        xe[4 * k + j] = tv[k][j] >> 9;
        const int t_ = __builtin_amdgcn_sbfe((int)tv[k][j], 18, 1);
        st[j] = (unsigned)(t_ * 8 + 4);
        ci[j] = (xe[4 * k + j] & 0x70u) | (((unsigned)t_ & 12u) | lb);
      }
      asm volatile("s_mov_b64 exec, %4\n\tds_add_rtn_u32 %0, %8, %12 offset:%13\n\t"
                   "s_mov_b64 exec, %5\n\tds_add_rtn_u32 %1, %9, %14 offset:%13\n\t"
                   "s_mov_b64 exec, %6\n\tds_add_rtn_u32 %2, %10, %15 offset:%13\n\t"
                   "s_mov_b64 exec, %7\n\tds_add_rtn_u32 %3, %11, %16 offset:%13\n\t"
                   "s_mov_b64 exec, -1"
                   : "=&v"(ad[4 * k]), "=&v"(ad[4 * k + 1]), "=&v"(ad[4 * k + 2]), "=&v"(ad[4 * k + 3])
                   : "s"(vm[4 * k]), "s"(vm[4 * k + 1]), "s"(vm[4 * k + 2]), "s"(vm[4 * k + 3]),
                     "v"(ci[0]), "v"(ci[1]), "v"(ci[2]), "v"(ci[3]), "v"(st[0]), "n"(M_CNT), "v"(st[1]), "v"(st[2]), "v"(st[3])
                   : "memory");
    }
    {
      constexpr int k = 3;
      asm volatile("s_waitcnt vmcnt(%1)" : "+v"(tv[k]) : "n"(4) : "memory");
      const unsigned h0 = (unsigned)__builtin_amdgcn_mov_dpp((int)tv[k][0], 0x00, 0xF, 0xF, true);
      const unsigned h1 = (unsigned)__builtin_amdgcn_mov_dpp((int)tv[k][1], 0x00, 0xF, 0xF, true);
      hw[k] = ((lane & 2) ? h1 : h0) >> ((lane & 1) * 16);
      const unsigned m = mk[k] = h0 & 0xFFu;
      unsigned ci[4], st[4];
#pragma unroll
      for (int j = 0; j < 4; ++j) {
        vm[4 * k + j] = __builtin_amdgcn_ballot_w64((unsigned)((lane & 3) * 4 + j - 2) < m);
        xe[4 * k + j] = tv[k][j] >> 9;
        const int t_ = __builtin_amdgcn_sbfe((int)tv[k][j], 18, 1);
        st[j] = (unsigned)(t_ * 8 + 4);
        ci[j] = (xe[4 * k + j] & 0x70u) | (((unsigned)t_ & 12u) | lb);
      }
      asm volatile("s_mov_b64 exec, %4\n\tds_add_rtn_u32 %0, %8, %12 offset:%13\n\t"
                   "s_mov_b64 exec, %5\n\tds_add_rtn_u32 %1, %9, %14 offset:%13\n\t"
                   "s_mov_b64 exec, %6\n\tds_add_rtn_u32 %2, %10, %15 offset:%13\n\t"
                   "s_mov_b64 exec, %7\n\tds_add_rtn_u32 %3, %11, %16 offset:%13\n\t"
                   "s_mov_b64 exec, -1"
                   : "=&v"(ad[4 * k]), "=&v"(ad[4 * k + 1]), "=&v"(ad[4 * k + 2]), "=&v"(ad[4 * k + 3])
                   : "s"(vm[4 * k]), "s"(vm[4 * k + 1]), "s"(vm[4 * k + 2]), "s"(vm[4 * k + 3]),
                     "v"(ci[0]), "v"(ci[1]), "v"(ci[2]), "v"(ci[3]), "v"(st[0]), "n"(M_CNT), "v"(st[1]), "v"(st[2]), "v"(st[3])
                   : "memory");
    }
    {
      constexpr int k = 2;
      const unsigned c13 = (unsigned)(wave * 32 + k * 8 + (lane >> 3)) << 13;
      unsigned ee[4];
#pragma unroll
      for (int j = 0; j < 4; ++j) ee[j] = (tv[k][j] & 0x1FFFu) | ((((hw[k] >> (4 * j)) & 15u) << 21) | c13);
      asm volatile("s_waitcnt lgkmcnt(4)\n\t"
                   "s_mov_b64 exec, %0\n\tds_write2st64_b32 %4, %8, %12 offset0:%16 offset1:%17\n\t"
                   "s_mov_b64 exec, %1\n\tds_write2st64_b32 %5, %9, %13 offset0:%16 offset1:%17\n\t"
                   "s_mov_b64 exec, %2\n\tds_write2st64_b32 %6, %10, %14 offset0:%16 offset1:%17\n\t"
                   "s_mov_b64 exec, %3\n\tds_write2st64_b32 %7, %11, %15 offset0:%16 offset1:%17\n\t"
                   "s_mov_b64 exec, -1"
                   :: "s"(vm[4 * k]), "s"(vm[4 * k + 1]), "s"(vm[4 * k + 2]), "s"(vm[4 * k + 3]),
                      "v"(ad[4 * k]), "v"(ad[4 * k + 1]), "v"(ad[4 * k + 2]), "v"(ad[4 * k + 3]),
                      "v"(xe[4 * k]), "v"(xe[4 * k + 1]), "v"(xe[4 * k + 2]), "v"(xe[4 * k + 3]),
                      "v"(ee[0]), "v"(ee[1]), "v"(ee[2]), "v"(ee[3]), "n"(0), "n"(LED / 64)
                   : "memory");
    }
    {
      constexpr int k = 3;
      const unsigned c13 = (unsigned)(wave * 32 + k * 8 + (lane >> 3)) << 13;
      unsigned ee[4];
#pragma unroll
      for (int j = 0; j < 4; ++j) ee[j] = (tv[k][j] & 0x1FFFu) | ((((hw[k] >> (4 * j)) & 15u) << 21) | c13);
      asm volatile("s_waitcnt lgkmcnt(4)\n\t"
                   "s_mov_b64 exec, %0\n\tds_write2st64_b32 %4, %8, %12 offset0:%16 offset1:%17\n\t"
                   "s_mov_b64 exec, %1\n\tds_write2st64_b32 %5, %9, %13 offset0:%16 offset1:%17\n\t"
                   "s_mov_b64 exec, %2\n\tds_write2st64_b32 %6, %10, %14 offset0:%16 offset1:%17\n\t"
                   "s_mov_b64 exec, %3\n\tds_write2st64_b32 %7, %11, %15 offset0:%16 offset1:%17\n\t"
                   "s_mov_b64 exec, -1"
                   :: "s"(vm[4 * k]), "s"(vm[4 * k + 1]), "s"(vm[4 * k + 2]), "s"(vm[4 * k + 3]),
                      "v"(ad[4 * k]), "v"(ad[4 * k + 1]), "v"(ad[4 * k + 2]), "v"(ad[4 * k + 3]),
                      "v"(xe[4 * k]), "v"(xe[4 * k + 1]), "v"(xe[4 * k + 2]), "v"(xe[4 * k + 3]),
                      "v"(ee[0]), "v"(ee[1]), "v"(ee[2]), "v"(ee[3]), "n"(0), "n"(LED / 64)
                   : "memory");
    }
    if (__builtin_expect(__builtin_amdgcn_ballot_w64(max(max(mk[0], mk[1]), max(mk[2], mk[3])) > CAP / 2) != 0, 0)) {
#pragma unroll 1
      for (int k = 0; k < 4; ++k) {
        const unsigned m = k == 0 ? mk[0] : k == 1 ? mk[1] : k == 2 ? mk[2] : mk[3];
        if (pc + CAP / 2 < m) {
          const u32x4 v = OV[(wave * 32 + k * 8 + (lane >> 3)) * OVCAP + pc];
          if ((int)v[2] == b) {
            LAS unsigned* p = LP(LDS_ADD((LAS int*)((LAS char*)cnt + (v[0] & 0x70u)), 4));
            p[0] = v[0];
            p[LED] = v[1];
          }
          if (m > CAP / 2 + 8) cnt[1] = 1;
        }
      }
    }
  }
  auto weights = [&]() NOPK __attribute__((always_inline)) {
    const auto rw = SRD(wpack, NFRAG * 1024);
    const unsigned vo = (unsigned)(wave * 1024 + lane * 16);
    GLDS(vo + 16384u, rw, lb + M_SC + 8192 + wave * 1024);
    GLDS(vo, rw, lb + M_SC + wave * 1024);
    asm volatile("s_mov_b32 m0, %2\n\ts_nop 0\n\tbuffer_load_dword %0, %1, 0 offen lds"
                 :: "v"(24576u + (vo >> 2)), "s"(rw), "s"(lb + M_SC + 16384 + wave * 256) : "memory");
    GLDS(vo + 8192u, rw, lb + M_WL + wave * 1024);
    GLDS(vo + 26624u, rw, lb + M_WL + 8192 + wave * 1024);
  };
  weights();
  asm volatile("s_waitcnt lgkmcnt(0)\n\ts_barrier" ::: "memory");
  const int cu = cnt[4 * pc], cd = cnt[4 * pc + 3];
  const int cpc = cu - cd;
  const int cpa = cnt[4 * (lane >> 3)] - cnt[4 * (lane >> 3) + 3];
  const int lo = lb + M_LX + pc * (LPAD * 4);
  int mo0 = cnt[1];
  bool ready = __builtin_amdgcn_ballot_w64(max(cu - LCAP * 4 - lo, lo - 4 - cd) > 0) == 0;
  int grp;
  {
    const unsigned long long gt = __builtin_amdgcn_ballot_w64(cpc * 2 + pc > cpa * 2 + (lane >> 3));
    const unsigned wd = h ? (unsigned)(gt >> 32) : (unsigned)gt;
    const int rk = __builtin_popcount((wd >> (r & 24)) & 0xFFu);
    grp = __builtin_ctzll(__builtin_amdgcn_ballot_w64(rk == (wave < 4 ? wave : 11 - wave))) >> 3;
  }
  asm volatile("" : "+v"(mo0));
  const int gn0 = b * NPB + grp;
  const int nodes = max((min(NPB - grp, N - gn0) + 7) >> 3, 0);
  const int n0 = min(gn0, N - 1);
  ((LAS unsigned*)(smem + M_ST))[tid] = (ready ? 0x80000u : 0xFFFFu) | ((unsigned)(tid & 1) << 16);

  f32x16 Y[4];
#pragma unroll
  for (int q = 0; q < 4; ++q)
#pragma unroll
    for (int i = 0; i < 16; ++i) Y[q][i] = q < 2 ? 1e-6f * 65536.f : 0.f;

  const unsigned ec0 = (unsigned)(((lane & 1) ^ ((lane >> 4) & 1)) * 16);
  const int ohoff = ((((r >> 2) & 1) * 16) + ((r >> 4) * 8) + (((r >> 3) & 1) * 4) + (r & 3)) * 2;
  const int xa0 = (r & 3) * XB + (r >> 2) * 128 + (h ^ ((r >> 3) & 1)) * 64;
  const int ea0 = 4 * XB + r * 32 + ((h ^ ((r >> 3) & 1)) * 16);
#define SB() __builtin_amdgcn_sched_barrier(0)
  bf16x8 wB[4][2], wC[2], XAf[2][2];
  const LAS bf16x8* wvl = wl + lane;
  f32x16 G0, G1, V;
  u32x4 pd[2], pn[2];
  bf16x8 S[2], St[2], as[4], ae, bv[4];
  const f32x16 zero16 = {0.f, 0.f, 0.f, 0.f, 0.f, 0.f, 0.f, 0.f, 0.f, 0.f, 0.f, 0.f, 0.f, 0.f, 0.f, 0.f};
  LAS char* sm = (LAS char*)(smem + M_SC + wave * SSCR);
  LAS u32x4* mine = (LAS u32x4*)(sm + r * 80 + h * 32);
  const LAS char* ring = (const LAS char*)wbase;
  unsigned ga[5];
  unsigned slr;
  int sl;

#define E1(i) Gc[i] = __builtin_amdgcn_exp2f(Gc[i])
#define PD(j) pd[(j) >> 2][(j) & 3]
#define CV(j) PD(j) = pack_f16(Gc[2 * (j)], Gc[2 * (j) + 1])
#define PA(j) PD(j) = __builtin_bit_cast(unsigned, __builtin_bit_cast(f16x2, (unsigned)PD(j)) + __builtin_bit_cast(f16x2, one15))
#define RL(j) do { unsigned t_ = PD(j); asm volatile("v_rcp_f16_sdwa %0, %0 dst_sel:WORD_0 dst_unused:UNUSED_PRESERVE src0_sel:WORD_0" : "+v"(t_)); PD(j) = t_; } while (0)
#define RH(j) do { unsigned t_ = PD(j); asm volatile("v_rcp_f16_sdwa %0, %0 dst_sel:WORD_1 dst_unused:UNUSED_PRESERVE src0_sel:WORD_1" : "+v"(t_)); PD(j) = t_; } while (0)
#define MX(i) V[i] = __builtin_fmaf(V[i], (float)__builtin_bit_cast(f16x2, (unsigned)PD((i) >> 1))[(i) & 1], 0.0f)
#define HC(v) __builtin_bit_cast(f16x8, v)
#define CN(j) pn[(j) >> 2][(j) & 3] = pack_bf16(V[2 * (j)], V[2 * (j) + 1])
#define XG(k) ((xo[k] & ~127u) | xc0)
#define BC(v) __builtin_bit_cast(bf16x8, v)
#define PIN(v) asm volatile("" : "+v"(v))

  const unsigned one15 = 0x02000200u;
  int more, ntiles, nq;
  LAS int* pown = cnt + 4 * wave + 2;
  const LAS int* poth = cnt + 4 * (wave ^ 4) + 2;
  PIN(pown); PIN(poth);
  const LAS unsigned *lxp, *lep, *lsp;
  auto round_begin = [&](int sa, int ea, int mo) NOPK __attribute__((always_inline)) {
    const int s4 = sa & ~15;
    const int cn = (ea - s4) >> 2;
    more = __builtin_amdgcn_readfirstlane(mo);
    ntiles = (cn + 31) >> 5;
    nq = (cn + 7) >> 3;
    lxp = LP(s4) + (lane >> 3) * 4;
    lep = LP(s4) + LED + (lane >> 1);
    lsp = LP(s4) + LED + r;
    LAS unsigned* w0 = LP(ea) + lane;
    LAS unsigned* w1 = (h && s4 + 4 * r < sa) ? LP(s4) + r : LP(ea) + 64 + r;
    w0[0] = 0xFFFFFF80u;
    w0[LED] = 0xFFFFFFFFu;
    w1[0] = 0xFFFFFF80u;
    w1[LED] = 0xFFFFFFFFu;
  };
  auto gather = [&](int t, bool issue) NOPK __attribute__((always_inline)) {
    const u32x4 xo = *(const LAS u32x4*)(lxp + t * 32);
    const unsigned eo = lep[t * 32];
    ga[0] = XG(0); ga[1] = XG(1); ga[2] = XG(2); ga[3] = XG(3);
    ga[4] = (eo & ~31u) | ec0;
    if (issue) {
      GLDS(ga[0], rx, ring_a); GLDS(ga[1], rx, ring_a + XB); GLDS(ga[2], rx, ring_a + 2 * XB);
      GLDS(ga[3], rx, ring_a + 3 * XB); GLDS(ga[4], re, ring_a + 4 * XB);
    }
  };
  if (ready) {
    round_begin(__builtin_amdgcn_readlane(cd, grp) + 4, __builtin_amdgcn_readlane(cu, grp), mo0);
    if (wave < 4) gather(0, true);
  } else {
    weights();
  }
  asm volatile("s_cmp_lg_u32 %0, 0\n\ts_cbranch_scc1 1f\n\ts_waitcnt vmcnt(0)\n1:\n\ts_waitcnt vmcnt(5)\n\ts_barrier"
               :: "s"(__builtin_amdgcn_readfirstlane((ready && wave < 4) ? 1 : 0)) : "memory", "scc");
  {
      const LAS bf16x8* ws = (const LAS bf16x8*)(smem + M_SC) + lane;
      bf16x8 xg[4], wA[8];
#pragma unroll
      for (int k = 0; k < 4; ++k) xg[k] = *(const LAS bf16x8*)(smem + M_XG + grp * (4 * XB) + xa0 + 16 * k);
#pragma unroll
      for (int k = 0; k < 8; ++k) wA[k] = ws[(8 + k) * 64];
#pragma unroll
      for (int k = 0; k < 8; ++k) wB[k >> 1][k & 1] = ws[k * 64];
      wC[0] = ws[16 * 64];
      wC[1] = ws[17 * 64];
      SB();
      asm volatile("s_waitcnt lgkmcnt(0)\n\ts_barrier"
                   : "+v"(wB[0][0]), "+v"(wB[0][1]), "+v"(wB[1][0]), "+v"(wB[1][1]), "+v"(wB[2][0]), "+v"(wB[2][1]),
                     "+v"(wB[3][0]), "+v"(wB[3][1]), "+v"(wC[0]), "+v"(wC[1]),
                     "+v"(xg[0]), "+v"(xg[1]), "+v"(xg[2]), "+v"(xg[3]), "+v"(wA[0]), "+v"(wA[1]), "+v"(wA[2]),
                     "+v"(wA[3]), "+v"(wA[4]), "+v"(wA[5]), "+v"(wA[6]), "+v"(wA[7]) :: "memory");
      SB();
      if (ready) {
        if (wave >= 4) gather(0, true);

      }
      SB();
#pragma unroll
      for (int ct = 0; ct < 2; ++ct) {
        f32x16 A = zero16;
#pragma unroll
        for (int k = 0; k < 4; ++k) A = MFMA32(xg[k], wA[k * 2 + ct], A);
#pragma unroll
        for (int s = 0; s < 2; ++s) {
          u32x4 p;
#pragma unroll
          for (int j = 0; j < 4; ++j) p[j] = pack_bf16(A[8 * s + 2 * j] * NLOG2E, A[8 * s + 2 * j + 1] * NLOG2E);
          if (s == 1 && h) p[3] = 0xC170u;
          XAf[s][ct] = BC(p);
        }
      }
      const u32x4 z4 = {0u, 0u, 0u, 0u};
      mine[0] = z4;
      mine[1] = z4;
      SB();
    }
  for (;;) {
   if (__builtin_expect(!ready, 0)) {
    asm volatile("s_waitcnt vmcnt(0)" ::: "memory");
    WGSYNC();
    {
      int t2 = (int)__builtin_amdgcn_mbcnt_hi(~0u, __builtin_amdgcn_mbcnt_lo(~0u, 0u));
      asm volatile("" : "+v"(t2));
      const int l2 = t2;
      t2 += wave * 64;
      if (t2 < 32) cnt[t2] = 0;
      WGSYNC();
      auto put = [&](unsigned t, unsigned e) NOPK -> bool {
        const unsigned gg = (t >> 4) & 7u;
        const int ad = LDS_ADD(&cnt[4 * gg], 4) >> 2;
        if (ad >= LCAP) return false;
        LAS unsigned* p = LP(lb + M_LX) + gg * LPAD + ad;
        p[0] = t;
        p[LED] = e;
        return true;
      };
      LAS unsigned* stp = (LAS unsigned*)(smem + M_ST) + t2;
      unsigned pe = *stp & 0xFFFFu, ocur = *stp >> 16;
#pragma unroll 1
      for (int j = 0; j < 16; ++j) {
        if ((pe >> j) & 1u) {
          const int cc = wave * 32 + (j >> 2) * 8 + (l2 >> 3);
          const unsigned* cp = TC + ((size_t)b * NCH + cc) * CELL;
          const int d = (l2 & 7) * 4 + (j & 3);
          const unsigned w = cp[d];
          const unsigned nib = (cp[(d & 16) + ((d >> 3) & 1)] >> (4 * (d & 7))) & 15u;
          if ((unsigned)((d & 15) - 2) >= (cp[d & 16] & 0xFFu) ||
              put(w >> 9, (w & 0x1FFFu) | (nib << 21) | ((unsigned)cc << 13))) pe &= ~(1u << j);
        }
      }
      const unsigned on = OVH[t2 >> 1];
      const size_t ob = (size_t)(t2 >> 1) * OVCAP;
#pragma unroll 1
      for (; ocur < on; ocur += 2) {
        const u32x4 v = OV[ob + ocur];
        if ((int)v[2] == b && !put(v[0], v[1])) break;
      }
      if (pe != 0u || ocur < on) cnt[1] = 1;
      *stp = pe | (ocur << 16);
    }
    WGSYNC();
    round_begin(lb + M_LX + grp * (LPAD * 4), lb + M_LX + grp * (LPAD * 4) + min(__builtin_amdgcn_readfirstlane(cnt[4 * grp]), LCAP * 4), cnt[1]);
    gather(0, true);
   }
    gather(1, false);
    ready = false;
    sl = lsp[0] & 31u;
    St[0] = ohl[sl * 4 + h * 2 + 0];
    St[1] = ohl[sl * 4 + h * 2 + 1];
    bv[0] = wvl[0 * 64];
    bv[1] = wvl[2 * 64];
    asm volatile("s_waitcnt vmcnt(0)" ::: "memory");
#pragma unroll
    for (int k = 0; k < 4; ++k) as[k] = *(const LAS bf16x8*)(ring + xa0 + 16 * k);
    ae = *(const LAS bf16x8*)(ring + ea0);
    G0 = MFMA32(St[0], XAf[0][0], zero16);
    G0 = MFMA32(St[1], XAf[1][0], G0);
    SB();
    asm volatile("" : "+v"(as[0]), "+v"(as[1]), "+v"(as[2]), "+v"(as[3]), "+v"(ae) :: "memory");
    G0 = MFMA32(ae, wC[0], G0);
    GLDS(ga[0], rx, ring_a);
    SB();
    G0 = MFMA32(as[0], wB[0][0], G0);
    GLDS(ga[1], rx, ring_a + XB);
    SB();
    G0 = MFMA32(as[1], wB[1][0], G0);
    GLDS(ga[2], rx, ring_a + 2 * XB);
    SB();
    G0 = MFMA32(as[2], wB[2][0], G0);
    GLDS(ga[3], rx, ring_a + 3 * XB);
    SB();
    G0 = MFMA32(as[3], wB[3][0], G0);
    GLDS(ga[4], re, ring_a + 4 * XB);
    pd[1] = u32x4{0u, 0u, 0u, 0u};
    pn[1] = u32x4{0u, 0u, 0u, 0u};
    S[1] = BC(pd[1]);
    SB();
    for (int i = 0; 4 * i + 2 < nq; ++i) {
      {
        f32x16& Gc = G0;
        LAS unsigned short* ohw = (LAS unsigned short*)(sm + sl * 80 + ohoff);
        Y[1] = MFMA32H(HC(pd[1]), HC(S[1]), Y[1]);
        bv[2] = wvl[4 * 64];
        *(volatile LAS int*)pown = nq - 4 * i;
        E1(0); E1(1); E1(2); E1(3);
        SB();
        Y[3] = MFMA32(BC(pn[1]), S[1], Y[3]);
        bv[3] = wvl[6 * 64];
        E1(4); CV(0); E1(5); CV(1); PA(0);
        SB();
        V = MFMA32(as[0], bv[0], zero16);
        const int prem = *(volatile const LAS int*)poth;
        E1(6); PA(1); RL(0); E1(7); CV(2);
        SB();
        V = MFMA32(as[1], bv[1], V);
        RH(0); E1(8); PA(2); E1(9); CV(3);
        SB();
        V = MFMA32(as[2], bv[2], V);
        RL(1); E1(10); PA(3); E1(11); CV(4);
        SB();
        V = MFMA32(as[3], bv[3], V);
        RH(1); E1(12); PA(4); E1(13); CV(5);
        SB();
        G1 = MFMA32(St[0], XAf[0][1], zero16);
        asm volatile("s_cmp_ge_i32 %0, %1\n\ts_setprio 0\n\ts_cbranch_scc0 1f\n\ts_setprio 1\n1:"
                     :: "s"(nq - 4 * i), "s"(__builtin_amdgcn_readfirstlane(prem)) : "scc");
        RL(2); E1(14); PA(5); E1(15); CV(6);
        SB();
        G1 = MFMA32(St[1], XAf[1][1], G1);
        *ohw = (unsigned short)0x4000;
        asm volatile("" ::: "memory");
        S[0] = BC(mine[0]);
        S[1] = BC(mine[1]);
        asm volatile("" ::: "memory");
        *ohw = (unsigned short)0;
        RH(2); PA(6); RL(3); CV(7);
        SB();
        G1 = MFMA32(ae, wC[1], G1);
        RH(3); PA(7); MX(0); RL(4); MX(1); MX(2);
        SB();
        G1 = MFMA32(as[0], wB[0][1], G1);
        RH(4); MX(3); RL(5); CN(0); MX(4);
        SB();
        G1 = MFMA32(as[1], wB[1][1], G1);
        RH(5); MX(5); RL(6); CN(1); MX(6);
        SB();
        G1 = MFMA32(as[2], wB[2][1], G1);
        RH(6); MX(7); RL(7); CN(2); MX(8);
        SB();
        G1 = MFMA32(as[3], wB[3][1], G1);
        slr = lsp[(i + 1) * 32];
        RH(7); MX(9); CN(3); MX(10); MX(11); CN(4);
        SB();
        Y[0] = MFMA32H(HC(pd[0]), HC(S[0]), Y[0]);
        bv[0] = wvl[1 * 64];
        MX(12); MX(13); CN(5); MX(14); MX(15);
        SB();
        Y[2] = MFMA32(BC(pn[0]), S[0], Y[2]);
        bv[1] = wvl[3 * 64];
        CN(6); CN(7);
        SB();
      }
      {
        f32x16& Gc = G1;
        sl = slr & 31u;
        Y[0] = MFMA32H(HC(pd[1]), HC(S[1]), Y[0]);
        bv[2] = wvl[5 * 64];
        St[0] = ohl[sl * 4 + h * 2 + 0];
        E1(0); E1(1); E1(2); E1(3);
        SB();
        Y[2] = MFMA32(BC(pn[1]), S[1], Y[2]);
        bv[3] = wvl[7 * 64];
        St[1] = ohl[sl * 4 + h * 2 + 1];
        E1(4); CV(0); E1(5); CV(1); PA(0);
        SB();
        V = MFMA32(as[0], bv[0], zero16);
        const u32x4 xo = *(const LAS u32x4*)(lxp + (i + 2) * 32);
        const unsigned eo = lep[(i + 2) * 32];
        E1(6); PA(1); RL(0); E1(7); CV(2);
        SB();
        V = MFMA32(as[1], bv[1], V);
        RH(0); E1(8); PA(2); E1(9); CV(3);
        SB();
        V = MFMA32(as[2], bv[2], V);
        RL(1); E1(10); PA(3); E1(11); CV(4);
        SB();
        V = MFMA32(as[3], bv[3], V);
        RH(1); E1(12); PA(4); E1(13); CV(5);
        SB();
        G0 = MFMA32(St[0], XAf[0][0], zero16);
        asm volatile("s_waitcnt vmcnt(0)" ::: "memory");
        ae = *(const LAS bf16x8*)(ring + ea0);
        as[0] = *(const LAS bf16x8*)(ring + xa0);
        as[1] = *(const LAS bf16x8*)(ring + xa0 + 16);
        as[2] = *(const LAS bf16x8*)(ring + xa0 + 32);
        as[3] = *(const LAS bf16x8*)(ring + xa0 + 48);
        RL(2); E1(14); PA(5); E1(15); CV(6);
        SB();
        G0 = MFMA32(St[1], XAf[1][0], G0);
        ga[0] = XG(0);
        PIN(ga[0]);
        RH(2); PA(6); RL(3); CV(7);
        SB();
        asm volatile("" : "+v"(as[0]), "+v"(as[1]), "+v"(as[2]), "+v"(as[3]), "+v"(ae) :: "memory");
        G0 = MFMA32(ae, wC[0], G0);
        GLDS(ga[0], rx, ring_a);
        ga[1] = XG(1);
        PIN(ga[1]);
        RH(3); PA(7); MX(0); RL(4); MX(1); MX(2);
        SB();
        G0 = MFMA32(as[0], wB[0][0], G0);
        GLDS(ga[1], rx, ring_a + XB);
        ga[2] = XG(2);
        PIN(ga[2]);
        RH(4); MX(3); RL(5); CN(0); MX(4);
        SB();
        G0 = MFMA32(as[1], wB[1][0], G0);
        GLDS(ga[2], rx, ring_a + 2 * XB);
        ga[3] = XG(3);
        PIN(ga[3]);
        RH(5); MX(5); RL(6); CN(1); MX(6);
        SB();
        G0 = MFMA32(as[2], wB[2][0], G0);
        GLDS(ga[3], rx, ring_a + 3 * XB);
        ga[4] = (eo & ~31u) | ec0;
        PIN(ga[4]);
        RH(6); MX(7); RL(7); CN(2); MX(8);
        SB();
        G0 = MFMA32(as[3], wB[3][0], G0);
        GLDS(ga[4], re, ring_a + 4 * XB);
        RH(7); MX(9); CN(3); MX(10); MX(11); CN(4);
        SB();
        Y[1] = MFMA32H(HC(pd[0]), HC(S[0]), Y[1]);
        bv[0] = wvl[0 * 64];
        MX(12); MX(13); CN(5); MX(14); MX(15);
        SB();
        Y[3] = MFMA32(BC(pn[0]), S[0], Y[3]);
        bv[1] = wvl[2 * 64];
        CN(6); CN(7);
        SB();
      }
    }
    *(volatile LAS int*)pown = 0;
    Y[1] = MFMA32H(HC(pd[1]), HC(S[1]), Y[1]);
#define TP(P, j) P[(j) >> 2][(j) & 3]
#define pm (pn + 1)
#define TE(G, i) G[i] = __builtin_amdgcn_exp2f(G[i])
#define TCV(P, G, j) TP(P, j) = pack_f16(G[2 * (j)], G[2 * (j) + 1])
#define TPA(P, j) TP(P, j) = __builtin_bit_cast(unsigned, __builtin_bit_cast(f16x2, (unsigned)TP(P, j)) + __builtin_bit_cast(f16x2, one15))
#define TRL(P, j) do { unsigned t_ = TP(P, j); asm volatile("v_rcp_f16_sdwa %0, %0 dst_sel:WORD_0 dst_unused:UNUSED_PRESERVE src0_sel:WORD_0" : "+v"(t_)); TP(P, j) = t_; } while (0)
#define TRH(P, j) do { unsigned t_ = TP(P, j); asm volatile("v_rcp_f16_sdwa %0, %0 dst_sel:WORD_1 dst_unused:UNUSED_PRESERVE src0_sel:WORD_1" : "+v"(t_)); TP(P, j) = t_; } while (0)
#define TMX(W, P, i) W[i] = __builtin_fmaf(W[i], (float)__builtin_bit_cast(f16x2, (unsigned)TP(P, (i) >> 1))[(i) & 1], 0.0f)
#define TCN(Q, W, j) TP(Q, j) = pack_bf16(W[2 * (j)], W[2 * (j) + 1])
    {
      LAS unsigned short* ohw = (LAS unsigned short*)(sm + sl * 80 + ohoff);
      const int nl = 1 << (nq - 4 * max(ntiles - 1, 0));
      {
      int hc = nl & 6;
      asm volatile("" : "+s"(hc));
#pragma unroll 1
      for (; hc > 0; hc -= 64) {
        u32x4 qd[2], qn[2];
        bv[2] = wvl[4 * 64];
        TE(G0,0); TE(G0,1); TE(G0,2);
        SB();
        Y[3] = MFMA32(BC(pn[1]), S[1], Y[3]);
        bv[3] = wvl[6 * 64];
        TCV(pd,G0,0); TE(G0,3); TPA(pd,0); TCV(pd,G0,1); TE(G0,4);
        SB();
        G1 = MFMA32(St[0], XAf[0][1], zero16);
        *ohw = (unsigned short)0x4000; asm volatile("" ::: "memory"); S[0] = BC(mine[0]); asm volatile("" ::: "memory"); *ohw = (unsigned short)0;
        TE(G0,5); TRL(pd,0); TPA(pd,1); TCV(pd,G0,2);
        SB();
        G1 = MFMA32(St[1], XAf[1][1], G1);
        TRH(pd,0); TE(G0,6); TE(G0,7);
        SB();
        G1 = MFMA32(ae, wC[1], G1);
        TRL(pd,1); TPA(pd,2); TCV(pd,G0,3); TRH(pd,1);
        SB();
        G1 = MFMA32(as[0], wB[0][1], G1);
        TRL(pd,2); TPA(pd,3); TRH(pd,2);
        SB();
        G1 = MFMA32(as[1], wB[1][1], G1);
        TRL(pd,3); TRH(pd,3);
        SB();
        G1 = MFMA32(as[2], wB[2][1], G1);
        SB();
        G1 = MFMA32(as[3], wB[3][1], G1);
        SB();
        V = MFMA32(as[0], bv[0], zero16);
        bv[0] = wvl[1 * 64];
        SB();
        V = MFMA32(as[1], bv[1], V);
        bv[1] = wvl[3 * 64];
        TE(G1,0); TE(G1,1); TE(G1,2);
        SB();
        V = MFMA32(as[2], bv[2], V);
        bv[2] = wvl[5 * 64];
        TCV(qd,G1,0); TE(G1,3); TPA(qd,0); TCV(qd,G1,1); TE(G1,4);
        SB();
        V = MFMA32(as[3], bv[3], V);
        bv[3] = wvl[7 * 64];
        TE(G1,5); TRL(qd,0); TPA(qd,1); TCV(qd,G1,2);
        SB();
        G0 = MFMA32(as[0], bv[0], zero16);
        TRH(qd,0); TE(G1,6); TE(G1,7);
        SB();
        G0 = MFMA32(as[1], bv[1], G0);
        TRL(qd,1); TPA(qd,2); TCV(qd,G1,3); TRH(qd,1);
        SB();
        G0 = MFMA32(as[2], bv[2], G0);
        TRL(qd,2); TPA(qd,3); TMX(V,pd,0); TRH(qd,2);
        SB();
        G0 = MFMA32(as[3], bv[3], G0);
        TRL(qd,3); TMX(V,pd,1); TMX(V,pd,2); TRH(qd,3);
        SB();
        Y[0] = MFMA32H(HC(pd[0]), HC(S[0]), Y[0]);
        TCN(qn,V,0); TMX(V,pd,3); TMX(V,pd,4); TCN(qn,V,1); TMX(V,pd,5); TMX(V,pd,6);
        SB();
        Y[1] = MFMA32H(HC(qd[0]), HC(S[0]), Y[1]);
        TCN(qn,V,2); TMX(V,pd,7); TMX(G0,qd,0); TCN(qn,V,3); TMX(G0,qd,1); TMX(G0,qd,2);
        SB();
        Y[2] = MFMA32(BC(qn[0]), S[0], Y[2]);
        TCN(pm,G0,0); TMX(G0,qd,3); TMX(G0,qd,4); TCN(pm,G0,1); TMX(G0,qd,5); TMX(G0,qd,6); TCN(pm,G0,2); TMX(G0,qd,7); TCN(pm,G0,3);
        S[1] = S[0];
        SB();
        asm volatile("" : "=v"(St[0]), "=v"(St[1]), "=v"(ae), "=v"(bv[0]), "=v"(bv[1]), "=v"(G0), "=v"(as[0]), "=v"(as[1]), "=v"(as[2]), "=v"(as[3]));
      }
      }
    }
    Y[3] = MFMA32(BC(pn[1]), S[1], Y[3]);
    if (__builtin_expect(!more, 1)) break;
  }
  {
    bf16x8 fa[2][6];
#pragma unroll
    for (int k = 0; k < 4; ++k) as[k] = *(const LAS bf16x8*)(smem + M_XG + grp * (4 * XB) + xa0 + 16 * k);
    fa[0][0] = fa[1][0] = ohl[r * 4 + h * 2];
    u32x4 t1 = __builtin_bit_cast(u32x4, ohl[r * 4 + h * 2 + 1]);
#pragma unroll
    for (int k = 0; k < 8; ++k) fa[k & 1][2 + (k >> 1)] = wvl[(8 + k) * 64];
    LAS char* zw = (LAS char*)wbase + r * 144 + h * 64;
    u32x4 o;
    f32x16 X[2], U[2];
#define FR(ct, i) do { float t_ = __builtin_amdgcn_rcpf(Y[ct][i]); PIN(t_); Y[ct][i] = t_; } while (0)
#define FM(ct, m) do { if ((m) == 0) X[ct] = MFMA32(fa[ct][0], as[2 * (ct)], zero16);          \
                       else if ((m) == 1) X[ct] = MFMA32(fa[ct][1], as[2 * (ct) + 1], X[ct]);   \
                       else U[ct] = MFMA32(fa[ct][m], as[(m) - 2], (m) == 2 ? X[ct] : U[ct]); } while (0)
#define FZ(ct, i) __builtin_amdgcn_fmed3f(__builtin_fmaf(Y[2 + (ct)][i], Y[ct][i], U[ct][i]), X[ct][i], 3.4028234e38f)
#define FO(q, j) do { o[j] = pack_bf16(FZ((q) >> 1, ((q) & 1) * 8 + 2 * (j)), FZ((q) >> 1, ((q) & 1) * 8 + 2 * (j) + 1)); \
      if ((j) == 3) *(LAS u32x4*)(zw + (q) * 16) = o; } while (0)
#pragma unroll
    for (int i = 0; i < 8; ++i) FR(0, i);
    SB();
#pragma unroll
    for (int m = 0; m < 8; ++m) {
      FM(m / 6, m % 6);
      SB();
      if (m == 0) {
        t1[3] &= (h && r != 30) ? 0xFFFF0000u : 0xFFFFFFFFu;
        fa[0][1] = fa[1][1] = BC(t1);
      }
#pragma unroll
      for (int i = 8 + 3 * m; i < 11 + 3 * m; ++i) FR(i >> 4, i & 15);
      SB();
    }
    asm volatile("s_waitcnt vmcnt(0)" ::: "memory");
    FM(1, 2); SB(); FO(0, 0); SB();
    FM(1, 3); SB(); FO(0, 1); SB();
    FM(1, 4); SB(); FO(0, 2); SB();
    FM(1, 5); SB(); FO(0, 3); FO(1, 0); FO(1, 1); FO(1, 2); FO(1, 3); SB();
    FO(2, 0); FO(2, 1); FO(2, 2); FO(2, 3); FO(3, 0); FO(3, 1); FO(3, 2); FO(3, 3);
  }
  const LAS char* zl = (const LAS char*)wbase;
  u32x4 o[4];
#pragma unroll
  for (int q = 0; q < 4; ++q) o[q] = *(const LAS u32x4*)(zl + (q * 8 + (lane >> 3)) * 144 + (lane & 7) * 16);
#pragma unroll
  for (int q = 0; q < 4; ++q) {
    const int row = q * 8 + (lane >> 3);
    __builtin_amdgcn_raw_buffer_store_b128(o[q], SRD(out, N * 128),
                                           row < nodes ? (n0 + 8 * row) * 128 + (lane & 7) * 16 : 0x7FFFFFF0, 0, 17);
  }
}

extern "C" void kernel_launch(void* const* d_in, const int* in_sizes, int n_in,
                              void* d_out, int out_size, void* d_ws, size_t ws_size,
                              hipStream_t stream) {
  const unsigned short* x = (const unsigned short*)d_in[0];
  const unsigned short* eattr = (const unsigned short*)d_in[1];
  const int* src = (const int*)d_in[2];
  const int* dst = (const int*)d_in[3];
  const unsigned short* wa = (const unsigned short*)d_in[4];
  const unsigned short* wb = (const unsigned short*)d_in[5];
  const unsigned short* wc = (const unsigned short*)d_in[6];
  const unsigned short* wu = (const unsigned short*)d_in[7];
  const unsigned short* wv = (const unsigned short*)d_in[8];
  unsigned short* out = (unsigned short*)d_out;
  const int N = in_sizes[0] / 64;
  const int E = in_sizes[2];
  const int nb = (N + NPB - 1) / NPB;

  char* ws = (char*)d_ws;
  k_part<<<NCH, 1024, 0, stream>>>(src, dst, E, nb, (unsigned*)(ws + W_TC), (unsigned*)(ws + W_OVH), (u32x4*)(ws + W_OV),
                                   wa, wb, wc, wu, wv, (unsigned short*)(ws + W_WP));
  k_main<<<nb, 512, 0, stream>>>(x, eattr, ws, E, N, out);
}
```

```cpp
#include <hip/hip_runtime.h>
#include <hip/hip_bf16.h>
#include <stdint.h>

using bf16x8 = __attribute__((ext_vector_type(8))) short;
using f16x8 = __attribute__((ext_vector_type(8))) _Float16;
using f16x2 = __attribute__((ext_vector_type(2))) _Float16;
using f32x16 = __attribute__((ext_vector_type(16))) float;
using u32x4 = __attribute__((ext_vector_type(4))) unsigned;
using u32x2 = __attribute__((ext_vector_type(2))) unsigned;
using i32x4 = __attribute__((ext_vector_type(4))) int;
using f32x4 = __attribute__((ext_vector_type(4))) float;

#define MFMA32(a, b, c) __builtin_amdgcn_mfma_f32_32x32x16_bf16((a), (b), (c), 0, 0, 0)
#define MFMA32H(a, b, c) __builtin_amdgcn_mfma_f32_32x32x16_f16((a), (b), (c), 0, 0, 0)
using b16x8 = __attribute__((ext_vector_type(8))) __bf16;
using b16x16 = __attribute__((ext_vector_type(16))) __bf16;
using u32x8 = __attribute__((ext_vector_type(8))) unsigned;
#define SMFMA(a, b, c, ix) __builtin_amdgcn_smfmac_f32_32x32x32_bf16(__builtin_bit_cast(b16x8, a), __builtin_bit_cast(b16x16, b), (c), (ix), 0, 0)
#define LAS __attribute__((address_space(3)))
#if defined(__HIP_DEVICE_COMPILE__)
#define NOPK __attribute__((target("no-packed-fp32-ops")))
#else
#define NOPK
#endif
#define SRD(p, bytes) __builtin_amdgcn_make_buffer_rsrc((void*)(p), (short)0, (int)(bytes), 0x00020000)
#define LDS_ADD(p, v) __hip_atomic_fetch_add((p), (v), __ATOMIC_RELAXED, __HIP_MEMORY_SCOPE_WORKGROUP)

#define CST(p, v) __builtin_amdgcn_raw_buffer_store_b128((v), rc, (p) * 4, 0, 17)
#define WALL 1
constexpr int GPB = 8;
constexpr int NPB = 196;
constexpr int MAXB = 256;
constexpr int NCH = 256;
constexpr int CELL = 32;
constexpr int CAP = 28;
constexpr int CSTR = 36;
constexpr int OVCAP = 4096;
constexpr int NFRAG = 34;
constexpr size_t W_TC = 0;
constexpr size_t W_OVH = W_TC + (size_t)MAXB * NCH * CELL * 4;
constexpr size_t W_WP = W_OVH + NCH * 4;
constexpr size_t W_OV = W_WP + NFRAG * 1024;
constexpr float NLOG2E = -1.44269504088896340736f;

__global__ __launch_bounds__(1024) void k_part(const int* __restrict__ src, const int* __restrict__ dst,
                                               int E, int nb,
                                               unsigned* __restrict__ TC,
                                               unsigned* __restrict__ OVH, u32x4* __restrict__ OV,
                                               const unsigned short* __restrict__ wa,
                                               const unsigned short* __restrict__ wb,
                                               const unsigned short* __restrict__ wc,
                                               const unsigned short* __restrict__ wu,
                                               const unsigned short* __restrict__ wv,
                                               unsigned short* __restrict__ wpack) {
  __shared__ __attribute__((aligned(16))) unsigned cT[(MAXB + 1) * CSTR];
  __shared__ int cnt[MAXB + 4];
  const int tid = threadIdx.x, lane = tid & 63;
  const int rnd = __builtin_amdgcn_readfirstlane(tid >> 6);
  const int c = blockIdx.x;
  const int e0 = (rnd * NCH + c) * 256 + lane * 4;
  const auto rd = SRD(dst, E * 4), rs = SRD(src, E * 4);

  const i32x4 dd = __builtin_bit_cast(i32x4, __builtin_amdgcn_raw_buffer_load_b128(rd, e0 * 4, 0, 0));
  const i32x4 ss = __builtin_bit_cast(i32x4, __builtin_amdgcn_raw_buffer_load_b128(rs, e0 * 4, 0, 0));
  unsigned wv8[8];
  if (rnd == 15 && c < NFRAG) {
    const int nn = ((lane >> 2) & 1) * 32 + (c & 1) * 16 + ((lane >> 3) & 3) * 4 + (lane & 3), h = lane >> 5;
    const unsigned short* p;
    if (c < 24) {
      const int m = c >> 3, t = (c >> 1) & 3;
      p = ((m == 0) ? wb : (m == 1) ? wv : wa) + (32 * h + 8 * t) * 64 + nn;
    } else if (c < 26) {
      p = wc + (8 * h) * 64 + nn;
    } else {
      p = wu + (32 * h + 8 * ((c - 26) >> 1)) * 64 + nn;
    }
#pragma unroll
    for (int j = 0; j < 8; ++j) wv8[j] = p[j * 64];
  }
#define CPN 88
  u32x4 t;
  if (rnd == 14 && c < CPN) {
    unsigned long long pc;
    int rel;
    asm volatile("s_getpc_b64 %0\n\ts_mov_b32 %1, _Z6k_mainPKtS0_PKciiPt@rel32@lo+4" : "=s"(pc), "=s"(rel));
    pc += (long long)rel;
    asm volatile("global_load_dwordx4 %0, %1, %2" : "=v"(t) : "v"(min((c >> 3) * 1024 + lane * 16, 10736)), "s"(pc) : "memory");
  }
  if (tid < 2 * MAXB) *reinterpret_cast<u32x2*>(cT + (tid >> 1) * CSTR + (tid & 1) * 16) = u32x2{0u, 0u};
  else if (tid < 3 * MAXB + 4) cnt[tid - 2 * MAXB] = 0;
  __syncthreads();
  if ((rnd * NCH + c) * 256 < E) {
    int bk[4], rk[4];
#pragma unroll
    for (int k = 0; k < 4; ++k) {
      const bool v = e0 + k < E;
      bk[k] = v ? (int)((unsigned)dd[k] / (unsigned)NPB) : MAXB + 1;
      rk[k] = LDS_ADD(&cnt[bk[k]], 1);
    }
    unsigned w[4];
    bool ovf = false;
#pragma unroll
    for (int k = 0; k < 4; ++k) {
      const unsigned s = (unsigned)(dd[k] - bk[k] * NPB);
      w[k] = ((unsigned)ss[k] << 16) | ((s & 7u) << 13) | ((unsigned)(lane * 4 + k) << 5) | (s >> 3);
      const int d = (rk[k] + (rk[k] < CAP / 2 ? 2 : 4)) & 31;
      unsigned* cp = cT + (rk[k] < CAP ? min(bk[k], MAXB) : MAXB) * CSTR;
      cp[d] = w[k];
      __hip_atomic_fetch_or(cp + (d & 16) + ((d >> 3) & 1), (unsigned)rnd << (4 * (d & 7)),
                            __ATOMIC_RELAXED, __HIP_MEMORY_SCOPE_WORKGROUP);
      ovf |= rk[k] >= CAP;
    }
    if (__builtin_amdgcn_ballot_w64(ovf) != 0) {
#pragma unroll 1
      for (int k = 0; k < 4; ++k) {
        const int bq = k == 0 ? bk[0] : k == 1 ? bk[1] : k == 2 ? bk[2] : bk[3];
        const int rq = k == 0 ? rk[0] : k == 1 ? rk[1] : k == 2 ? rk[2] : rk[3];
        const unsigned wq = k == 0 ? w[0] : k == 1 ? w[1] : k == 2 ? w[2] : w[3];
        if (bq < MAXB && rq >= CAP) {
          const int o = LDS_ADD(&cnt[MAXB], 1);
          OV[c * OVCAP + o] = u32x4{wq >> 9, ((unsigned)(e0 + k) << 5) | (wq & 31u), (unsigned)bq, 0u};
        }
      }
    }
  }
  asm volatile("s_waitcnt lgkmcnt(0)\n\ts_barrier" ::: "memory");
  {
    const auto rc = SRD(TC, MAXB * NCH * CELL * 4);
    const int p = tid & 7;
    const int first = (CAP / 2) * (p >> 2) + 4 * (p & 3) - 2;
    const int novf = cnt[MAXB];
#pragma unroll
    for (int q = 0; q < 2; ++q) {
      const int b = (tid >> 3) + q * 128;
      const int m = cnt[b];
      u32x4 v = *reinterpret_cast<const u32x4*>(cT + b * CSTR + 4 * p);
      if ((p & 3) == 0) v[0] |= (unsigned)(m > CAP ? min(CAP / 2 + novf, 255) : min(max(m - first - 2, 0), CAP / 2));
      if (b < nb && (WALL || (p & 3) == 0 || first < m)) CST((b * NCH + c) * CELL + 4 * p, v);
    }
    if (tid == 0) __builtin_amdgcn_raw_buffer_store_b32((unsigned)novf, SRD(OVH, NCH * 4), c * 4, 0, 17);
  }
  if (rnd == 15 && c < NFRAG) {
    if (c < 8 || c == 24 || c == 25) {
#pragma unroll
      for (int j = 0; j < 8; ++j) {
        const unsigned u = __float_as_uint(__uint_as_float(wv8[j] << 16) * NLOG2E);
        wv8[j] = (u + 0x7FFFu + ((u >> 16) & 1u)) >> 16;
      }
    }
    const u32x4 v = {wv8[0] | (wv8[1] << 16), wv8[2] | (wv8[3] << 16), wv8[4] | (wv8[5] << 16), wv8[6] | (wv8[7] << 16)};
    __builtin_amdgcn_raw_buffer_store_b128(v, SRD(wpack, NFRAG * 1024), c * 1024 + lane * 16, 0, 17);
  }
  asm volatile("s_waitcnt vmcnt(0)" :: "v"(t) : "memory");
}

__device__ __forceinline__ NOPK unsigned pack_bf16(float lo, float hi) {
  typedef __attribute__((ext_vector_type(2))) float f2;
  typedef __attribute__((ext_vector_type(2))) __bf16 b2;
  f2 v = {lo, hi};
  b2 r = __builtin_convertvector(v, b2);
  return __builtin_bit_cast(unsigned, r);
}
__device__ __forceinline__ NOPK unsigned pack_f16(float lo, float hi) {
  typedef __attribute__((ext_vector_type(2))) float f2;
  f2 v = {lo, hi};
  f16x2 r = __builtin_convertvector(v, f16x2);
  return __builtin_bit_cast(unsigned, r);
}
#define WGSYNC() do { __builtin_amdgcn_fence(__ATOMIC_RELEASE, "workgroup"); __builtin_amdgcn_s_barrier(); \
                      __builtin_amdgcn_fence(__ATOMIC_ACQUIRE, "workgroup"); } while (0)
#define U2F(u) __builtin_bit_cast(float, (unsigned)(u))
constexpr int XB = 1040;
constexpr int RING_SLOT = 4 * XB + 1024;
constexpr int LCAP = 576;
constexpr int LPAD = LCAP + 96;
constexpr int SSCR = 32 * 80;
constexpr int WAVE_LDS = RING_SLOT;
constexpr int M_WL = 0;
constexpr int M_OH = 16 * 1024;
constexpr int M_OS = M_OH + 2048;
constexpr int M_CNT = M_OS + 1024;
constexpr int M_ST = M_CNT + 256;
constexpr int M_SC = M_ST + 2048;
constexpr int M_WV = M_SC + GPB * SSCR;
constexpr int M_LX = M_WV + 3 * WAVE_LDS;
constexpr int LED = GPB * LPAD;
constexpr int M_XG = M_WV + GPB * WAVE_LDS + 2 * LED * 4;
constexpr int MAIN_LDS = M_XG + GPB * 4 * XB;
static_assert(M_LX % 16 == 0 && LED % 64 == 0 && LCAP % 32 == 0 && NCH / 2 * CAP * 4 <= 3 * WAVE_LDS &&
              NCH * (CAP / 2 + 8) * 4 <= 5 * WAVE_LDS, "lists");
static_assert(MAIN_LDS <= 160 * 1024, "LDS");

__global__ __launch_bounds__(512, 2) NOPK void k_main(const unsigned short* __restrict__ x,
                                                      const unsigned short* __restrict__ eattr,
                                                      const char* __restrict__ wsp, int E,
                                                      int N, unsigned short* __restrict__ out) {
  __shared__ __attribute__((aligned(16))) char smem[MAIN_LDS];
  asm volatile("" :: "s"(x), "s"(eattr), "s"(wsp), "s"(E), "s"(N), "s"(out));
  const unsigned* TC = reinterpret_cast<const unsigned*>(wsp + W_TC);
  const unsigned* OVH = reinterpret_cast<const unsigned*>(wsp + W_OVH);
  const u32x4* OV = reinterpret_cast<const u32x4*>(wsp + W_OV);
  const char* wpack = wsp + W_WP;
  auto body = [&](const int tid, const int sc) NOPK __attribute__((always_inline)) -> int {
  const int lane = tid & 63;
  const int wave = __builtin_amdgcn_readfirstlane(tid >> 6);
  const int r = lane & 31, h = lane >> 5;
  const int b = blockIdx.x;

  const int pc = lane & 7;
  u32x4 tv[4];
  const unsigned* cp0 = TC + ((size_t)b * NCH + wave * 32) * CELL;
#define CLD(k) asm volatile("global_load_dwordx4 %0, %1, %2 offset:%3" : "=v"(tv[k]) : "v"(lane * 16), "s"(cp0), "n"((k) * 1024) : "memory")
  CLD(0);
  CLD(1);
#define GLDS(vo, rs, mv) asm volatile("s_mov_b32 m0, %2\n\ts_nop 0\n\tbuffer_load_dwordx4 %0, %1, 0 offen lds" \
                                      :: "v"(vo), "s"(rs), "s"(mv) : "memory")
  const auto rx = SRD(x, N * 128);
  const auto re = SRD(eattr, E * 32);
  const unsigned xc0 = (unsigned)(((lane & 7) ^ ((lane >> 2) & 4)) * 16);
  const unsigned lb = (unsigned)(size_t)(LAS char*)smem;
  LAS int* cnt = (LAS int*)(smem + M_CNT);
  if (tid < 32) {
    const int mid = lb + M_LX + (tid >> 2) * (LPAD * 4) + LCAP * 2;
    cnt[tid] = (tid & 3) == 0 ? mid : (tid & 3) == 3 ? mid - 4 : (tid & 3) == 2 ? 1023 : 0;
  }
  __builtin_amdgcn_s_setprio(1);
  {
    unsigned* oh = reinterpret_cast<unsigned*>(smem + M_OH);
    const int i = tid;
    const int sl = i >> 4, hh = (i >> 3) & 1, ks = (i >> 2) & 1, jp = i & 3;
    const int j0 = 2 * jp, j1 = 2 * jp + 1;
    const int nd0 = 16 * ks + 8 * (j0 >> 2) + 4 * hh + (j0 & 3);
    const int nd1 = 16 * ks + 8 * (j1 >> 2) + 4 * hh + (j1 & 3);
    oh[i] = (sl == nd0 ? 0x3F80u : 0u) | (sl == nd1 ? 0x3F800000u : 0u);
    const int s2 = (i >> 3) & 31, q2 = ((s2 >> 2) & 4) | ((s2 >> 1) & 2) | ((s2 >> 3) & 1);
    oh[512 + (i & 255)] = (i & 7) == 7 ? 0x3F800000u : q2 == (i & 7) ? 0x3F80u : 0u;
  }
  asm volatile("s_waitcnt lgkmcnt(0)\n\ts_barrier" ::: "memory");
  CLD(2);
  CLD(3);
  auto own_rows = [&]() NOPK __attribute__((always_inline)) {
    const int nw = (min(NPB - wave, N - b * NPB - wave) + 7) >> 3;
#pragma unroll
    for (int k = 0; k < 4; ++k) {
      const int s = (lane >> 3) * 4 + k;
      const unsigned xo = s < nw ? (unsigned)(b * NPB + wave + 8 * s) << 7 : 0xFFFFFF80u;
      GLDS(xo | xc0, rx, lb + M_XG + wave * (4 * XB) + k * XB);
    }
  };
  const LAS bf16x8* wl = (const LAS bf16x8*)(smem + M_WL);
  const LAS bf16x8* ohl = (const LAS bf16x8*)(smem + M_OH);
  const LAS bf16x8* osl = (const LAS bf16x8*)(smem + M_OS) + h;
  const unsigned ixh = h ? 0u - 0x11110u : 0u;
#define LP(a) ((LAS unsigned*)(size_t)(unsigned)(a))
  char* wbase = smem + M_WV + wave * WAVE_LDS + (wave >= 3 ? 2 * LED * 4 : 0);
  const unsigned ring_a = (unsigned)(size_t)(LAS char*)wbase;
  {
    unsigned long long vm[16];
    unsigned ad[16], xe[16], hw[4], mk[4];
    const unsigned four = (lane & 8) ? -4 : 4, cof = lb + ((lane & 8) ? 12u : 0u);
    {
      constexpr int k = 0;
      asm volatile("s_waitcnt vmcnt(%1)" : "+v"(tv[k]) : "n"(3) : "memory");
      const unsigned h0 = (unsigned)__builtin_amdgcn_mov_dpp((int)tv[k][0], 0x00, 0xF, 0xF, true);
      const unsigned h1 = (unsigned)__builtin_amdgcn_mov_dpp((int)tv[k][1], 0x00, 0xF, 0xF, true);
      hw[k] = ((lane & 2) ? h1 : h0) >> ((lane & 1) * 16);
      const unsigned m = mk[k] = h0 & 0xFFu;
      unsigned ci[4];
#pragma unroll
      for (int j = 0; j < 4; ++j) {
        vm[4 * k + j] = __builtin_amdgcn_ballot_w64((unsigned)((lane & 3) * 4 + j - 2) < m);
        xe[4 * k + j] = tv[k][j] >> 9;
        ci[j] = (xe[4 * k + j] & 0x70u) | cof;
      }
      asm volatile("s_mov_b64 exec, %4\n\tds_add_rtn_u32 %0, %8, %12 offset:%13\n\t"
                   "s_mov_b64 exec, %5\n\tds_add_rtn_u32 %1, %9, %12 offset:%13\n\t"
                   "s_mov_b64 exec, %6\n\tds_add_rtn_u32 %2, %10, %12 offset:%13\n\t"
                   "s_mov_b64 exec, %7\n\tds_add_rtn_u32 %3, %11, %12 offset:%13\n\t"
                   "s_mov_b64 exec, -1"
                   : "=&v"(ad[4 * k]), "=&v"(ad[4 * k + 1]), "=&v"(ad[4 * k + 2]), "=&v"(ad[4 * k + 3])
                   : "s"(vm[4 * k]), "s"(vm[4 * k + 1]), "s"(vm[4 * k + 2]), "s"(vm[4 * k + 3]),
                     "v"(ci[0]), "v"(ci[1]), "v"(ci[2]), "v"(ci[3]), "v"(four), "n"(M_CNT)
                   : "memory");
    }
    {
      constexpr int k = 1;
      asm volatile("s_waitcnt vmcnt(%1)" : "+v"(tv[k]) : "n"(2) : "memory");
      const unsigned h0 = (unsigned)__builtin_amdgcn_mov_dpp((int)tv[k][0], 0x00, 0xF, 0xF, true);
      const unsigned h1 = (unsigned)__builtin_amdgcn_mov_dpp((int)tv[k][1], 0x00, 0xF, 0xF, true);
      hw[k] = ((lane & 2) ? h1 : h0) >> ((lane & 1) * 16);
      const unsigned m = mk[k] = h0 & 0xFFu;
      unsigned ci[4];
#pragma unroll
      for (int j = 0; j < 4; ++j) {
        vm[4 * k + j] = __builtin_amdgcn_ballot_w64((unsigned)((lane & 3) * 4 + j - 2) < m);
        xe[4 * k + j] = tv[k][j] >> 9;
        ci[j] = (xe[4 * k + j] & 0x70u) | cof;
      }
      asm volatile("s_mov_b64 exec, %4\n\tds_add_rtn_u32 %0, %8, %12 offset:%13\n\t"
                   "s_mov_b64 exec, %5\n\tds_add_rtn_u32 %1, %9, %12 offset:%13\n\t"
                   "s_mov_b64 exec, %6\n\tds_add_rtn_u32 %2, %10, %12 offset:%13\n\t"
                   "s_mov_b64 exec, %7\n\tds_add_rtn_u32 %3, %11, %12 offset:%13\n\t"
                   "s_mov_b64 exec, -1"
                   : "=&v"(ad[4 * k]), "=&v"(ad[4 * k + 1]), "=&v"(ad[4 * k + 2]), "=&v"(ad[4 * k + 3])
                   : "s"(vm[4 * k]), "s"(vm[4 * k + 1]), "s"(vm[4 * k + 2]), "s"(vm[4 * k + 3]),
                     "v"(ci[0]), "v"(ci[1]), "v"(ci[2]), "v"(ci[3]), "v"(four), "n"(M_CNT)
                   : "memory");
    }
    {
      constexpr int k = 0;
      const unsigned c13 = (unsigned)(wave * 32 + k * 8 + (lane >> 3)) << 13;
      unsigned ee[4];
#pragma unroll
      for (int j = 0; j < 4; ++j) ee[j] = (tv[k][j] & 0x1FFFu) | ((((hw[k] >> (4 * j)) & 15u) << 21) | c13);
      asm volatile("s_waitcnt lgkmcnt(4)\n\t"
                   "s_mov_b64 exec, %0\n\tds_write2st64_b32 %4, %8, %12 offset0:%16 offset1:%17\n\t"
                   "s_mov_b64 exec, %1\n\tds_write2st64_b32 %5, %9, %13 offset0:%16 offset1:%17\n\t"
                   "s_mov_b64 exec, %2\n\tds_write2st64_b32 %6, %10, %14 offset0:%16 offset1:%17\n\t"
                   "s_mov_b64 exec, %3\n\tds_write2st64_b32 %7, %11, %15 offset0:%16 offset1:%17\n\t"
                   "s_mov_b64 exec, -1"
                   :: "s"(vm[4 * k]), "s"(vm[4 * k + 1]), "s"(vm[4 * k + 2]), "s"(vm[4 * k + 3]),
                      "v"(ad[4 * k]), "v"(ad[4 * k + 1]), "v"(ad[4 * k + 2]), "v"(ad[4 * k + 3]),
                      "v"(xe[4 * k]), "v"(xe[4 * k + 1]), "v"(xe[4 * k + 2]), "v"(xe[4 * k + 3]),
                      "v"(ee[0]), "v"(ee[1]), "v"(ee[2]), "v"(ee[3]), "n"(0), "n"(LED / 64)
                   : "memory");
    }
    {
      constexpr int k = 1;
      const unsigned c13 = (unsigned)(wave * 32 + k * 8 + (lane >> 3)) << 13;
      unsigned ee[4];
#pragma unroll
      for (int j = 0; j < 4; ++j) ee[j] = (tv[k][j] & 0x1FFFu) | ((((hw[k] >> (4 * j)) & 15u) << 21) | c13);
      asm volatile("s_waitcnt lgkmcnt(4)\n\t"
                   "s_mov_b64 exec, %0\n\tds_write2st64_b32 %4, %8, %12 offset0:%16 offset1:%17\n\t"
                   "s_mov_b64 exec, %1\n\tds_write2st64_b32 %5, %9, %13 offset0:%16 offset1:%17\n\t"
                   "s_mov_b64 exec, %2\n\tds_write2st64_b32 %6, %10, %14 offset0:%16 offset1:%17\n\t"
                   "s_mov_b64 exec, %3\n\tds_write2st64_b32 %7, %11, %15 offset0:%16 offset1:%17\n\t"
                   "s_mov_b64 exec, -1"
                   :: "s"(vm[4 * k]), "s"(vm[4 * k + 1]), "s"(vm[4 * k + 2]), "s"(vm[4 * k + 3]),
                      "v"(ad[4 * k]), "v"(ad[4 * k + 1]), "v"(ad[4 * k + 2]), "v"(ad[4 * k + 3]),
                      "v"(xe[4 * k]), "v"(xe[4 * k + 1]), "v"(xe[4 * k + 2]), "v"(xe[4 * k + 3]),
                      "v"(ee[0]), "v"(ee[1]), "v"(ee[2]), "v"(ee[3]), "n"(0), "n"(LED / 64)
                   : "memory");
    }
    own_rows();
    {
      constexpr int k = 2;
      asm volatile("s_waitcnt vmcnt(%1)" : "+v"(tv[k]) : "n"(5) : "memory");
      const unsigned h0 = (unsigned)__builtin_amdgcn_mov_dpp((int)tv[k][0], 0x00, 0xF, 0xF, true);
      const unsigned h1 = (unsigned)__builtin_amdgcn_mov_dpp((int)tv[k][1], 0x00, 0xF, 0xF, true);
      hw[k] = ((lane & 2) ? h1 : h0) >> ((lane & 1) * 16);
      const unsigned m = mk[k] = h0 & 0xFFu;
      unsigned ci[4];
#pragma unroll
      for (int j = 0; j < 4; ++j) {
        vm[4 * k + j] = __builtin_amdgcn_ballot_w64((unsigned)((lane & 3) * 4 + j - 2) < m);
        xe[4 * k + j] = tv[k][j] >> 9;
        ci[j] = (xe[4 * k + j] & 0x70u) | cof;
      }
      asm volatile("s_mov_b64 exec, %4\n\tds_add_rtn_u32 %0, %8, %12 offset:%13\n\t"
                   "s_mov_b64 exec, %5\n\tds_add_rtn_u32 %1, %9, %12 offset:%13\n\t"
                   "s_mov_b64 exec, %6\n\tds_add_rtn_u32 %2, %10, %12 offset:%13\n\t"
                   "s_mov_b64 exec, %7\n\tds_add_rtn_u32 %3, %11, %12 offset:%13\n\t"
                   "s_mov_b64 exec, -1"
                   : "=&v"(ad[4 * k]), "=&v"(ad[4 * k + 1]), "=&v"(ad[4 * k + 2]), "=&v"(ad[4 * k + 3])
                   : "s"(vm[4 * k]), "s"(vm[4 * k + 1]), "s"(vm[4 * k + 2]), "s"(vm[4 * k + 3]),
                     "v"(ci[0]), "v"(ci[1]), "v"(ci[2]), "v"(ci[3]), "v"(four), "n"(M_CNT)
                   : "memory");
    }
    {
      constexpr int k = 3;
      asm volatile("s_waitcnt vmcnt(%1)" : "+v"(tv[k]) : "n"(4) : "memory");
      const unsigned h0 = (unsigned)__builtin_amdgcn_mov_dpp((int)tv[k][0], 0x00, 0xF, 0xF, true);
      const unsigned h1 = (unsigned)__builtin_amdgcn_mov_dpp((int)tv[k][1], 0x00, 0xF, 0xF, true);
      hw[k] = ((lane & 2) ? h1 : h0) >> ((lane & 1) * 16);
      const unsigned m = mk[k] = h0 & 0xFFu;
      unsigned ci[4];
#pragma unroll
      for (int j = 0; j < 4; ++j) {
        vm[4 * k + j] = __builtin_amdgcn_ballot_w64((unsigned)((lane & 3) * 4 + j - 2) < m);
        xe[4 * k + j] = tv[k][j] >> 9;
        ci[j] = (xe[4 * k + j] & 0x70u) | cof;
      }
      asm volatile("s_mov_b64 exec, %4\n\tds_add_rtn_u32 %0, %8, %12 offset:%13\n\t"
                   "s_mov_b64 exec, %5\n\tds_add_rtn_u32 %1, %9, %12 offset:%13\n\t"
                   "s_mov_b64 exec, %6\n\tds_add_rtn_u32 %2, %10, %12 offset:%13\n\t"
                   "s_mov_b64 exec, %7\n\tds_add_rtn_u32 %3, %11, %12 offset:%13\n\t"
                   "s_mov_b64 exec, -1"
                   : "=&v"(ad[4 * k]), "=&v"(ad[4 * k + 1]), "=&v"(ad[4 * k + 2]), "=&v"(ad[4 * k + 3])
                   : "s"(vm[4 * k]), "s"(vm[4 * k + 1]), "s"(vm[4 * k + 2]), "s"(vm[4 * k + 3]),
                     "v"(ci[0]), "v"(ci[1]), "v"(ci[2]), "v"(ci[3]), "v"(four), "n"(M_CNT)
                   : "memory");
    }
    {
      constexpr int k = 2;
      const unsigned c13 = (unsigned)(wave * 32 + k * 8 + (lane >> 3)) << 13;
      unsigned ee[4];
#pragma unroll
      for (int j = 0; j < 4; ++j) ee[j] = (tv[k][j] & 0x1FFFu) | ((((hw[k] >> (4 * j)) & 15u) << 21) | c13);
      asm volatile("s_waitcnt lgkmcnt(4)\n\t"
                   "s_mov_b64 exec, %0\n\tds_write2st64_b32 %4, %8, %12 offset0:%16 offset1:%17\n\t"
                   "s_mov_b64 exec, %1\n\tds_write2st64_b32 %5, %9, %13 offset0:%16 offset1:%17\n\t"
                   "s_mov_b64 exec, %2\n\tds_write2st64_b32 %6, %10, %14 offset0:%16 offset1:%17\n\t"
                   "s_mov_b64 exec, %3\n\tds_write2st64_b32 %7, %11, %15 offset0:%16 offset1:%17\n\t"
                   "s_mov_b64 exec, -1"
                   :: "s"(vm[4 * k]), "s"(vm[4 * k + 1]), "s"(vm[4 * k + 2]), "s"(vm[4 * k + 3]),
                      "v"(ad[4 * k]), "v"(ad[4 * k + 1]), "v"(ad[4 * k + 2]), "v"(ad[4 * k + 3]),
                      "v"(xe[4 * k]), "v"(xe[4 * k + 1]), "v"(xe[4 * k + 2]), "v"(xe[4 * k + 3]),
                      "v"(ee[0]), "v"(ee[1]), "v"(ee[2]), "v"(ee[3]), "n"(0), "n"(LED / 64)
                   : "memory");
    }
    {
      constexpr int k = 3;
      const unsigned c13 = (unsigned)(wave * 32 + k * 8 + (lane >> 3)) << 13;
      unsigned ee[4];
#pragma unroll
      for (int j = 0; j < 4; ++j) ee[j] = (tv[k][j] & 0x1FFFu) | ((((hw[k] >> (4 * j)) & 15u) << 21) | c13);
      asm volatile("s_waitcnt lgkmcnt(4)\n\t"
                   "s_mov_b64 exec, %0\n\tds_write2st64_b32 %4, %8, %12 offset0:%16 offset1:%17\n\t"
                   "s_mov_b64 exec, %1\n\tds_write2st64_b32 %5, %9, %13 offset0:%16 offset1:%17\n\t"
                   "s_mov_b64 exec, %2\n\tds_write2st64_b32 %6, %10, %14 offset0:%16 offset1:%17\n\t"
                   "s_mov_b64 exec, %3\n\tds_write2st64_b32 %7, %11, %15 offset0:%16 offset1:%17\n\t"
                   "s_mov_b64 exec, -1"
                   :: "s"(vm[4 * k]), "s"(vm[4 * k + 1]), "s"(vm[4 * k + 2]), "s"(vm[4 * k + 3]),
                      "v"(ad[4 * k]), "v"(ad[4 * k + 1]), "v"(ad[4 * k + 2]), "v"(ad[4 * k + 3]),
                      "v"(xe[4 * k]), "v"(xe[4 * k + 1]), "v"(xe[4 * k + 2]), "v"(xe[4 * k + 3]),
                      "v"(ee[0]), "v"(ee[1]), "v"(ee[2]), "v"(ee[3]), "n"(0), "n"(LED / 64)
                   : "memory");
    }
    if (__builtin_expect(__builtin_amdgcn_ballot_w64(max(max(mk[0], mk[1]), max(mk[2], mk[3])) > CAP / 2) != 0, 0)) {
#pragma unroll 1
      for (int k = 0; k < 4; ++k) {
        const unsigned m = k == 0 ? mk[0] : k == 1 ? mk[1] : k == 2 ? mk[2] : mk[3];
        if (pc + CAP / 2 < m) {
          const u32x4 v = OV[(wave * 32 + k * 8 + (lane >> 3)) * OVCAP + pc];
          if ((int)v[2] == b) {
            LAS unsigned* p = LP(LDS_ADD((LAS int*)((LAS char*)cnt + (v[0] & 0x70u)), 4));
            p[0] = v[0];
            p[LED] = v[1];
          }
          if (m > CAP / 2 + 8) cnt[1] = 1;
        }
      }
    }
  }
    {
      const auto rw = SRD(wpack, NFRAG * 1024);
      const unsigned vo = (unsigned)(wave * 1024 + lane * 16);
      GLDS(vo + 16384u, rw, lb + M_SC + 8192 + wave * 1024);
      GLDS(vo, rw, lb + M_SC + wave * 1024);
      asm volatile("s_mov_b32 m0, %2\n\ts_nop 0\n\tbuffer_load_dword %0, %1, 0 offen lds"
                   :: "v"(24576u + (vo >> 2)), "s"(rw), "s"(lb + M_SC + 16384 + wave * 256) : "memory");
      GLDS(vo + 8192u, rw, lb + M_WL + wave * 1024);
      GLDS(vo + 26624u, rw, lb + M_WL + 8192 + wave * 1024);
    }
  asm volatile("s_waitcnt lgkmcnt(0)\n\ts_barrier" ::: "memory");
  const int cu = cnt[4 * pc], cd = cnt[4 * pc + 3];
  const int cpc = cu - cd;
  const int cpa = cnt[4 * (lane >> 3)] - cnt[4 * (lane >> 3) + 3];
  const int lo = lb + M_LX + pc * (LPAD * 4);
  int mo0 = cnt[1];
  bool ready = __builtin_amdgcn_ballot_w64(max(cu - LCAP * 4 - lo, lo - 4 - cd) > 0) == 0;
  int grp;
  {
    const unsigned long long gt = __builtin_amdgcn_ballot_w64(cpc * 2 + pc > cpa * 2 + (lane >> 3));
    const unsigned wd = h ? (unsigned)(gt >> 32) : (unsigned)gt;
    const int rk = __builtin_popcount((wd >> (r & 24)) & 0xFFu);
    grp = __builtin_ctzll(__builtin_amdgcn_ballot_w64(rk == (wave < 4 ? wave : 11 - wave))) >> 3;
  }
  asm volatile("" : "+v"(mo0));
  const int gn0 = b * NPB + grp;
  const int nodes = max((min(NPB - grp, N - gn0) + 7) >> 3, 0);
  const int n0 = min(gn0, N - 1);
  ((LAS unsigned*)(smem + M_ST))[tid] = (ready ? 0x80000u : 0xFFFFu) | ((unsigned)(tid & 1) << 16);

  f32x16 Y[4];
#pragma unroll
  for (int q = 0; q < 4; ++q)
#pragma unroll
    for (int i = 0; i < 16; ++i) Y[q][i] = q < 2 ? 1e-6f * 8192.f : 0.f;

  const unsigned ec0 = (unsigned)(((lane & 1) ^ ((lane >> 4) & 1)) * 16);
  const int ohoff = ((((r >> 2) & 1) * 16) + ((r >> 4) * 8) + (((r >> 3) & 1) * 4) + (r & 3)) * 2;
  const int xa0 = (r & 3) * XB + (r >> 2) * 128 + (h ^ ((r >> 3) & 1)) * 64;
  const int ea0 = 4 * XB + r * 32 + ((h ^ ((r >> 3) & 1)) * 16);
#define SB() __builtin_amdgcn_sched_barrier(0)
  bf16x8 wB[4][2], wC[2];
  u32x8 XAf[2];
  const LAS bf16x8* wvl = wl + lane;
  f32x16 G0, G1, V;
  u32x4 pd[2], pn[2];
  bf16x8 S[2], St, as[4], ae, bv[4];
  unsigned ix;
  const f32x16 zero16 = {0.f, 0.f, 0.f, 0.f, 0.f, 0.f, 0.f, 0.f, 0.f, 0.f, 0.f, 0.f, 0.f, 0.f, 0.f, 0.f};
  LAS char* sm = (LAS char*)(smem + M_SC + wave * SSCR);
  LAS u32x4* mine = (LAS u32x4*)(sm + r * 80 + h * 32);
  const LAS char* ring = (const LAS char*)wbase;
  unsigned ga[5];
  unsigned slr;
  int sl;

#define E1(i) Gc[i] = __builtin_amdgcn_exp2f(Gc[i])
#define PD(j) pd[(j) >> 2][(j) & 3]
#define CV(j) PD(j) = pack_f16(Gc[2 * (j)], Gc[2 * (j) + 1])
#define PA(j) PD(j) = __builtin_bit_cast(unsigned, __builtin_bit_cast(f16x2, (unsigned)PD(j)) + __builtin_bit_cast(f16x2, one15))
#define RL(j) do { unsigned t_ = PD(j); asm volatile("v_rcp_f16_sdwa %0, %0 dst_sel:WORD_0 dst_unused:UNUSED_PRESERVE src0_sel:WORD_0" : "+v"(t_)); PD(j) = t_; } while (0)
#define RH(j) do { unsigned t_ = PD(j); asm volatile("v_rcp_f16_sdwa %0, %0 dst_sel:WORD_1 dst_unused:UNUSED_PRESERVE src0_sel:WORD_1" : "+v"(t_)); PD(j) = t_; } while (0)
#define TP(P, j) P[(j) >> 2][(j) & 3]
#define TMX(Q, W, P, i) do { \
    if ((i) & 1) TP(Q, (i) >> 1) = __builtin_bit_cast(unsigned, __builtin_bit_cast(f16x2, (unsigned)TP(Q, (i) >> 1)) * __builtin_bit_cast(f16x2, (unsigned)TP(P, (i) >> 1))); \
    else TP(Q, (i) >> 1) = pack_f16(W[i], W[(i) + 1]); } while (0)
#define MX(i) TMX(pn, V, pd, i)
#define HC(v) __builtin_bit_cast(f16x8, v)
#define XG(k) ((xo[k] & ~127u) | xc0)
#define BC(v) __builtin_bit_cast(bf16x8, v)
#define PIN(v) asm volatile("" : "+v"(v))
#define IX(s) ((unsigned)(s) * 0x1111u + ixh)

  const unsigned one15 = 0x0C000C00u;
  int more, ntiles, nq;
  LAS int* pown = cnt + 4 * wave + 2;
  const LAS int* poth = cnt + 4 * (wave ^ 4) + 2;
  PIN(pown); PIN(poth);
  const LAS unsigned *lxp, *lep, *lsp;
  auto round_begin = [&](int sa, int ea, int mo) NOPK __attribute__((always_inline)) {
    const int s4 = sa & ~15;
    const int cn = (ea - s4) >> 2;
    more = __builtin_amdgcn_readfirstlane(mo);
    ntiles = (cn + 31) >> 5;
    nq = (cn + 7) >> 3;
    lxp = LP(s4) + (lane >> 3) * 4;
    lep = LP(s4) + LED + (lane >> 1);
    lsp = LP(s4) + LED + r;
    LAS unsigned* w0 = LP(ea) + lane;
    LAS unsigned* w1 = (h && s4 + 4 * r < sa) ? LP(s4) + r : LP(ea) + 64 + r;
    w0[0] = 0xFFFFFF80u;
    w0[LED] = 0xFFFFFFFFu;
    w1[0] = 0xFFFFFF80u;
    w1[LED] = 0xFFFFFFFFu;
  };
  auto gather = [&](int t, bool issue) NOPK __attribute__((always_inline)) {
    const u32x4 xo = *(const LAS u32x4*)(lxp + t * 32);
    const unsigned eo = lep[t * 32];
    ga[0] = XG(0); ga[1] = XG(1); ga[2] = XG(2); ga[3] = XG(3);
    ga[4] = (eo & ~31u) | ec0;
    if (issue) {
      GLDS(ga[0], rx, ring_a); GLDS(ga[1], rx, ring_a + XB); GLDS(ga[2], rx, ring_a + 2 * XB);
      GLDS(ga[3], rx, ring_a + 3 * XB); GLDS(ga[4], re, ring_a + 4 * XB);
    }
  };
  if (ready) {
    round_begin(__builtin_amdgcn_readlane(cd, grp) + 4, __builtin_amdgcn_readlane(cu, grp), mo0);
    if (wave < 4) gather(0, true);
  }
  asm volatile("s_cmp_lg_u32 %0, 0\n\ts_cbranch_scc1 1f\n\ts_waitcnt vmcnt(0)\n1:\n\ts_waitcnt vmcnt(5)\n\ts_barrier"
               :: "s"(__builtin_amdgcn_readfirstlane((ready && wave < 4) ? 1 : 0)) : "memory", "scc");
  if (__builtin_expect(sc != 0, 0)) {
    LAS u32x4* p = (LAS u32x4*)(smem + M_WL) + tid;
    u32x4 v = *p;
    const float f = U2F((127 - sc) << 23);
#pragma unroll
    for (int j = 0; j < 4; ++j) v[j] = pack_bf16(U2F(v[j] << 16) * f, U2F(v[j] & 0xFFFF0000u) * f);
    *p = v;
  }
  {
      const LAS bf16x8* ws = (const LAS bf16x8*)(smem + M_SC) + lane;
      bf16x8 xg[4], wA[8];
#pragma unroll
      for (int k = 0; k < 4; ++k) xg[k] = *(const LAS bf16x8*)(smem + M_XG + grp * (4 * XB) + xa0 + 16 * k);
#pragma unroll
      for (int k = 0; k < 8; ++k) wA[k] = ws[(8 + k) * 64];
#pragma unroll
      for (int k = 0; k < 8; ++k) wB[k >> 1][k & 1] = ws[k * 64];
      wC[0] = ws[16 * 64];
      wC[1] = ws[17 * 64];
      SB();
      asm volatile("s_waitcnt lgkmcnt(0)\n\ts_barrier"
                   : "+v"(wB[0][0]), "+v"(wB[0][1]), "+v"(wB[1][0]), "+v"(wB[1][1]), "+v"(wB[2][0]), "+v"(wB[2][1]),
                     "+v"(wB[3][0]), "+v"(wB[3][1]), "+v"(wC[0]), "+v"(wC[1]),
                     "+v"(xg[0]), "+v"(xg[1]), "+v"(xg[2]), "+v"(xg[3]), "+v"(wA[0]), "+v"(wA[1]), "+v"(wA[2]),
                     "+v"(wA[3]), "+v"(wA[4]), "+v"(wA[5]), "+v"(wA[6]), "+v"(wA[7]) :: "memory");
      SB();
      if (ready) {
        if (wave >= 4) gather(0, true);

      }
      SB();
#pragma unroll
      for (int ct = 0; ct < 2; ++ct) {
        f32x16 A = zero16;
#pragma unroll
        for (int k = 0; k < 4; ++k) A = MFMA32(xg[k], wA[k * 2 + ct], A);
#pragma unroll
        for (int j = 0; j < 8; ++j) XAf[ct][j] = pack_bf16(A[2 * j] * NLOG2E, A[2 * j + 1] * NLOG2E);
        if (h) XAf[ct][6] = XAf[ct][7] = 0xC140C140u;
      }
      const u32x4 z4 = {0u, 0u, 0u, 0u};
      mine[0] = z4;
      mine[1] = z4;
      SB();
    }
  for (;;) {
   if (__builtin_expect(!ready, 0)) {
    asm volatile("s_waitcnt vmcnt(0)" ::: "memory");
    WGSYNC();
    {
      int t2 = (int)__builtin_amdgcn_mbcnt_hi(~0u, __builtin_amdgcn_mbcnt_lo(~0u, 0u));
      asm volatile("" : "+v"(t2));
      const int l2 = t2;
      t2 += wave * 64;
      if (t2 < 32) cnt[t2] = 0;
      WGSYNC();
      auto put = [&](unsigned t, unsigned e) NOPK -> bool {
        const unsigned gg = (t >> 4) & 7u;
        const int ad = LDS_ADD(&cnt[4 * gg], 4) >> 2;
        if (ad >= LCAP) return false;
        LAS unsigned* p = LP(lb + M_LX) + gg * LPAD + ad;
        p[0] = t;
        p[LED] = e;
        return true;
      };
      LAS unsigned* stp = (LAS unsigned*)(smem + M_ST) + t2;
      unsigned pe = *stp & 0xFFFFu, ocur = *stp >> 16;
#pragma unroll 1
      for (int j = 0; j < 16; ++j) {
        if ((pe >> j) & 1u) {
          const int cc = wave * 32 + (j >> 2) * 8 + (l2 >> 3);
          const unsigned* cp = TC + ((size_t)b * NCH + cc) * CELL;
          const int d = (l2 & 7) * 4 + (j & 3);
          const unsigned w = cp[d];
          const unsigned nib = (cp[(d & 16) + ((d >> 3) & 1)] >> (4 * (d & 7))) & 15u;
          if ((unsigned)((d & 15) - 2) >= (cp[d & 16] & 0xFFu) ||
              put(w >> 9, (w & 0x1FFFu) | (nib << 21) | ((unsigned)cc << 13))) pe &= ~(1u << j);
        }
      }
      const unsigned on = OVH[t2 >> 1];
      const size_t ob = (size_t)(t2 >> 1) * OVCAP;
#pragma unroll 1
      for (; ocur < on; ocur += 2) {
        const u32x4 v = OV[ob + ocur];
        if ((int)v[2] == b && !put(v[0], v[1])) break;
      }
      if (pe != 0u || ocur < on) cnt[1] = 1;
      *stp = pe | (ocur << 16);
    }
    WGSYNC();
    round_begin(lb + M_LX + grp * (LPAD * 4), lb + M_LX + grp * (LPAD * 4) + min(__builtin_amdgcn_readfirstlane(cnt[4 * grp]), LCAP * 4), cnt[1]);
    gather(0, true);
   }
    gather(1, false);
    ready = false;
    sl = lsp[0] & 31u;
    St = osl[sl * 2];
    ix = IX(sl);
    bv[0] = wvl[0 * 64];
    bv[1] = wvl[2 * 64];
    G0 = SMFMA(St, XAf[0], zero16, ix);
    pd[1] = u32x4{0u, 0u, 0u, 0u};
    pn[1] = u32x4{0u, 0u, 0u, 0u};
    S[1] = BC(pd[1]);
    asm volatile("" : "+v"(G0), "+v"(pd[1]), "+v"(pn[1]), "+v"(S[1]));
    asm volatile("s_waitcnt vmcnt(0)" ::: "memory");
#pragma unroll
    for (int k = 0; k < 4; ++k) as[k] = *(const LAS bf16x8*)(ring + xa0 + 16 * k);
    ae = *(const LAS bf16x8*)(ring + ea0);
    SB();
    asm volatile("" : "+v"(as[0]), "+v"(as[1]), "+v"(as[2]), "+v"(as[3]), "+v"(ae) :: "memory");
    G0 = MFMA32(ae, wC[0], G0);
    GLDS(ga[0], rx, ring_a);
    SB();
    G0 = MFMA32(as[0], wB[0][0], G0);
    GLDS(ga[1], rx, ring_a + XB);
    SB();
    G0 = MFMA32(as[1], wB[1][0], G0);
    GLDS(ga[2], rx, ring_a + 2 * XB);
    SB();
    G0 = MFMA32(as[2], wB[2][0], G0);
    GLDS(ga[3], rx, ring_a + 3 * XB);
    SB();
    G0 = MFMA32(as[3], wB[3][0], G0);
    GLDS(ga[4], re, ring_a + 4 * XB);
    SB();
    for (int i = 0; 4 * i + 2 < nq; ++i) {
      {
        f32x16& Gc = G0;
        LAS unsigned short* ohw = (LAS unsigned short*)(sm + sl * 80 + ohoff);
        Y[1] = MFMA32H(HC(pd[1]), HC(S[1]), Y[1]);
        bv[2] = wvl[4 * 64];
        *(volatile LAS int*)pown = nq - 4 * i;
        asm volatile("s_cmp_gt_i32 %0, 11\n\ts_cbranch_scc1 1f\n\ts_sleep 10\n1:" :: "s"(4 * i + 2) : "scc");
        E1(0); E1(1); E1(2); E1(3);
        SB();
        Y[3] = MFMA32H(HC(pn[1]), HC(S[1]), Y[3]);
        bv[3] = wvl[6 * 64];
        E1(4); CV(0); E1(5); CV(1); PA(0);
        SB();
        V = MFMA32(as[0], bv[0], zero16);
        const int prem = *(volatile const LAS int*)poth;
        E1(6); PA(1); RL(0); E1(7); CV(2);
        SB();
        V = MFMA32(as[1], bv[1], V);
        RH(0); E1(8); PA(2); E1(9); CV(3);
        SB();
        V = MFMA32(as[2], bv[2], V);
        RL(1); E1(10); PA(3); E1(11); CV(4);
        SB();
        V = MFMA32(as[3], bv[3], V);
        RH(1); E1(12); PA(4); E1(13); CV(5);
        SB();
        G1 = MFMA32(ae, wC[1], zero16);
        asm volatile("s_cmp_ge_i32 %0, %1\n\ts_setprio 0\n\ts_cbranch_scc0 1f\n\ts_setprio 1\n1:"
                     :: "s"(nq - 4 * i), "s"(__builtin_amdgcn_readfirstlane(prem)) : "scc");
        RL(2); E1(14); PA(5); E1(15); CV(6);
        SB();
        G1 = SMFMA(St, XAf[1], G1, ix);
        *ohw = (unsigned short)0x4000;
        asm volatile("" ::: "memory");
        S[0] = BC(mine[0]);
        S[1] = BC(mine[1]);
        asm volatile("" ::: "memory");
        *ohw = (unsigned short)0;
        RH(2); PA(6); RL(3); CV(7);
        SB();
        G1 = MFMA32(as[0], wB[0][1], G1);
        RH(3); PA(7); MX(0); RL(4); MX(1); MX(2);
        SB();
        G1 = MFMA32(as[1], wB[1][1], G1);
        slr = lsp[(i + 1) * 32];
        RH(4); MX(3); RL(5); MX(4);
        SB();
        G1 = MFMA32(as[2], wB[2][1], G1);
        bv[0] = wvl[1 * 64];
        RH(5); MX(5); RL(6); MX(6);
        SB();
        G1 = MFMA32(as[3], wB[3][1], G1);
        bv[1] = wvl[3 * 64];
        RH(6); MX(7); RL(7); MX(8);
        SB();
        Y[0] = MFMA32H(HC(pd[0]), HC(S[0]), Y[0]);
        bv[2] = wvl[5 * 64];
        RH(7); MX(9); MX(10); MX(11);
        SB();
        Y[2] = MFMA32H(HC(pn[0]), HC(S[0]), Y[2]);
        bv[3] = wvl[7 * 64];
        MX(12); MX(13); MX(14); MX(15);
        SB();
      }
      {
        f32x16& Gc = G1;
        sl = slr & 31u;
        V = MFMA32(as[0], bv[0], zero16);
        const u32x4 xo = *(const LAS u32x4*)(lxp + (i + 2) * 32);
        const unsigned eo = lep[(i + 2) * 32];
        E1(0); E1(1); E1(2); E1(3);
        SB();
        V = MFMA32(as[1], bv[1], V);
        St = osl[sl * 2];
        ix = IX(sl);
        E1(4); CV(0); E1(5); CV(1); PA(0);
        SB();
        V = MFMA32(as[2], bv[2], V);
        E1(6); PA(1); RL(0); E1(7); CV(2);
        SB();
        V = MFMA32(as[3], bv[3], V);
        RH(0); E1(8); PA(2); E1(9); CV(3);
        SB();
        Y[0] = MFMA32H(HC(pd[1]), HC(S[1]), Y[0]);
        SB();
        asm volatile("s_waitcnt vmcnt(0)" ::: "memory");
        ae = *(const LAS bf16x8*)(ring + ea0);
        as[0] = *(const LAS bf16x8*)(ring + xa0);
        as[1] = *(const LAS bf16x8*)(ring + xa0 + 16);
        as[2] = *(const LAS bf16x8*)(ring + xa0 + 32);
        as[3] = *(const LAS bf16x8*)(ring + xa0 + 48);
        RL(1); E1(10); PA(3); E1(11); CV(4);
        SB();
        Y[2] = MFMA32H(HC(pn[1]), HC(S[1]), Y[2]);
        ga[0] = XG(0);
        PIN(ga[0]);
        RH(1); E1(12); PA(4); E1(13); CV(5);
        SB();
        asm volatile("" : "+v"(as[0]), "+v"(as[1]), "+v"(as[2]), "+v"(as[3]), "+v"(ae) :: "memory");
        G0 = MFMA32(ae, wC[0], zero16);
        GLDS(ga[0], rx, ring_a);
        ga[1] = XG(1);
        PIN(ga[1]);
        RL(2); E1(14); PA(5); E1(15); CV(6);
        SB();
        G0 = SMFMA(St, XAf[0], G0, ix);
        GLDS(ga[1], rx, ring_a + XB);
        ga[2] = XG(2);
        PIN(ga[2]);
        RH(2); PA(6); RL(3); CV(7);
        SB();
        G0 = MFMA32(as[0], wB[0][0], G0);
        GLDS(ga[2], rx, ring_a + 2 * XB);
        ga[3] = XG(3);
        PIN(ga[3]);
        RH(3); PA(7); MX(0); RL(4); MX(1); MX(2);
        SB();
        G0 = MFMA32(as[1], wB[1][0], G0);
        GLDS(ga[3], rx, ring_a + 3 * XB);
        ga[4] = (eo & ~31u) | ec0;
        PIN(ga[4]);
        RH(4); MX(3); RL(5); MX(4);
        SB();
        G0 = MFMA32(as[2], wB[2][0], G0);
        GLDS(ga[4], re, ring_a + 4 * XB);
        RH(5); MX(5); RL(6); MX(6);
        SB();
        G0 = MFMA32(as[3], wB[3][0], G0);
        RH(6); MX(7); RL(7); MX(8);
        SB();
        Y[1] = MFMA32H(HC(pd[0]), HC(S[0]), Y[1]);
        bv[0] = wvl[0 * 64];
        RH(7); MX(9); MX(10); MX(11);
        SB();
        Y[3] = MFMA32H(HC(pn[0]), HC(S[0]), Y[3]);
        bv[1] = wvl[2 * 64];
        MX(12); MX(13); MX(14); MX(15);
        SB();
      }
    }
    *(volatile LAS int*)pown = 0;
    Y[1] = MFMA32H(HC(pd[1]), HC(S[1]), Y[1]);
#define pm (pn + 1)
#define TE(G, i) G[i] = __builtin_amdgcn_exp2f(G[i])
#define TCV(P, G, j) TP(P, j) = pack_f16(G[2 * (j)], G[2 * (j) + 1])
#define TPA(P, j) TP(P, j) = __builtin_bit_cast(unsigned, __builtin_bit_cast(f16x2, (unsigned)TP(P, j)) + __builtin_bit_cast(f16x2, one15))
#define TRL(P, j) do { unsigned t_ = TP(P, j); asm volatile("v_rcp_f16_sdwa %0, %0 dst_sel:WORD_0 dst_unused:UNUSED_PRESERVE src0_sel:WORD_0" : "+v"(t_)); TP(P, j) = t_; } while (0)
#define TRH(P, j) do { unsigned t_ = TP(P, j); asm volatile("v_rcp_f16_sdwa %0, %0 dst_sel:WORD_1 dst_unused:UNUSED_PRESERVE src0_sel:WORD_1" : "+v"(t_)); TP(P, j) = t_; } while (0)
    {
      LAS unsigned short* ohw = (LAS unsigned short*)(sm + sl * 80 + ohoff);
      const int nl = 1 << (nq - 4 * max(ntiles - 1, 0));
      {
      int hc = nl & 6;
      asm volatile("" : "+s"(hc));
#pragma unroll 1
      for (; hc > 0; hc -= 64) {
        u32x4 qd[2], qn[2];
        bv[2] = wvl[4 * 64];
        TE(G0,0); TE(G0,1); TE(G0,2);
        SB();
        Y[3] = MFMA32H(HC(pn[1]), HC(S[1]), Y[3]);
        bv[3] = wvl[6 * 64];
        TCV(pd,G0,0); TE(G0,3); TPA(pd,0); TCV(pd,G0,1); TE(G0,4);
        SB();
        G1 = MFMA32(ae, wC[1], zero16);
        *ohw = (unsigned short)0x4000; asm volatile("" ::: "memory"); S[0] = BC(mine[0]); asm volatile("" ::: "memory"); *ohw = (unsigned short)0;
        TE(G0,5); TRL(pd,0); TPA(pd,1); TCV(pd,G0,2);
        SB();
        G1 = SMFMA(St, XAf[1], G1, ix);
        TRH(pd,0); TE(G0,6); TE(G0,7);
        SB();
        G1 = MFMA32(as[0], wB[0][1], G1);
        TRL(pd,1); TPA(pd,2); TCV(pd,G0,3); TRH(pd,1);
        SB();
        G1 = MFMA32(as[1], wB[1][1], G1);
        TRL(pd,2); TPA(pd,3); TRH(pd,2);
        SB();
        G1 = MFMA32(as[2], wB[2][1], G1);
        TRL(pd,3); TRH(pd,3);
        SB();
        G1 = MFMA32(as[3], wB[3][1], G1);
        SB();
        V = MFMA32(as[0], bv[0], zero16);
        bv[0] = wvl[1 * 64];
        SB();
        V = MFMA32(as[1], bv[1], V);
        bv[1] = wvl[3 * 64];
        TE(G1,0); TE(G1,1); TE(G1,2);
        SB();
        V = MFMA32(as[2], bv[2], V);
        bv[2] = wvl[5 * 64];
        TCV(qd,G1,0); TE(G1,3); TPA(qd,0); TCV(qd,G1,1); TE(G1,4);
        SB();
        V = MFMA32(as[3], bv[3], V);
        bv[3] = wvl[7 * 64];
        TE(G1,5); TRL(qd,0); TPA(qd,1); TCV(qd,G1,2);
        SB();
        G0 = MFMA32(as[0], bv[0], zero16);
        TRH(qd,0); TE(G1,6); TE(G1,7);
        SB();
        G0 = MFMA32(as[1], bv[1], G0);
        TRL(qd,1); TPA(qd,2); TCV(qd,G1,3); TRH(qd,1);
        SB();
        G0 = MFMA32(as[2], bv[2], G0);
        TRL(qd,2); TPA(qd,3); TMX(qn,V,pd,0); TRH(qd,2);
        SB();
        G0 = MFMA32(as[3], bv[3], G0);
        TRL(qd,3); TMX(qn,V,pd,1); TMX(qn,V,pd,2); TRH(qd,3);
        SB();
        Y[0] = MFMA32H(HC(pd[0]), HC(S[0]), Y[0]);
        TMX(qn,V,pd,3); TMX(qn,V,pd,4); TMX(qn,V,pd,5); TMX(qn,V,pd,6);
        SB();
        Y[1] = MFMA32H(HC(qd[0]), HC(S[0]), Y[1]);
        TMX(qn,V,pd,7); TMX(pm,G0,qd,0); TMX(pm,G0,qd,1); TMX(pm,G0,qd,2);
        SB();
        Y[2] = MFMA32H(HC(qn[0]), HC(S[0]), Y[2]);
        TMX(pm,G0,qd,3); TMX(pm,G0,qd,4); TMX(pm,G0,qd,5); TMX(pm,G0,qd,6); TMX(pm,G0,qd,7);
        S[1] = S[0];
        SB();
        asm volatile("" : "=v"(St), "=v"(ix), "=v"(ae), "=v"(bv[0]), "=v"(bv[1]), "=v"(G0), "=v"(as[0]), "=v"(as[1]), "=v"(as[2]), "=v"(as[3]));
      }
      }
    }
    Y[3] = MFMA32H(HC(pn[1]), HC(S[1]), Y[3]);
    if (__builtin_expect(!more, 1)) break;
  }
  if (__builtin_expect(sc != 0, 0)) {
    Y[0] *= U2F((127 - sc) << 23);
    Y[1] *= U2F((127 - sc) << 23);
  }
  {
    bf16x8 fa[2][6];
#pragma unroll
    for (int k = 0; k < 4; ++k) as[k] = *(const LAS bf16x8*)(smem + M_XG + grp * (4 * XB) + xa0 + 16 * k);
    fa[0][0] = fa[1][0] = ohl[r * 4 + h * 2];
    fa[0][1] = fa[1][1] = ohl[r * 4 + h * 2 + 1];
#pragma unroll
    for (int k = 0; k < 8; ++k) fa[k & 1][2 + (k >> 1)] = wvl[(8 + k) * 64];
    LAS char* zw = (LAS char*)wbase + r * 144 + h * 64;
    u32x4 o;
    f32x16 X[2], U[2];
#define FR(ct, i) do { float t_ = __builtin_amdgcn_rcpf(Y[ct][i]); PIN(t_); Y[ct][i] = t_; } while (0)
#define FM(ct, m) do { if ((m) == 0) X[ct] = MFMA32(fa[ct][0], as[2 * (ct)], zero16);          \
                       else if ((m) == 1) X[ct] = MFMA32(fa[ct][1], as[2 * (ct) + 1], X[ct]);   \
                       else U[ct] = MFMA32(fa[ct][m], as[(m) - 2], (m) == 2 ? X[ct] : U[ct]); } while (0)
#define FZ(ct, i) __builtin_amdgcn_fmed3f(__builtin_fmaf(Y[2 + (ct)][i], Y[ct][i], U[ct][i]), X[ct][i], 3.4028234e38f)
#define FO(q, j) do { o[j] = pack_bf16(FZ((q) >> 1, ((q) & 1) * 8 + 2 * (j)), FZ((q) >> 1, ((q) & 1) * 8 + 2 * (j) + 1)); \
      if ((j) == 3) *(LAS u32x4*)(zw + (q) * 16) = o; } while (0)
#pragma unroll
    for (int i = 0; i < 8; ++i) FR(0, i);
    SB();
#pragma unroll
    for (int m = 0; m < 8; ++m) {
      FM(m / 6, m % 6);
      SB();
#pragma unroll
      for (int i = 8 + 3 * m; i < 11 + 3 * m; ++i) FR(i >> 4, i & 15);
      SB();
    }
    asm volatile("s_waitcnt vmcnt(0)" ::: "memory");
    FM(1, 2); SB(); FO(0, 0); SB();
    FM(1, 3); SB(); FO(0, 1); SB();
    FM(1, 4); SB(); FO(0, 2); SB();
    FM(1, 5); SB(); FO(0, 3); FO(1, 0); FO(1, 1); FO(1, 2); FO(1, 3); SB();
    FO(2, 0); FO(2, 1); FO(2, 2); FO(2, 3); FO(3, 0); FO(3, 1); FO(3, 2); FO(3, 3);
  }
  const LAS char* zl = (const LAS char*)wbase;
  u32x4 o[4];
#pragma unroll
  for (int q = 0; q < 4; ++q) o[q] = *(const LAS u32x4*)(zl + (q * 8 + (lane >> 3)) * 144 + (lane & 7) * 16);
#pragma unroll
  for (int q = 0; q < 4; ++q) {
    const int row = q * 8 + (lane >> 3);
    __builtin_amdgcn_raw_buffer_store_b128(o[q], SRD(out, N * 128),
                                           row < nodes ? (n0 + 8 * row) * 128 + (lane & 7) * 16 : 0x7FFFFFF0, 0, 17);
  }
  {
    SB();
    unsigned m = 0;
#pragma unroll
    for (int i = 0; i < 16; ++i) m |= __builtin_bit_cast(unsigned, (float)Y[2][i]) | __builtin_bit_cast(unsigned, (float)Y[3][i]);
    if (r == 30 && (m & 0x7F800000u) == 0x7F800000u) cnt[5] = 1;
    asm volatile("s_waitcnt lgkmcnt(0)\n\ts_barrier" ::: "memory");
    return __builtin_amdgcn_readfirstlane(cnt[5]) ? wave : -1;
  }
  };
  int again = body(threadIdx.x, 0);
#pragma unroll 1
  for (int sc = 10; __builtin_expect(again >= 0 && sc <= 110, 0); sc += 10) {
    asm volatile("s_waitcnt vmcnt(0)\n\ts_barrier" ::: "memory");
    int t = again * 64 + (int)__builtin_amdgcn_mbcnt_hi(~0u, __builtin_amdgcn_mbcnt_lo(~0u, 0u));
    asm volatile("" : "+v"(t));
    again = body(t, sc);
  }
}

extern "C" void kernel_launch(void* const* d_in, const int* in_sizes, int n_in,
                              void* d_out, int out_size, void* d_ws, size_t ws_size,
                              hipStream_t stream) {
  const unsigned short* x = (const unsigned short*)d_in[0];
  const unsigned short* eattr = (const unsigned short*)d_in[1];
  const int* src = (const int*)d_in[2];
  const int* dst = (const int*)d_in[3];
  const unsigned short* wa = (const unsigned short*)d_in[4];
  const unsigned short* wb = (const unsigned short*)d_in[5];
  const unsigned short* wc = (const unsigned short*)d_in[6];
  const unsigned short* wu = (const unsigned short*)d_in[7];
  const unsigned short* wv = (const unsigned short*)d_in[8];
  unsigned short* out = (unsigned short*)d_out;
  const int N = in_sizes[0] / 64;
  const int E = in_sizes[2];
  const int nb = (N + NPB - 1) / NPB;

  char* ws = (char*)d_ws;
  k_part<<<NCH, 1024, 0, stream>>>(src, dst, E, nb, (unsigned*)(ws + W_TC), (unsigned*)(ws + W_OVH), (u32x4*)(ws + W_OV),
                                   wa, wb, wc, wu, wv, (unsigned short*)(ws + W_WP));
  k_main<<<nb, 512, 0, stream>>>(x, eattr, ws, E, N, out);
}
```
